```python
import torch

def _softmax(x, axis=-1):
    return torch.softmax(x, dim=axis)

def _gelu(x):
    return torch.nn.functional.gelu(x, approximate='tanh')

def _silu(x):
    return torch.nn.functional.silu(x)

def _sigmoid(x):
    return torch.sigmoid(x)

def _layernorm(x, axis=-1, eps=1e-05):
    m = x.mean(dim=axis, keepdim=True)
    v = x.var(dim=axis, keepdim=True, unbiased=False)
    return (x - m) / torch.sqrt(v + eps)

def _scatter_add(tgt, idx, src, axis=0):
    return tgt.clone().index_add_(axis, idx, src)

def _segment_sum(src, seg, n):
    out = torch.zeros((n,) + src.shape[1:], dtype=src.dtype)
    return out.index_add_(0, seg, src)

def _pad(x, pw):
    flat = tuple((v for pair in reversed(pw) for v in pair))
    return torch.nn.functional.pad(x, flat)
kernel_name = 'gated_mix__compressed_kv_cla__mixer_gmlp__prompt'

def setup_inputs():

    _w = lambda a: (lambda t: t.to(torch.bfloat16) if t.is_floating_point() else t)(torch.as_tensor(a))

    def _A_setup():
        B, H, Sq, Sk, D, M = (1, 16, 2048, 2048, 256, 448)
        Dc = 4 * D
        return {'x': _w((lambda *s: torch.randn(*s, dtype=torch.bfloat16) * 0.05)(B, Sq, M)), 'c_kv': _w((lambda *s: torch.randn(*s, dtype=torch.bfloat16) * 0.8)(B, Sk, Dc)), 'w_q': _w((lambda *s: torch.randn(*s, dtype=torch.bfloat16) * 6.4)(M, H, D) * M ** (-0.5)), 'w_uk': _w((lambda *s: torch.randn(*s, dtype=torch.bfloat16) * 6.4)(Dc, H, D) * Dc ** (-0.5)), 'w_uv': _w((lambda *s: torch.randn(*s, dtype=torch.bfloat16) * 0.8)(Dc, H, D) * Dc ** (-0.5)), 'w_o': _w((lambda *s: torch.randn(*s, dtype=torch.bfloat16) * 0.8)(H, D, M) * (H * D) ** (-0.5))}
    _A_in = _A_setup()

    def _B_setup():
        B, S, D, F = (1, 2048, 448, 1792)
        Fh = F // 2
        return {'x': _w((lambda *s: torch.randn(*s, dtype=torch.bfloat16) * 0.05)(B, S, D)), 'w_in': _w((lambda *s: torch.randn(*s, dtype=torch.bfloat16) * 0.4)(D, 2 * Fh)), 'w_sp': _w(0.01 * (lambda *s: torch.randn(*s, dtype=torch.bfloat16) * 0.4)(S, S)), 'b_sp': _w(1.0 + 0.01 * (lambda *s: torch.randn(*s, dtype=torch.bfloat16) * 0.05)(S)), 'w_out': _w((lambda *s: torch.randn(*s, dtype=torch.bfloat16) * 0.05)(Fh, D))}
    _B_in = _B_setup()
    return {'A_x': _A_in['x'], 'A_c_kv': _A_in['c_kv'], 'A_w_q': _A_in['w_q'], 'A_w_uk': _A_in['w_uk'], 'A_w_uv': _A_in['w_uv'], 'A_w_o': _A_in['w_o'], 'B_x': _B_in['x'], 'B_w_in': _B_in['w_in'], 'B_w_sp': _B_in['w_sp'], 'B_b_sp': _B_in['b_sp'], 'B_w_out': _B_in['w_out'], 'gate_w': _w((lambda *s: torch.randn(*s, dtype=torch.bfloat16) * 0.05)(448) * 16.0)}

def reference(A_x, A_c_kv, A_w_q, A_w_uk, A_w_uv, A_w_o, B_x, B_w_in, B_w_sp, B_b_sp, B_w_out, gate_w):

    def _A_ref(x, c_kv, w_q, w_uk, w_uv, w_o):
        B, Sq, M = (x.shape[0], x.shape[1], x.shape[2])
        Sk = c_kv.shape[1]
        H, D = (w_q.shape[1], w_q.shape[2])
        q = torch.einsum('bqm,mhd->bhqd', x, w_q)
        k = torch.einsum('bkc,chd->bhkd', c_kv, w_uk)
        v = torch.einsum('bkc,chd->bhkd', c_kv, w_uv)
        scores = torch.einsum('bhqd,bhkd->bhqk', q, k) * D ** (-0.5)
        off = Sk - Sq
        qi = torch.arange(Sq)[:, None]
        kj = torch.arange(Sk)[None, :]
        cmask = kj <= qi + off
        scores = torch.where(cmask[None, None], scores, -1000000000.0)
        p = _softmax(scores, axis=-1)
        ctx = torch.einsum('bhqk,bhkd->bhqd', p, v)
        return torch.einsum('bhqd,hdm->bqm', ctx, w_o)

    def _B_ref(x, w_in, w_sp, b_sp, w_out):
        B, S, D = x.shape
        F = w_in.shape[-1]
        Fh = F // 2
        z = _gelu(torch.einsum('bsd,df->bsf', x, w_in))
        z1 = z[..., :Fh].clone()
        z2 = _layernorm(z[..., Fh:], axis=-1)
        g = torch.einsum('ts,bsf->btf', w_sp, z2) + b_sp[None, :, None]
        return torch.einsum('bsf,fd->bsd', z1 * g, w_out)
    x = A_x
    g = _sigmoid((x * gate_w).sum(-1, keepdims=True))
    a = _A_ref(x, A_c_kv, A_w_q, A_w_uk, A_w_uv, A_w_o)
    b = _B_ref(x, B_w_in, B_w_sp, B_b_sp, B_w_out)
    return g * a + (1 - g) * b
```

```cpp
#include <hip/hip_runtime.h>
#include <hip/hip_bf16.h>
#include <stdint.h>

typedef unsigned u32x4 __attribute__((ext_vector_type(4)));
typedef unsigned u32x2 __attribute__((ext_vector_type(2)));
typedef float f32x2 __attribute__((ext_vector_type(2)));
typedef __bf16 bf16x2 __attribute__((ext_vector_type(2)));
#define DI __device__ __forceinline__
DI float lo16(unsigned u) { return __uint_as_float(u << 16); }
DI float hi16(unsigned u) { return __uint_as_float(u & 0xffff0000u); }
DI unsigned pk2(float a, float b) { f32x2 v = {a, b}; bf16x2 r = __builtin_convertvector(v, bf16x2); return __builtin_bit_cast(unsigned, r); }
DI float gelu_tanh(float x) {
  const float a = x * (-2.3022081983f + -0.1029432396f * x * x);
  return x * __builtin_amdgcn_rcpf(1.0f + __builtin_amdgcn_exp2f(a));
}

constexpr size_t WS_GATE = 83886080ull;
constexpr size_t WS_Z2P = 71303168ull;
constexpr size_t WS_Z2N = 75497472ull;
constexpr size_t WS_CNT = 125829120ull;
constexpr size_t WS_WT = 104857600ull;
#define DBG 0
constexpr size_t WS_DBG = 127926272ull;
#define WARM 1
struct K1Args { const void* x; const void* ckv; const void* wq; const void* wuk; const void* wuv; const void* win; const void* gw; void* ws; const void* wo; const void* wout; const void* wsp; };
struct K2Args { void* ws; const void* wsp; const void* bsp; };
struct K3Args { void* ws; void* out; };

DI void touch(const void* base, unsigned off, unsigned n, unsigned step) {
  unsigned d, c;
  asm volatile("s_mov_b32 %1, %4\n"
               "1:\n"
               "global_load_dword %0, %2, %3\n"
               "v_add_u32 %2, %5, %2\n"
               "s_sub_u32 %1, %1, 1\n"
               "s_cmp_lg_u32 %1, 0\n"
               "s_cbranch_scc1 1b\n"
               "s_waitcnt vmcnt(0)"
               : "=&v"(d), "=&s"(c), "+v"(off) : "s"(base), "s"(n), "s"(step) : "memory", "scc");
}
DI void k1_side(const K1Args& p, unsigned char* lds, unsigned j, unsigned tid) {
  u32x4 wrow[3][7];
#pragma unroll
  for (int it = 0; it < 3; ++it) {
    const unsigned ks = j + 64u * it, kc = ks < 156u ? ks : j;
    const char* src = kc < 128u ? (const char*)p.wo + (size_t)kc * (32u * 896u) : (const char*)p.wout + (size_t)(kc - 128u) * (32u * 896u);
#pragma unroll
    for (int i = 0; i < 7; ++i) wrow[it][i] = ((const u32x4*)src)[tid + 256u * i];
  }
  const unsigned row = 32u * j + (tid >> 3), c = tid & 7u;
  const u32x4* xr = (const u32x4*)((const char*)p.x + (size_t)row * 896u);
  const u32x4* gw = (const u32x4*)p.gw;
  u32x4 xa[7], ga[7];
#pragma unroll
  for (int i = 0; i < 7; ++i) { xa[i] = xr[c + 8 * i]; ga[i] = gw[c + 8 * i]; }
#pragma unroll
  for (int it = 0; it < 3; ++it)
#pragma unroll
    for (int i = 0; i < 7; ++i) *(u32x4*)(lds + it * 28672u + (tid + 256u * i) * 16u) = wrow[it][i];
  {
    float s = 0.f;
#pragma unroll
    for (int i = 0; i < 7; ++i)
#pragma unroll
      for (int e = 0; e < 4; ++e) s += lo16(xa[i][e]) * lo16(ga[i][e]) + hi16(xa[i][e]) * hi16(ga[i][e]);
    s += __shfl_xor(s, 1); s += __shfl_xor(s, 2); s += __shfl_xor(s, 4);
    if (c == 0) ((float*)((char*)p.ws + WS_GATE))[row] = 1.0f / (1.0f + __expf(-s));
  }
  __syncthreads();
  for (unsigned it = 0; it < 3u; ++it) {
    const unsigned ks = j + 64u * it;
    if (ks >= 156u) break;
    const unsigned short* t = (const unsigned short*)(lds + it * 28672u);
#pragma unroll
    for (int i = 0; i < 7; ++i) {
      const unsigned c2 = tid + 256u * i, b = c2 >> 6, l = c2 & 63u, r = l & 15u, g = l >> 4;
      const unsigned nb = b / 7u, lb = b - 7u * nb;
      const unsigned m = 112u * nb + (lb < 6u ? 32u * (lb >> 1) + 8u * (r >> 2) + 4u * (lb & 1u) + (r & 3u) : 96u + r);
      u32x4 w;
#pragma unroll
      for (int e = 0; e < 4; ++e) {
        const unsigned k0 = 16u * (e >> 1) + 4u * g + 2u * (e & 1);
        w[e] = (unsigned)t[k0 * 448u + m] | ((unsigned)t[(k0 + 1u) * 448u + m] << 16);
      }
      *(u32x4*)((char*)p.ws + WS_WT + (size_t)(ks * 28u + b) * 1024u + l * 16u) = w;
    }
  }
}
DI void k1_warm(const K1Args& p, unsigned j, unsigned tid) {
  for (unsigned ks = j; ks < 156u; ks += 64u) {
    const char* src = ks < 128u ? (const char*)p.wo + (size_t)ks * (32u * 896u) : (const char*)p.wout + (size_t)(ks - 128u) * (32u * 896u);
    touch(src, tid < 224u ? tid * 64u : 0u, 2u, 14336u);
  }
}

template <int CTRL> DI float dpp_add(float x) { return x + __int_as_float(__builtin_amdgcn_update_dpp(0, __float_as_int(x), CTRL, 0xf, 0xf, true)); }
DI float row16_sum(float x) { x = dpp_add<0x128>(x); x = dpp_add<0x124>(x); x = dpp_add<0x122>(x); return dpp_add<0x121>(x); }
DI void k2_side(const K2Args& p, unsigned char* lds, unsigned sig) {
  const unsigned tid = threadIdx.x, w = __builtin_amdgcn_readfirstlane(tid >> 6), l = tid & 63u, g = l >> 4, f = l & 15u;
  const char* src = (const char*)p.ws + WS_Z2P + (size_t)(sig * 56u + 14u * w) * 1024u + l * 16u;
  u32x4 z[14];
#pragma unroll
  for (int i = 0; i < 14; ++i) z[i] = *(const u32x4*)(src + i * 1024);
  float y[14][8], s1[8], s2[8];
#pragma unroll
  for (int e = 0; e < 8; ++e) { s1[e] = 0.f; s2[e] = 0.f; }
#pragma unroll
  for (int i = 0; i < 14; ++i)
#pragma unroll
    for (int e = 0; e < 4; ++e) {
      const float a = gelu_tanh(lo16(z[i][e])), b = gelu_tanh(hi16(z[i][e]));
      y[i][2 * e] = a; y[i][2 * e + 1] = b;
      s1[2 * e] += a; s1[2 * e + 1] += b; s2[2 * e] = fmaf(a, a, s2[2 * e]); s2[2 * e + 1] = fmaf(b, b, s2[2 * e + 1]);
    }
#pragma unroll
  for (int e = 0; e < 8; ++e) { s1[e] = row16_sum(s1[e]); s2[e] = row16_sum(s2[e]); }
  float* red = (float*)lds;
  if (f == 0) {
#pragma unroll
    for (int e = 0; e < 8; ++e) { red[((w * 4u + g) * 8u + e) * 2u] = s1[e]; red[((w * 4u + g) * 8u + e) * 2u + 1u] = s2[e]; }
  }
  __syncthreads();
  unsigned tdump, tcnt, toff = (sig >> 3) * 1048576u + (sig & 7u) * 131072u + tid * 64u;
  asm volatile("s_mov_b32 %1, 8\n1:\nglobal_load_dword %0, %2, %3\nv_add_u32 %2, 0x4000, %2\ns_sub_u32 %1, %1, 1\ns_cmp_lg_u32 %1, 0\ns_cbranch_scc1 1b"
               : "=&v"(tdump), "=&s"(tcnt), "+v"(toff) : "s"(p.wsp) : "memory", "scc");
  float mu[8], rs[8];
#pragma unroll
  for (int e = 0; e < 8; ++e) {
    float a = 0.f, b = 0.f;
#pragma unroll
    for (int ww = 0; ww < 4; ++ww) { a += red[((ww * 4u + g) * 8u + e) * 2u]; b += red[((ww * 4u + g) * 8u + e) * 2u + 1u]; }
    const float m = a * (1.0f / 896.0f);
    const float v = fmaxf(b * (1.0f / 896.0f) - m * m, 0.f);
    rs[e] = __builtin_amdgcn_rsqf(v + 1e-5f); mu[e] = -m * rs[e];
  }
  const __amdgpu_buffer_rsrc_t rsrc = __builtin_amdgcn_make_buffer_rsrc((char*)p.ws + WS_Z2N, (short)0, 64 * 56 * 1024, 0x00020000);
  const unsigned voff = (((g >> 1) + 2u * (g & 1u)) * 16u + f) * 16u + (sig * 56u + 14u * w) * 1024u;
#pragma unroll
  for (int i = 0; i < 14; ++i) {
    const unsigned a0 = pk2(y[i][0] * rs[0] + mu[0], y[i][1] * rs[1] + mu[1]), a1 = pk2(y[i][2] * rs[2] + mu[2], y[i][3] * rs[3] + mu[3]);
    const unsigned b0 = pk2(y[i][4] * rs[4] + mu[4], y[i][5] * rs[5] + mu[5]), b1 = pk2(y[i][6] * rs[6] + mu[6], y[i][7] * rs[7] + mu[7]);
    const auto r0 = __builtin_amdgcn_permlane16_swap(a0, b0, false, false), r1 = __builtin_amdgcn_permlane16_swap(a1, b1, false, false);
    u32x4 o; o[0] = r0[0]; o[1] = r1[0]; o[2] = r0[1]; o[3] = r1[1];
    __builtin_amdgcn_raw_buffer_store_b128(o, rsrc, voff + i * 1024, 0, 16);
  }
  asm volatile("s_waitcnt vmcnt(0)" : "+v"(tdump) :: "memory");
  __syncthreads();
  if (tid == 0) {
    unsigned* cnt = (unsigned*)((char*)p.ws + WS_CNT + 128);
    __hip_atomic_fetch_add(cnt, 1u, __ATOMIC_RELAXED, __HIP_MEMORY_SCOPE_AGENT);
    for (unsigned spin = 0; spin < (1u << 20); ++spin) {
      if (__hip_atomic_load(cnt, __ATOMIC_RELAXED, __HIP_MEMORY_SCOPE_AGENT) >= 64u) break;
      __builtin_amdgcn_s_sleep(1);
    }
    __builtin_amdgcn_fence(__ATOMIC_ACQUIRE, "agent");
    asm volatile("s_waitcnt vmcnt(0)" ::: "memory");
  }
  __syncthreads();
}

__global__ __launch_bounds__(256, 1) void k1_proj(K1Args p) {
  __shared__ __attribute__((aligned(16))) unsigned char lds[163840];
  if ((blockIdx.x >> 3) >= 24u) k1_warm(p, (blockIdx.x & 7u) + 8u * ((blockIdx.x >> 3) - 24u), threadIdx.x);
  unsigned tid = threadIdx.x;
  const __attribute__((address_space(4))) void* ka = (const __attribute__((address_space(4))) void*)__builtin_amdgcn_kernarg_segment_ptr();
  unsigned bid = blockIdx.x;
  asm volatile(
R"ASM(.macro MZaaa d,a,b
v_mfma_f32_16x16x32_bf16 a[\d:\d+3], a[\a:\a+3], a[\b:\b+3], 0
.endm
.macro MAaaa d,a,b
v_mfma_f32_16x16x32_bf16 a[\d:\d+3], a[\a:\a+3], a[\b:\b+3], a[\d:\d+3]
.endm
.macro MZaav d,a,b
v_mfma_f32_16x16x32_bf16 a[\d:\d+3], a[\a:\a+3], v[\b:\b+3], 0
.endm
.macro MAaav d,a,b
v_mfma_f32_16x16x32_bf16 a[\d:\d+3], a[\a:\a+3], v[\b:\b+3], a[\d:\d+3]
.endm
.macro MZava d,a,b
v_mfma_f32_16x16x32_bf16 a[\d:\d+3], v[\a:\a+3], a[\b:\b+3], 0
.endm
.macro MAava d,a,b
v_mfma_f32_16x16x32_bf16 a[\d:\d+3], v[\a:\a+3], a[\b:\b+3], a[\d:\d+3]
.endm
.macro MZavv d,a,b
v_mfma_f32_16x16x32_bf16 a[\d:\d+3], v[\a:\a+3], v[\b:\b+3], 0
.endm
.macro MAavv d,a,b
v_mfma_f32_16x16x32_bf16 a[\d:\d+3], v[\a:\a+3], v[\b:\b+3], a[\d:\d+3]
.endm
.macro MZvaa d,a,b
v_mfma_f32_16x16x32_bf16 v[\d:\d+3], a[\a:\a+3], a[\b:\b+3], 0
.endm
.macro MAvaa d,a,b
v_mfma_f32_16x16x32_bf16 v[\d:\d+3], a[\a:\a+3], a[\b:\b+3], v[\d:\d+3]
.endm
.macro MZvav d,a,b
v_mfma_f32_16x16x32_bf16 v[\d:\d+3], a[\a:\a+3], v[\b:\b+3], 0
.endm
.macro MAvav d,a,b
v_mfma_f32_16x16x32_bf16 v[\d:\d+3], a[\a:\a+3], v[\b:\b+3], v[\d:\d+3]
.endm
.macro MZvva d,a,b
v_mfma_f32_16x16x32_bf16 v[\d:\d+3], v[\a:\a+3], a[\b:\b+3], 0
.endm
.macro MAvva d,a,b
v_mfma_f32_16x16x32_bf16 v[\d:\d+3], v[\a:\a+3], a[\b:\b+3], v[\d:\d+3]
.endm
.macro MZvvv d,a,b
v_mfma_f32_16x16x32_bf16 v[\d:\d+3], v[\a:\a+3], v[\b:\b+3], 0
.endm
.macro MAvvv d,a,b
v_mfma_f32_16x16x32_bf16 v[\d:\d+3], v[\a:\a+3], v[\b:\b+3], v[\d:\d+3]
.endm
.macro Ra d,a,o
ds_read_b128 a[\d:\d+3], v\a offset:\o
.endm
.macro Ta d,a,o
ds_read_b64_tr_b16 a[\d:\d+1], v\a offset:\o
.endm
.macro Rv d,a,o
ds_read_b128 v[\d:\d+3], v\a offset:\o
.endm
.macro Tv d,a,o
ds_read_b64_tr_b16 v[\d:\d+1], v\a offset:\o
.endm
.macro AR d,a
v_accvgpr_read_b32 v\d, a\a
.endm
.macro CV d,a,b
v_cvt_pk_bf16_f32 v\d, v\a, v\b
.endm
.macro EX d,a
v_exp_f32 v\d, v\a
.endm
v_mov_b32 v168, %0
s_mov_b32 s69, %2
s_load_dwordx16 s[8:23], %1, 0x0
v_lshrrev_b32 v160, 6, v168
s_nop 0
v_readfirstlane_b32 s68, v160
s_and_b32 s82, s69, 7
s_lshr_b32 s83, s69, 3
v_and_b32 v160, 63, v168
v_lshlrev_b32 v159, 4, v160
v_and_b32 v161, 15, v160
v_lshrrev_b32 v162, 1, v161
v_lshrrev_b32 v163, 4, v160
v_xor_b32 v162, v162, v163
v_lshlrev_b32 v162, 4, v162
v_lshlrev_b32 v161, 7, v161
v_add_u32 v161, v161, v162
s_lshr_b32 s64, s68, 1
s_lshl_b32 s64, s64, 14
v_add_u32 v140, s64, v161
v_lshrrev_b32 v161, 4, v160
v_bfe_u32 v162, v160, 2, 2
v_and_b32 v163, 3, v160
v_lshlrev_b32 v164, 3, v161
v_add_u32 v164, v164, v162
v_lshlrev_b32 v164, 9, v164
v_lshlrev_b32 v163, 3, v163
v_add_u32 v164, v164, v163
v_and_b32 v161, 1, v161
v_lshlrev_b32 v161, 2, v161
v_or_b32 v161, v161, v162
s_and_b32 s64, s68, 1
s_lshl_b32 s64, s64, 8
v_add_u32 v164, s64, v164
v_xor_b32 v165, 0, v161
v_lshlrev_b32 v165, 5, v165
v_add_u32 v132, v164, v165
v_xor_b32 v165, 1, v161
v_lshlrev_b32 v165, 5, v165
v_add_u32 v133, v164, v165
v_xor_b32 v165, 2, v161
v_lshlrev_b32 v165, 5, v165
v_add_u32 v134, v164, v165
v_xor_b32 v165, 3, v161
v_lshlrev_b32 v165, 5, v165
v_add_u32 v135, v164, v165
v_xor_b32 v165, 4, v161
v_lshlrev_b32 v165, 5, v165
v_add_u32 v136, v164, v165
v_xor_b32 v165, 5, v161
v_lshlrev_b32 v165, 5, v165
v_add_u32 v137, v164, v165
v_xor_b32 v165, 6, v161
v_lshlrev_b32 v165, 5, v165
v_add_u32 v138, v164, v165
v_xor_b32 v165, 7, v161
v_lshlrev_b32 v165, 5, v165
v_add_u32 v139, v164, v165
s_waitcnt lgkmcnt(0)
s_lshr_b32 s66, s68, 1
s_and_b32 s67, s68, 1
s_bfe_u32 s48, s83, 65537
s_lshl_b32 s64, s82, 1
s_add_u32 s48, s48, s64
s_lshr_b32 s50, s83, 2
s_and_b32 s75, s83, 1
s_lshl_b32 s64, s50, 19
s_add_u32 s36, s10, s64
s_addc_u32 s37, s11, 0
s_mov_b32 s38, 0
s_mov_b32 s39, 0x20000
s_sub_u32 s38, 4194304, s64
s_cmp_eq_u32 s75, 0
s_cselect_b32 s28, s14, s16
s_cselect_b32 s29, s15, s17
s_lshl_b32 s64, s48, 9
s_add_u32 s40, s28, s64
s_addc_u32 s41, s29, 0
s_mov_b32 s42, 0
s_mov_b32 s43, 0x20000
s_sub_u32 s42, 8388608, s64
s_mov_b32 s54, 65536
s_mov_b32 s55, 262144
s_mov_b32 s56, 2048
s_mov_b32 s57, 8388608
s_mul_i32 s58, s82, 256
s_mul_i32 s59, s82, 1048576
s_lshl_b32 s64, s48, 20
s_lshl_b32 s65, s50, 17
s_add_u32 s64, s64, s65
s_lshl_b32 s65, s66, 16
s_add_u32 s64, s64, s65
s_cmp_eq_u32 s75, 0
s_cselect_b32 s65, 12, 13
s_lshl_b32 s65, s67, s65
s_add_u32 s64, s64, s65
s_mul_i32 s65, s75, 16777216
s_add_u32 s64, s64, s65
s_add_u32 s72, s64, 16777216
s_lshl_b32 s73, 8192, s75
s_mov_b32 s80, 2048
s_mov_b32 s81, 8192
v_and_b32 v160, 63, v168
v_lshrrev_b32 v161, 3, v160
s_lshl_b32 s64, s68, 6
v_add_u32 v161, s64, v161
v_mul_lo_u32 v161, v161, s80
v_lshrrev_b32 v162, 4, v160
v_and_b32 v163, 7, v160
v_xor_b32 v163, v163, v162
v_xor_b32 v164, 4, v163
s_lshl_b32 s64, s80, 3
s_sub_u32 s64, s64, 1024
v_lshlrev_b32 v165, 4, v163
v_add_u32 v151, v165, v161
v_lshlrev_b32 v165, 4, v164
v_add_u32 v152, v165, v161
s_mul_i32 s65, s64, 1
v_add_u32 v152, s65, v152
v_lshlrev_b32 v165, 4, v163
v_add_u32 v153, v165, v161
s_mul_i32 s65, s64, 2
v_add_u32 v153, s65, v153
v_lshlrev_b32 v165, 4, v164
v_add_u32 v154, v165, v161
s_mul_i32 s65, s64, 3
v_add_u32 v154, s65, v154
v_lshrrev_b32 v161, 5, v160
s_lshl_b32 s64, s68, 3
v_add_u32 v162, s64, v161
v_mul_lo_u32 v162, v162, s81
v_and_b32 v163, 31, v160
v_lshrrev_b32 v163, 1, v163
s_and_b32 s64, s68, 1
s_lshl_b32 s64, s64, 2
v_xor_b32 v163, s64, v163
v_xor_b32 v163, v163, v161
v_and_b32 v164, 1, v160
v_lshlrev_b32 v164, 4, v164
v_add_u32 v162, v162, v164
s_lshl_b32 s64, s81, 1
s_sub_u32 s64, s64, 1024
v_xor_b32 v165, 0, v163
v_lshlrev_b32 v165, 5, v165
v_add_u32 v155, v165, v162
v_xor_b32 v165, 2, v163
v_lshlrev_b32 v165, 5, v165
v_add_u32 v156, v165, v162
s_mul_i32 s65, s64, 1
v_add_u32 v156, s65, v156
v_xor_b32 v165, 0, v163
v_lshlrev_b32 v165, 5, v165
v_add_u32 v157, v165, v162
s_mul_i32 s65, s64, 2
v_add_u32 v157, s65, v157
v_xor_b32 v165, 2, v163
v_lshlrev_b32 v165, 5, v165
v_add_u32 v158, v165, v162
s_mul_i32 s65, s64, 3
v_add_u32 v158, s65, v158
s_lshr_b32 s66, s68, 1
s_and_b32 s67, s68, 1
s_add_u32 s44, s22, 0
s_addc_u32 s45, s23, 0
s_mov_b32 s46, 130023424
s_mov_b32 s47, 0x20000
s_lshl_b32 s64, s69, 8
s_add_u32 s64, s64, 125829120
v_lshrrev_b32 v161, 2, v159
v_mov_b32 v162, 0
buffer_store_dword v162, v161, s[44:47], s64 offen
s_mov_b32 s78, 0x3db8aa3b
s_mov_b32 s79, 0x3db8aa3b
s_mov_b32 s74, 0
s_cmp_ge_u32 s83, 23
s_cbranch_scc1 Lk1_nounit1
s_cmp_ge_u32 s83, 16
s_cbranch_scc1 Lk1_z
s_lshr_b32 s50, s83, 1
s_and_b32 s48, s83, 1
s_lshl_b32 s64, s82, 1
s_add_u32 s48, s48, s64
s_mul_i32 s64, s50, 229376
s_add_u32 s24, s8, s64
s_addc_u32 s25, s9, 0
s_mov_b32 s26, 0
s_mov_b32 s27, 0x20000
s_sub_u32 s26, 1835008, s64
s_lshl_b32 s64, s48, 9
s_add_u32 s28, s12, s64
s_addc_u32 s29, s13, 0
s_mov_b32 s30, 0
s_mov_b32 s31, 0x20000
s_sub_u32 s30, 3670016, s64
s_mov_b32 s81, 8192
s_mov_b32 s74, 1
s_mov_b32 s71, 8192
s_lshl_b32 s64, s48, 20
s_lshl_b32 s65, s50, 17
s_add_u32 s64, s64, s65
s_lshl_b32 s65, s66, 16
s_add_u32 s64, s64, s65
s_lshl_b32 s65, s67, 12
s_add_u32 s70, s64, s65
s_branch Lk1_xunit
Lk1_z:
s_sub_u32 s48, s83, 16
s_mul_i32 s64, s82, 229376
s_add_u32 s24, s8, s64
s_addc_u32 s25, s9, 0
s_mov_b32 s26, 0
s_mov_b32 s27, 0x20000
s_sub_u32 s26, 1835008, s64
s_lshl_b32 s64, s48, 9
s_add_u32 s28, s18, s64
s_addc_u32 s29, s19, 0
s_mov_b32 s30, 0
s_mov_b32 s31, 0x20000
s_sub_u32 s30, 1605632, s64
s_mov_b32 s81, 3584
s_lshl_b32 s64, s48, 8
s_lshl_b32 s65, s67, 7
s_add_u32 s66, s64, s65
s_cmp_ge_u32 s66, 896
s_cbranch_scc1 Lk1_z2
s_lshr_b32 s65, s68, 1
s_lshl_b32 s65, s65, 3
s_lshl_b32 s64, s82, 4
s_add_u32 s64, s64, s65
s_mul_i32 s64, s64, 28
s_lshr_b32 s66, s66, 5
s_add_u32 s64, s64, s66
s_lshl_b32 s64, s64, 10
s_add_u32 s70, s64, 67108864
s_mov_b32 s71, 28672
s_mov_b32 s74, 2
s_branch Lk1_xunit
Lk1_z2:
s_lshr_b32 s65, s68, 1
s_lshl_b32 s65, s65, 2
s_lshl_b32 s64, s82, 3
s_add_u32 s64, s64, s65
s_mul_i32 s64, s64, 56
s_sub_u32 s66, s66, 896
s_lshr_b32 s66, s66, 4
s_add_u32 s64, s64, s66
s_lshl_b32 s64, s64, 10
s_add_u32 s70, s64, 71303168
s_mov_b32 s71, 57344
s_mov_b32 s74, 3
Lk1_xunit:
s_mov_b32 s80, 896
s_mov_b32 s49, 28672
s_lshl_b32 s51, s81, 5
s_mov_b32 s52, 896
s_mul_i32 s53, s81, 448
s_cmp_eq_u32 s82, 7
s_cselect_b32 s64, 0, s82
s_lshl_b32 s48, s64, 7
s_mul_i32 s50, s64, s51
s_lshl_b32 s50, s50, 1
v_and_b32 v160, 63, v168
v_lshrrev_b32 v161, 3, v160
s_lshl_b32 s64, s68, 6
v_add_u32 v161, s64, v161
v_mul_lo_u32 v161, v161, s80
v_lshrrev_b32 v162, 4, v160
v_and_b32 v163, 7, v160
v_xor_b32 v163, v163, v162
v_xor_b32 v164, 4, v163
s_lshl_b32 s64, s80, 3
s_sub_u32 s64, s64, 1024
v_lshlrev_b32 v165, 4, v163
v_add_u32 v143, v165, v161
v_lshlrev_b32 v165, 4, v164
v_add_u32 v144, v165, v161
s_mul_i32 s65, s64, 1
v_add_u32 v144, s65, v144
v_lshlrev_b32 v165, 4, v163
v_add_u32 v145, v165, v161
s_mul_i32 s65, s64, 2
v_add_u32 v145, s65, v145
v_lshlrev_b32 v165, 4, v164
v_add_u32 v146, v165, v161
s_mul_i32 s65, s64, 3
v_add_u32 v146, s65, v146
v_lshrrev_b32 v161, 5, v160
s_lshl_b32 s64, s68, 3
v_add_u32 v162, s64, v161
v_mul_lo_u32 v162, v162, s81
v_and_b32 v163, 31, v160
v_lshrrev_b32 v163, 1, v163
s_and_b32 s64, s68, 1
s_lshl_b32 s64, s64, 2
v_xor_b32 v163, s64, v163
v_xor_b32 v163, v163, v161
v_and_b32 v164, 1, v160
v_lshlrev_b32 v164, 4, v164
v_add_u32 v162, v162, v164
s_lshl_b32 s64, s81, 1
s_sub_u32 s64, s64, 1024
v_xor_b32 v165, 0, v163
v_lshlrev_b32 v165, 5, v165
v_add_u32 v147, v165, v162
v_xor_b32 v165, 2, v163
v_lshlrev_b32 v165, 5, v165
v_add_u32 v148, v165, v162
s_mul_i32 s65, s64, 1
v_add_u32 v148, s65, v148
v_xor_b32 v165, 0, v163
v_lshlrev_b32 v165, 5, v165
v_add_u32 v149, v165, v162
s_mul_i32 s65, s64, 2
v_add_u32 v149, s65, v149
v_xor_b32 v165, 2, v163
v_lshlrev_b32 v165, 5, v165
v_add_u32 v150, v165, v162
s_mul_i32 s65, s64, 3
v_add_u32 v150, s65, v150
s_mov_b32 s76, 1
s_branch Lk1_cursors
Lk1_nounit1:
s_mov_b64 s[24:25], s[36:37]
s_mov_b64 s[26:27], s[38:39]
s_mov_b64 s[28:29], s[40:41]
s_mov_b64 s[30:31], s[42:43]
s_mov_b32 s49, s54
s_mov_b32 s51, s55
s_mov_b32 s76, 0
s_mov_b32 s52, s56
s_mov_b32 s53, s57
s_mov_b32 s48, s58
s_mov_b32 s50, s59
v_mov_b32 v143, v151
v_mov_b32 v144, v152
v_mov_b32 v145, v153
v_mov_b32 v146, v154
v_mov_b32 v147, v155
v_mov_b32 v148, v156
v_mov_b32 v149, v157
v_mov_b32 v150, v158
s_mov_b32 s38, 0
s_mov_b32 s42, 0
Lk1_cursors:
s_lshl_b32 s64, s68, 13
s_add_u32 s61, s64, 65536
s_lshl_b32 s62, s68, 12
s_mov_b32 s63, 65536
s_load_dwordx2 s[8:9], %1, 0x50
s_load_dwordx2 s[16:17], %1, 0x40
s_load_dwordx2 s[20:21], %1, 0x48
s_mov_b32 m0, s61
s_nop 0
buffer_load_dwordx4 v143, s[24:27], s48 offen lds
buffer_load_dwordx4 v144, s[24:27], s48 offen offset:1024 lds
buffer_load_dwordx4 v145, s[24:27], s48 offen offset:2048 lds
buffer_load_dwordx4 v146, s[24:27], s48 offen offset:3072 lds
s_add_u32 s65, s48, s49
s_add_u32 m0, s61, 4096
s_nop 0
buffer_load_dwordx4 v143, s[24:27], s65 offen lds
buffer_load_dwordx4 v144, s[24:27], s65 offen offset:1024 lds
buffer_load_dwordx4 v145, s[24:27], s65 offen offset:2048 lds
buffer_load_dwordx4 v146, s[24:27], s65 offen offset:3072 lds
s_mov_b32 m0, s62
s_nop 0
buffer_load_dwordx4 v147, s[28:31], s50 offen lds
buffer_load_dwordx4 v148, s[28:31], s50 offen offset:1024 lds
buffer_load_dwordx4 v149, s[28:31], s50 offen offset:2048 lds
buffer_load_dwordx4 v150, s[28:31], s50 offen offset:3072 lds
s_add_u32 s48, s48, 128
s_cmp_eq_u32 s48, s52
s_cselect_b32 s48, 0, s48
s_add_u32 s61, s61, 32768
s_mov_b32 m0, s61
s_nop 0
buffer_load_dwordx4 v143, s[24:27], s48 offen lds
buffer_load_dwordx4 v144, s[24:27], s48 offen offset:1024 lds
buffer_load_dwordx4 v145, s[24:27], s48 offen offset:2048 lds
buffer_load_dwordx4 v146, s[24:27], s48 offen offset:3072 lds
s_add_u32 s50, s50, s51
s_cmp_eq_u32 s50, s53
s_cselect_b32 s50, 0, s50
s_add_u32 s62, s62, 16384
s_and_b32 s62, s62, 0xffff
s_mov_b32 m0, s62
s_nop 0
buffer_load_dwordx4 v147, s[28:31], s50 offen lds
buffer_load_dwordx4 v148, s[28:31], s50 offen offset:1024 lds
buffer_load_dwordx4 v149, s[28:31], s50 offen offset:2048 lds
buffer_load_dwordx4 v150, s[28:31], s50 offen offset:3072 lds
s_add_u32 s65, s48, s49
s_add_u32 m0, s61, 4096
s_nop 0
buffer_load_dwordx4 v143, s[24:27], s65 offen lds
buffer_load_dwordx4 v144, s[24:27], s65 offen offset:1024 lds
buffer_load_dwordx4 v145, s[24:27], s65 offen offset:2048 lds
buffer_load_dwordx4 v146, s[24:27], s65 offen offset:3072 lds
s_add_u32 s50, s50, s51
s_cmp_eq_u32 s50, s53
s_cselect_b32 s50, 0, s50
s_add_u32 s62, s62, 16384
s_and_b32 s62, s62, 0xffff
s_mov_b32 m0, s62
s_nop 0
buffer_load_dwordx4 v147, s[28:31], s50 offen lds
buffer_load_dwordx4 v148, s[28:31], s50 offen offset:1024 lds
buffer_load_dwordx4 v149, s[28:31], s50 offen offset:2048 lds
buffer_load_dwordx4 v150, s[28:31], s50 offen offset:3072 lds
s_waitcnt vmcnt(16)
s_barrier
s_add_u32 s48, s48, 128
s_cmp_eq_u32 s48, s52
s_cselect_b32 s48, 0, s48
s_add_u32 s61, s61, 32768
s_mov_b32 m0, s61
s_nop 0
buffer_load_dwordx4 v143, s[24:27], s48 offen lds
buffer_load_dwordx4 v144, s[24:27], s48 offen offset:1024 lds
buffer_load_dwordx4 v145, s[24:27], s48 offen offset:2048 lds
buffer_load_dwordx4 v146, s[24:27], s48 offen offset:3072 lds
s_add_u32 s50, s50, s51
s_cmp_eq_u32 s50, s53
s_cselect_b32 s50, 0, s50
s_add_u32 s62, s62, 16384
s_and_b32 s62, s62, 0xffff
s_mov_b32 m0, s62
s_nop 0
buffer_load_dwordx4 v147, s[28:31], s50 offen lds
buffer_load_dwordx4 v148, s[28:31], s50 offen offset:1024 lds
buffer_load_dwordx4 v149, s[28:31], s50 offen offset:2048 lds
buffer_load_dwordx4 v150, s[28:31], s50 offen offset:3072 lds
v_add_u32 v142, s63, v140
v_xor_b32 v141, 64, v142
Tv 68,132,0
Tv 70,132,2048
Tv 72,133,0
Tv 74,133,2048
Tv 76,134,0
Tv 78,134,2048
Tv 80,135,0
Tv 82,135,2048
Tv 84,136,0
Tv 86,136,2048
Tv 88,137,0
Tv 90,137,2048
Tv 92,138,0
Tv 94,138,2048
Tv 96,139,0
Tv 98,139,2048
Rv 4,142,0
Rv 8,142,2048
Rv 12,142,4096
Rv 16,142,6144
Rv 20,142,8192
Rv 24,142,10240
Rv 28,142,12288
Rv 32,142,14336
s_waitcnt lgkmcnt(0)
s_add_u32 s12, s8, 4194304
s_addc_u32 s13, s9, 0
s_mov_b32 s10, 4194304
s_mov_b32 s11, 0x20000
s_mov_b32 s14, 4194304
s_mov_b32 s15, 0x20000
s_mov_b32 s18, 3670016
s_mov_b32 s19, 0x20000
s_mov_b32 s22, 802816
s_mov_b32 s23, 0x20000
s_lshl_b32 s64, s69, 14
s_lshl_b32 s65, s68, 12
s_add_u32 s64, s64, s65
v_lshlrev_b32 v169, 2, v159
v_add_u32 v169, s64, v169
s_cmp_eq_u32 s74, 0
s_cbranch_scc1 Lk1_u2
s_cmp_eq_u32 s74, 3
s_cbranch_scc1 Lk1_u1V
s_cmp_eq_u32 s74, 2
s_cbranch_scc1 Lk1_u1K
s_waitcnt vmcnt(16) lgkmcnt(0)
s_barrier
MZavv 0,68,4
Tv 100,132,16384
MZavv 4,72,4
Tv 102,132,18432
MZavv 8,76,4
s_add_u32 s65, s48, s49
s_add_u32 m0, s61, 4096
MZavv 12,80,4
Rv 36,141,0
MZavv 16,84,4
buffer_load_dwordx4 v143, s[24:27], s65 offen lds
MZavv 20,88,4
Tv 104,133,16384
MZavv 24,92,4
MZavv 28,96,4
Tv 106,133,18432
MZavv 32,68,8
Rv 40,141,2048
MZavv 36,72,8
MZavv 40,76,8
Tv 108,134,16384
MZavv 44,80,8
buffer_load_dwordx4 v144, s[24:27], s65 offen offset:1024 lds
MZavv 48,84,8
Tv 110,134,18432
MZavv 52,88,8
MZavv 56,92,8
Rv 44,141,4096
MZavv 60,96,8
Tv 112,135,16384
MZavv 64,68,12
MZavv 68,72,12
Tv 114,135,18432
MZavv 72,76,12
buffer_load_dwordx4 v145, s[24:27], s65 offen offset:2048 lds
MZavv 76,80,12
Rv 48,141,6144
MZavv 80,84,12
MZavv 84,88,12
Tv 116,136,16384
MZavv 88,92,12
Tv 118,136,18432
MZavv 92,96,12
MZavv 96,68,16
Rv 52,141,8192
MZavv 100,72,16
buffer_load_dwordx4 v146, s[24:27], s65 offen offset:3072 lds
MZavv 104,76,16
Tv 120,137,16384
MZavv 108,80,16
s_add_u32 s50, s50, s51
s_cmp_eq_u32 s50, s53
s_cselect_b32 s50, 0, s50
MZavv 112,84,16
Tv 122,137,18432
MZavv 116,88,16
Rv 56,141,10240
MZavv 120,92,16
s_add_u32 s62, s62, 16384
s_and_b32 s62, s62, 0xffff
s_mov_b32 m0, s62
MZavv 124,96,16
Tv 124,138,16384
MZavv 128,68,20
buffer_load_dwordx4 v147, s[28:31], s50 offen lds
MZavv 132,72,20
Tv 126,138,18432
MZavv 136,76,20
MZavv 140,80,20
Rv 60,141,12288
MZavv 144,84,20
Tv 128,139,16384
MZavv 148,88,20
MZavv 152,92,20
Tv 130,139,18432
MZavv 156,96,20
buffer_load_dwordx4 v148, s[28:31], s50 offen offset:1024 lds
MZavv 160,68,24
Rv 64,141,14336
MZavv 164,72,24
MZavv 168,76,24
MZavv 172,80,24
MZavv 176,84,24
MZavv 180,88,24
MZavv 184,92,24
buffer_load_dwordx4 v149, s[28:31], s50 offen offset:2048 lds
MZavv 188,96,24
MZavv 192,68,28
MZavv 196,72,28
MZavv 200,76,28
MZavv 204,80,28
MZavv 208,84,28
MZavv 212,88,28
buffer_load_dwordx4 v150, s[28:31], s50 offen offset:3072 lds
MZavv 216,92,28
MZavv 220,96,28
MZavv 224,68,32
MZavv 228,72,32
MZavv 232,76,32
MZavv 236,80,32
MZavv 240,84,32
MZavv 244,88,32
MZavv 248,92,32
MZavv 252,96,32
s_waitcnt vmcnt(16) lgkmcnt(0)
s_barrier
MAavv 0,100,36
s_add_u32 s63, s63, 32768
s_cmp_eq_u32 s63, 163840
s_cselect_b32 s63, 65536, s63
MAavv 4,104,36
v_add_u32 v142, s63, v140
v_xor_b32 v141, 64, v142
MAavv 8,108,36
v_xor_b32 v132, 0x8000, v132
v_xor_b32 v133, 0x8000, v133
MAavv 12,112,36
v_xor_b32 v134, 0x8000, v134
v_xor_b32 v135, 0x8000, v135
MAavv 16,116,36
v_xor_b32 v136, 0x8000, v136
v_xor_b32 v137, 0x8000, v137
MAavv 20,120,36
v_xor_b32 v138, 0x8000, v138
v_xor_b32 v139, 0x8000, v139
MAavv 24,124,36
Tv 68,132,0
MAavv 28,128,36
Tv 70,132,2048
MAavv 32,100,40
Rv 4,142,0
MAavv 36,104,40
Tv 72,133,0
MAavv 40,108,40
Tv 74,133,2048
MAavv 44,112,40
Rv 8,142,2048
MAavv 48,116,40
s_add_u32 s48, s48, 128
s_cmp_eq_u32 s48, s52
s_cselect_b32 s48, 0, s48
MAavv 52,120,40
Tv 76,134,0
MAavv 56,124,40
s_add_u32 s61, s61, 32768
s_cmp_ge_u32 s61, 163840
s_cselect_b32 s64, 98304, 0
MAavv 60,128,40
Tv 78,134,2048
s_sub_u32 s61, s61, s64
s_mov_b32 m0, s61
MAavv 64,100,44
buffer_load_dwordx4 v143, s[24:27], s48 offen lds
MAavv 68,104,44
Rv 12,142,4096
MAavv 72,108,44
Tv 80,135,0
MAavv 76,112,44
buffer_load_dwordx4 v144, s[24:27], s48 offen offset:1024 lds
MAavv 80,116,44
Tv 82,135,2048
MAavv 84,120,44
buffer_load_dwordx4 v145, s[24:27], s48 offen offset:2048 lds
MAavv 88,124,44
Rv 16,142,6144
MAavv 92,128,44
MAavv 96,100,48
Tv 84,136,0
MAavv 100,104,48
Tv 86,136,2048
MAavv 104,108,48
buffer_load_dwordx4 v146, s[24:27], s48 offen offset:3072 lds
MAavv 108,112,48
Rv 20,142,8192
MAavv 112,116,48
s_add_u32 s50, s50, s51
s_cmp_eq_u32 s50, s53
s_cselect_b32 s50, 0, s50
MAavv 116,120,48
Tv 88,137,0
MAavv 120,124,48
s_add_u32 s62, s62, 16384
s_and_b32 s62, s62, 0xffff
s_mov_b32 m0, s62
MAavv 124,128,48
Tv 90,137,2048
MAavv 128,100,52
Rv 24,142,10240
MAavv 132,104,52
buffer_load_dwordx4 v147, s[28:31], s50 offen lds
MAavv 136,108,52
Tv 92,138,0
MAavv 140,112,52
MAavv 144,116,52
Tv 94,138,2048
MAavv 148,120,52
MAavv 152,124,52
Rv 28,142,12288
MAavv 156,128,52
Tv 96,139,0
MAavv 160,100,56
buffer_load_dwordx4 v148, s[28:31], s50 offen offset:1024 lds
MAavv 164,104,56
Tv 98,139,2048
MAavv 168,108,56
MAavv 172,112,56
Rv 32,142,14336
MAavv 176,116,56
MAavv 180,120,56
MAavv 184,124,56
buffer_load_dwordx4 v149, s[28:31], s50 offen offset:2048 lds
MAavv 188,128,56
MAavv 192,100,60
MAavv 196,104,60
MAavv 200,108,60
MAavv 204,112,60
MAavv 208,116,60
MAavv 212,120,60
buffer_load_dwordx4 v150, s[28:31], s50 offen offset:3072 lds
MAavv 216,124,60
MAavv 220,128,60
MAavv 224,100,64
MAavv 228,104,64
MAavv 232,108,64
MAavv 236,112,64
MAavv 240,116,64
MAavv 244,120,64
MAavv 248,124,64
MAavv 252,128,64
s_waitcnt vmcnt(16) lgkmcnt(0)
s_barrier
MAavv 0,68,4
Tv 100,132,16384
MAavv 4,72,4
Tv 102,132,18432
MAavv 8,76,4
s_add_u32 s65, s48, s49
s_add_u32 m0, s61, 4096
MAavv 12,80,4
Rv 36,141,0
MAavv 16,84,4
buffer_load_dwordx4 v143, s[24:27], s65 offen lds
MAavv 20,88,4
Tv 104,133,16384
MAavv 24,92,4
MAavv 28,96,4
Tv 106,133,18432
MAavv 32,68,8
Rv 40,141,2048
MAavv 36,72,8
MAavv 40,76,8
Tv 108,134,16384
MAavv 44,80,8
buffer_load_dwordx4 v144, s[24:27], s65 offen offset:1024 lds
MAavv 48,84,8
Tv 110,134,18432
MAavv 52,88,8
MAavv 56,92,8
Rv 44,141,4096
MAavv 60,96,8
Tv 112,135,16384
MAavv 64,68,12
MAavv 68,72,12
Tv 114,135,18432
MAavv 72,76,12
buffer_load_dwordx4 v145, s[24:27], s65 offen offset:2048 lds
MAavv 76,80,12
Rv 48,141,6144
MAavv 80,84,12
MAavv 84,88,12
Tv 116,136,16384
MAavv 88,92,12
Tv 118,136,18432
MAavv 92,96,12
MAavv 96,68,16
Rv 52,141,8192
MAavv 100,72,16
buffer_load_dwordx4 v146, s[24:27], s65 offen offset:3072 lds
MAavv 104,76,16
Tv 120,137,16384
MAavv 108,80,16
s_add_u32 s50, s50, s51
s_cmp_eq_u32 s50, s53
s_cselect_b32 s50, 0, s50
MAavv 112,84,16
Tv 122,137,18432
MAavv 116,88,16
Rv 56,141,10240
MAavv 120,92,16
s_add_u32 s62, s62, 16384
s_and_b32 s62, s62, 0xffff
s_mov_b32 m0, s62
MAavv 124,96,16
Tv 124,138,16384
MAavv 128,68,20
buffer_load_dwordx4 v147, s[28:31], s50 offen lds
MAavv 132,72,20
Tv 126,138,18432
MAavv 136,76,20
MAavv 140,80,20
Rv 60,141,12288
MAavv 144,84,20
Tv 128,139,16384
MAavv 148,88,20
MAavv 152,92,20
Tv 130,139,18432
MAavv 156,96,20
buffer_load_dwordx4 v148, s[28:31], s50 offen offset:1024 lds
MAavv 160,68,24
Rv 64,141,14336
MAavv 164,72,24
MAavv 168,76,24
MAavv 172,80,24
MAavv 176,84,24
MAavv 180,88,24
MAavv 184,92,24
buffer_load_dwordx4 v149, s[28:31], s50 offen offset:2048 lds
MAavv 188,96,24
MAavv 192,68,28
MAavv 196,72,28
MAavv 200,76,28
MAavv 204,80,28
MAavv 208,84,28
MAavv 212,88,28
buffer_load_dwordx4 v150, s[28:31], s50 offen offset:3072 lds
MAavv 216,92,28
MAavv 220,96,28
MAavv 224,68,32
MAavv 228,72,32
MAavv 232,76,32
MAavv 236,80,32
MAavv 240,84,32
MAavv 244,88,32
MAavv 248,92,32
MAavv 252,96,32
s_waitcnt vmcnt(16) lgkmcnt(0)
s_barrier
MAavv 0,100,36
s_add_u32 s63, s63, 32768
s_cmp_eq_u32 s63, 163840
s_cselect_b32 s63, 65536, s63
MAavv 4,104,36
v_add_u32 v142, s63, v140
v_xor_b32 v141, 64, v142
MAavv 8,108,36
v_xor_b32 v132, 0x8000, v132
v_xor_b32 v133, 0x8000, v133
MAavv 12,112,36
v_xor_b32 v134, 0x8000, v134
v_xor_b32 v135, 0x8000, v135
MAavv 16,116,36
v_xor_b32 v136, 0x8000, v136
v_xor_b32 v137, 0x8000, v137
MAavv 20,120,36
v_xor_b32 v138, 0x8000, v138
v_xor_b32 v139, 0x8000, v139
MAavv 24,124,36
Tv 68,132,0
MAavv 28,128,36
Tv 70,132,2048
MAavv 32,100,40
Rv 4,142,0
MAavv 36,104,40
Tv 72,133,0
MAavv 40,108,40
Tv 74,133,2048
MAavv 44,112,40
Rv 8,142,2048
MAavv 48,116,40
s_add_u32 s48, s48, 128
s_cmp_eq_u32 s48, s52
s_cselect_b32 s48, 0, s48
MAavv 52,120,40
Tv 76,134,0
MAavv 56,124,40
s_add_u32 s61, s61, 32768
s_cmp_ge_u32 s61, 163840
s_cselect_b32 s64, 98304, 0
MAavv 60,128,40
Tv 78,134,2048
s_sub_u32 s61, s61, s64
s_mov_b32 m0, s61
MAavv 64,100,44
buffer_load_dwordx4 v143, s[24:27], s48 offen lds
MAavv 68,104,44
Rv 12,142,4096
MAavv 72,108,44
Tv 80,135,0
MAavv 76,112,44
buffer_load_dwordx4 v144, s[24:27], s48 offen offset:1024 lds
MAavv 80,116,44
Tv 82,135,2048
MAavv 84,120,44
buffer_load_dwordx4 v145, s[24:27], s48 offen offset:2048 lds
MAavv 88,124,44
Rv 16,142,6144
MAavv 92,128,44
MAavv 96,100,48
Tv 84,136,0
MAavv 100,104,48
Tv 86,136,2048
MAavv 104,108,48
buffer_load_dwordx4 v146, s[24:27], s48 offen offset:3072 lds
MAavv 108,112,48
Rv 20,142,8192
MAavv 112,116,48
s_add_u32 s50, s50, s51
s_cmp_eq_u32 s50, s53
s_cselect_b32 s50, 0, s50
MAavv 116,120,48
Tv 88,137,0
MAavv 120,124,48
s_add_u32 s62, s62, 16384
s_and_b32 s62, s62, 0xffff
s_mov_b32 m0, s62
MAavv 124,128,48
Tv 90,137,2048
MAavv 128,100,52
Rv 24,142,10240
MAavv 132,104,52
buffer_load_dwordx4 v147, s[28:31], s50 offen lds
MAavv 136,108,52
Tv 92,138,0
MAavv 140,112,52
MAavv 144,116,52
Tv 94,138,2048
MAavv 148,120,52
MAavv 152,124,52
Rv 28,142,12288
MAavv 156,128,52
Tv 96,139,0
MAavv 160,100,56
buffer_load_dwordx4 v148, s[28:31], s50 offen offset:1024 lds
MAavv 164,104,56
Tv 98,139,2048
MAavv 168,108,56
MAavv 172,112,56
Rv 32,142,14336
MAavv 176,116,56
MAavv 180,120,56
MAavv 184,124,56
buffer_load_dwordx4 v149, s[28:31], s50 offen offset:2048 lds
MAavv 188,128,56
MAavv 192,100,60
MAavv 196,104,60
MAavv 200,108,60
MAavv 204,112,60
MAavv 208,116,60
MAavv 212,120,60
buffer_load_dwordx4 v150, s[28:31], s50 offen offset:3072 lds
MAavv 216,124,60
MAavv 220,128,60
MAavv 224,100,64
MAavv 228,104,64
MAavv 232,108,64
MAavv 236,112,64
MAavv 240,116,64
MAavv 244,120,64
MAavv 248,124,64
MAavv 252,128,64
s_waitcnt vmcnt(16) lgkmcnt(0)
s_barrier
MAavv 0,68,4
Tv 100,132,16384
MAavv 4,72,4
Tv 102,132,18432
MAavv 8,76,4
s_add_u32 s65, s48, s49
s_add_u32 m0, s61, 4096
MAavv 12,80,4
Rv 36,141,0
MAavv 16,84,4
buffer_load_dwordx4 v143, s[24:27], s65 offen lds
MAavv 20,88,4
Tv 104,133,16384
MAavv 24,92,4
MAavv 28,96,4
Tv 106,133,18432
MAavv 32,68,8
Rv 40,141,2048
MAavv 36,72,8
MAavv 40,76,8
Tv 108,134,16384
MAavv 44,80,8
buffer_load_dwordx4 v144, s[24:27], s65 offen offset:1024 lds
MAavv 48,84,8
Tv 110,134,18432
MAavv 52,88,8
MAavv 56,92,8
Rv 44,141,4096
MAavv 60,96,8
Tv 112,135,16384
MAavv 64,68,12
MAavv 68,72,12
Tv 114,135,18432
MAavv 72,76,12
buffer_load_dwordx4 v145, s[24:27], s65 offen offset:2048 lds
MAavv 76,80,12
Rv 48,141,6144
MAavv 80,84,12
MAavv 84,88,12
Tv 116,136,16384
MAavv 88,92,12
Tv 118,136,18432
MAavv 92,96,12
MAavv 96,68,16
Rv 52,141,8192
MAavv 100,72,16
buffer_load_dwordx4 v146, s[24:27], s65 offen offset:3072 lds
MAavv 104,76,16
Tv 120,137,16384
MAavv 108,80,16
s_add_u32 s50, s50, s51
s_cmp_eq_u32 s50, s53
s_cselect_b32 s50, 0, s50
MAavv 112,84,16
Tv 122,137,18432
MAavv 116,88,16
Rv 56,141,10240
MAavv 120,92,16
s_add_u32 s62, s62, 16384
s_and_b32 s62, s62, 0xffff
s_mov_b32 m0, s62
MAavv 124,96,16
Tv 124,138,16384
MAavv 128,68,20
buffer_load_dwordx4 v147, s[28:31], s50 offen lds
MAavv 132,72,20
Tv 126,138,18432
MAavv 136,76,20
MAavv 140,80,20
Rv 60,141,12288
MAavv 144,84,20
Tv 128,139,16384
MAavv 148,88,20
MAavv 152,92,20
Tv 130,139,18432
MAavv 156,96,20
buffer_load_dwordx4 v148, s[28:31], s50 offen offset:1024 lds
MAavv 160,68,24
Rv 64,141,14336
MAavv 164,72,24
MAavv 168,76,24
MAavv 172,80,24
MAavv 176,84,24
MAavv 180,88,24
MAavv 184,92,24
buffer_load_dwordx4 v149, s[28:31], s50 offen offset:2048 lds
MAavv 188,96,24
MAavv 192,68,28
MAavv 196,72,28
MAavv 200,76,28
MAavv 204,80,28
MAavv 208,84,28
MAavv 212,88,28
buffer_load_dwordx4 v150, s[28:31], s50 offen offset:3072 lds
MAavv 216,92,28
MAavv 220,96,28
MAavv 224,68,32
MAavv 228,72,32
MAavv 232,76,32
MAavv 236,80,32
MAavv 240,84,32
MAavv 244,88,32
MAavv 248,92,32
MAavv 252,96,32
s_waitcnt vmcnt(16) lgkmcnt(0)
s_barrier
MAavv 0,100,36
s_add_u32 s63, s63, 32768
s_cmp_eq_u32 s63, 163840
s_cselect_b32 s63, 65536, s63
MAavv 4,104,36
v_add_u32 v142, s63, v140
v_xor_b32 v141, 64, v142
MAavv 8,108,36
v_xor_b32 v132, 0x8000, v132
v_xor_b32 v133, 0x8000, v133
MAavv 12,112,36
v_xor_b32 v134, 0x8000, v134
v_xor_b32 v135, 0x8000, v135
MAavv 16,116,36
v_xor_b32 v136, 0x8000, v136
v_xor_b32 v137, 0x8000, v137
MAavv 20,120,36
v_xor_b32 v138, 0x8000, v138
v_xor_b32 v139, 0x8000, v139
MAavv 24,124,36
Tv 68,132,0
MAavv 28,128,36
Tv 70,132,2048
MAavv 32,100,40
Rv 4,142,0
MAavv 36,104,40
Tv 72,133,0
MAavv 40,108,40
Tv 74,133,2048
MAavv 44,112,40
Rv 8,142,2048
MAavv 48,116,40
s_add_u32 s48, s48, 128
s_cmp_eq_u32 s48, s52
s_cselect_b32 s48, 0, s48
MAavv 52,120,40
Tv 76,134,0
MAavv 56,124,40
s_add_u32 s61, s61, 32768
s_cmp_ge_u32 s61, 163840
s_cselect_b32 s64, 98304, 0
MAavv 60,128,40
Tv 78,134,2048
s_sub_u32 s61, s61, s64
s_mov_b32 m0, s61
MAavv 64,100,44
buffer_load_dwordx4 v143, s[24:27], s48 offen lds
MAavv 68,104,44
Rv 12,142,4096
MAavv 72,108,44
Tv 80,135,0
MAavv 76,112,44
buffer_load_dwordx4 v144, s[24:27], s48 offen offset:1024 lds
MAavv 80,116,44
Tv 82,135,2048
MAavv 84,120,44
buffer_load_dwordx4 v145, s[24:27], s48 offen offset:2048 lds
MAavv 88,124,44
Rv 16,142,6144
MAavv 92,128,44
MAavv 96,100,48
Tv 84,136,0
MAavv 100,104,48
Tv 86,136,2048
MAavv 104,108,48
buffer_load_dwordx4 v146, s[24:27], s48 offen offset:3072 lds
MAavv 108,112,48
Rv 20,142,8192
MAavv 112,116,48
s_add_u32 s50, s50, s51
s_cmp_eq_u32 s50, s53
s_cselect_b32 s50, 0, s50
MAavv 116,120,48
Tv 88,137,0
MAavv 120,124,48
s_add_u32 s62, s62, 16384
s_and_b32 s62, s62, 0xffff
s_mov_b32 m0, s62
MAavv 124,128,48
Tv 90,137,2048
MAavv 128,100,52
Rv 24,142,10240
MAavv 132,104,52
buffer_load_dwordx4 v147, s[28:31], s50 offen lds
MAavv 136,108,52
Tv 92,138,0
MAavv 140,112,52
MAavv 144,116,52
Tv 94,138,2048
MAavv 148,120,52
MAavv 152,124,52
Rv 28,142,12288
MAavv 156,128,52
Tv 96,139,0
MAavv 160,100,56
buffer_load_dwordx4 v148, s[28:31], s50 offen offset:1024 lds
MAavv 164,104,56
Tv 98,139,2048
MAavv 168,108,56
MAavv 172,112,56
Rv 32,142,14336
MAavv 176,116,56
MAavv 180,120,56
MAavv 184,124,56
buffer_load_dwordx4 v149, s[28:31], s50 offen offset:2048 lds
MAavv 188,128,56
MAavv 192,100,60
MAavv 196,104,60
MAavv 200,108,60
MAavv 204,112,60
MAavv 208,116,60
MAavv 212,120,60
buffer_load_dwordx4 v150, s[28:31], s50 offen offset:3072 lds
MAavv 216,124,60
MAavv 220,128,60
MAavv 224,100,64
MAavv 228,104,64
MAavv 232,108,64
MAavv 236,112,64
MAavv 240,116,64
MAavv 244,120,64
MAavv 248,124,64
MAavv 252,128,64
s_waitcnt vmcnt(16) lgkmcnt(0)
s_barrier
MAavv 0,68,4
Tv 100,132,16384
MAavv 4,72,4
Tv 102,132,18432
MAavv 8,76,4
s_add_u32 s65, s48, s49
s_add_u32 m0, s61, 4096
MAavv 12,80,4
Rv 36,141,0
MAavv 16,84,4
buffer_load_dwordx4 v143, s[24:27], s65 offen lds
MAavv 20,88,4
Tv 104,133,16384
MAavv 24,92,4
MAavv 28,96,4
Tv 106,133,18432
MAavv 32,68,8
Rv 40,141,2048
MAavv 36,72,8
MAavv 40,76,8
Tv 108,134,16384
MAavv 44,80,8
buffer_load_dwordx4 v144, s[24:27], s65 offen offset:1024 lds
MAavv 48,84,8
Tv 110,134,18432
MAavv 52,88,8
MAavv 56,92,8
Rv 44,141,4096
MAavv 60,96,8
Tv 112,135,16384
MAavv 64,68,12
MAavv 68,72,12
Tv 114,135,18432
MAavv 72,76,12
buffer_load_dwordx4 v145, s[24:27], s65 offen offset:2048 lds
MAavv 76,80,12
Rv 48,141,6144
MAavv 80,84,12
MAavv 84,88,12
Tv 116,136,16384
MAavv 88,92,12
Tv 118,136,18432
MAavv 92,96,12
MAavv 96,68,16
Rv 52,141,8192
MAavv 100,72,16
buffer_load_dwordx4 v146, s[24:27], s65 offen offset:3072 lds
MAavv 104,76,16
Tv 120,137,16384
MAavv 108,80,16
s_add_u32 s50, s50, s51
s_cmp_eq_u32 s50, s53
s_cselect_b32 s50, 0, s50
MAavv 112,84,16
Tv 122,137,18432
MAavv 116,88,16
Rv 56,141,10240
MAavv 120,92,16
s_add_u32 s62, s62, 16384
s_and_b32 s62, s62, 0xffff
s_mov_b32 m0, s62
MAavv 124,96,16
Tv 124,138,16384
MAavv 128,68,20
buffer_load_dwordx4 v147, s[28:31], s50 offen lds
MAavv 132,72,20
Tv 126,138,18432
MAavv 136,76,20
MAavv 140,80,20
Rv 60,141,12288
MAavv 144,84,20
Tv 128,139,16384
MAavv 148,88,20
MAavv 152,92,20
Tv 130,139,18432
MAavv 156,96,20
buffer_load_dwordx4 v148, s[28:31], s50 offen offset:1024 lds
MAavv 160,68,24
Rv 64,141,14336
MAavv 164,72,24
MAavv 168,76,24
MAavv 172,80,24
MAavv 176,84,24
)ASM"
R"ASM(MAavv 180,88,24
MAavv 184,92,24
buffer_load_dwordx4 v149, s[28:31], s50 offen offset:2048 lds
MAavv 188,96,24
MAavv 192,68,28
MAavv 196,72,28
MAavv 200,76,28
MAavv 204,80,28
MAavv 208,84,28
MAavv 212,88,28
buffer_load_dwordx4 v150, s[28:31], s50 offen offset:3072 lds
MAavv 216,92,28
MAavv 220,96,28
MAavv 224,68,32
MAavv 228,72,32
MAavv 232,76,32
MAavv 236,80,32
MAavv 240,84,32
MAavv 244,88,32
MAavv 248,92,32
MAavv 252,96,32
s_waitcnt vmcnt(16) lgkmcnt(0)
s_barrier
MAavv 0,100,36
s_add_u32 s63, s63, 32768
s_cmp_eq_u32 s63, 163840
s_cselect_b32 s63, 65536, s63
MAavv 4,104,36
v_add_u32 v142, s63, v140
v_xor_b32 v141, 64, v142
MAavv 8,108,36
v_xor_b32 v132, 0x8000, v132
v_xor_b32 v133, 0x8000, v133
MAavv 12,112,36
v_xor_b32 v134, 0x8000, v134
v_xor_b32 v135, 0x8000, v135
MAavv 16,116,36
v_xor_b32 v136, 0x8000, v136
v_xor_b32 v137, 0x8000, v137
MAavv 20,120,36
v_xor_b32 v138, 0x8000, v138
v_xor_b32 v139, 0x8000, v139
MAavv 24,124,36
Tv 68,132,0
MAavv 28,128,36
Tv 70,132,2048
MAavv 32,100,40
Rv 4,142,0
MAavv 36,104,40
Tv 72,133,0
MAavv 40,108,40
Tv 74,133,2048
MAavv 44,112,40
Rv 8,142,2048
MAavv 48,116,40
s_add_u32 s48, s48, 128
s_cmp_eq_u32 s48, s52
s_cselect_b32 s48, 0, s48
MAavv 52,120,40
Tv 76,134,0
MAavv 56,124,40
s_add_u32 s61, s61, 32768
s_cmp_ge_u32 s61, 163840
s_cselect_b32 s64, 98304, 0
MAavv 60,128,40
Tv 78,134,2048
s_sub_u32 s61, s61, s64
s_mov_b32 m0, s61
MAavv 64,100,44
buffer_load_dwordx4 v143, s[24:27], s48 offen lds
MAavv 68,104,44
Rv 12,142,4096
MAavv 72,108,44
Tv 80,135,0
MAavv 76,112,44
buffer_load_dwordx4 v144, s[24:27], s48 offen offset:1024 lds
MAavv 80,116,44
Tv 82,135,2048
MAavv 84,120,44
buffer_load_dwordx4 v145, s[24:27], s48 offen offset:2048 lds
MAavv 88,124,44
Rv 16,142,6144
MAavv 92,128,44
MAavv 96,100,48
Tv 84,136,0
MAavv 100,104,48
Tv 86,136,2048
MAavv 104,108,48
buffer_load_dwordx4 v146, s[24:27], s48 offen offset:3072 lds
MAavv 108,112,48
Rv 20,142,8192
MAavv 112,116,48
s_add_u32 s50, s50, s51
s_cmp_eq_u32 s50, s53
s_cselect_b32 s50, 0, s50
MAavv 116,120,48
Tv 88,137,0
MAavv 120,124,48
s_add_u32 s62, s62, 16384
s_and_b32 s62, s62, 0xffff
s_mov_b32 m0, s62
MAavv 124,128,48
Tv 90,137,2048
MAavv 128,100,52
Rv 24,142,10240
MAavv 132,104,52
buffer_load_dwordx4 v147, s[28:31], s50 offen lds
MAavv 136,108,52
Tv 92,138,0
MAavv 140,112,52
MAavv 144,116,52
Tv 94,138,2048
MAavv 148,120,52
MAavv 152,124,52
Rv 28,142,12288
MAavv 156,128,52
Tv 96,139,0
MAavv 160,100,56
buffer_load_dwordx4 v148, s[28:31], s50 offen offset:1024 lds
MAavv 164,104,56
Tv 98,139,2048
MAavv 168,108,56
MAavv 172,112,56
Rv 32,142,14336
MAavv 176,116,56
MAavv 180,120,56
MAavv 184,124,56
buffer_load_dwordx4 v149, s[28:31], s50 offen offset:2048 lds
MAavv 188,128,56
MAavv 192,100,60
MAavv 196,104,60
MAavv 200,108,60
MAavv 204,112,60
MAavv 208,116,60
MAavv 212,120,60
buffer_load_dwordx4 v150, s[28:31], s50 offen offset:3072 lds
MAavv 216,124,60
MAavv 220,128,60
MAavv 224,100,64
MAavv 228,104,64
MAavv 232,108,64
MAavv 236,112,64
MAavv 240,116,64
MAavv 244,120,64
MAavv 248,124,64
MAavv 252,128,64
s_waitcnt vmcnt(16) lgkmcnt(0)
s_barrier
MAavv 0,68,4
Tv 100,132,16384
MAavv 4,72,4
Tv 102,132,18432
MAavv 8,76,4
s_add_u32 s65, s48, s49
s_add_u32 m0, s61, 4096
MAavv 12,80,4
Rv 36,141,0
MAavv 16,84,4
buffer_load_dwordx4 v143, s[24:27], s65 offen lds
MAavv 20,88,4
Tv 104,133,16384
MAavv 24,92,4
MAavv 28,96,4
Tv 106,133,18432
MAavv 32,68,8
Rv 40,141,2048
MAavv 36,72,8
MAavv 40,76,8
Tv 108,134,16384
MAavv 44,80,8
buffer_load_dwordx4 v144, s[24:27], s65 offen offset:1024 lds
MAavv 48,84,8
Tv 110,134,18432
MAavv 52,88,8
MAavv 56,92,8
Rv 44,141,4096
MAavv 60,96,8
Tv 112,135,16384
MAavv 64,68,12
MAavv 68,72,12
Tv 114,135,18432
MAavv 72,76,12
buffer_load_dwordx4 v145, s[24:27], s65 offen offset:2048 lds
MAavv 76,80,12
Rv 48,141,6144
MAavv 80,84,12
MAavv 84,88,12
Tv 116,136,16384
MAavv 88,92,12
Tv 118,136,18432
MAavv 92,96,12
MAavv 96,68,16
Rv 52,141,8192
MAavv 100,72,16
buffer_load_dwordx4 v146, s[24:27], s65 offen offset:3072 lds
MAavv 104,76,16
Tv 120,137,16384
MAavv 108,80,16
s_add_u32 s50, s50, s51
s_cmp_eq_u32 s50, s53
s_cselect_b32 s50, 0, s50
MAavv 112,84,16
Tv 122,137,18432
MAavv 116,88,16
Rv 56,141,10240
MAavv 120,92,16
s_add_u32 s62, s62, 16384
s_and_b32 s62, s62, 0xffff
s_mov_b32 m0, s62
MAavv 124,96,16
Tv 124,138,16384
MAavv 128,68,20
buffer_load_dwordx4 v147, s[28:31], s50 offen lds
MAavv 132,72,20
Tv 126,138,18432
MAavv 136,76,20
MAavv 140,80,20
Rv 60,141,12288
MAavv 144,84,20
Tv 128,139,16384
MAavv 148,88,20
MAavv 152,92,20
Tv 130,139,18432
MAavv 156,96,20
buffer_load_dwordx4 v148, s[28:31], s50 offen offset:1024 lds
MAavv 160,68,24
Rv 64,141,14336
MAavv 164,72,24
MAavv 168,76,24
MAavv 172,80,24
MAavv 176,84,24
MAavv 180,88,24
MAavv 184,92,24
buffer_load_dwordx4 v149, s[28:31], s50 offen offset:2048 lds
MAavv 188,96,24
MAavv 192,68,28
MAavv 196,72,28
MAavv 200,76,28
MAavv 204,80,28
MAavv 208,84,28
MAavv 212,88,28
buffer_load_dwordx4 v150, s[28:31], s50 offen offset:3072 lds
MAavv 216,92,28
MAavv 220,96,28
MAavv 224,68,32
MAavv 228,72,32
MAavv 232,76,32
MAavv 236,80,32
MAavv 240,84,32
MAavv 244,88,32
MAavv 248,92,32
MAavv 252,96,32
s_waitcnt vmcnt(16) lgkmcnt(0)
s_barrier
MAavv 0,100,36
s_add_u32 s63, s63, 32768
s_cmp_eq_u32 s63, 163840
s_cselect_b32 s63, 65536, s63
MAavv 4,104,36
v_add_u32 v142, s63, v140
v_xor_b32 v141, 64, v142
MAavv 8,108,36
v_xor_b32 v132, 0x8000, v132
v_xor_b32 v133, 0x8000, v133
MAavv 12,112,36
v_xor_b32 v134, 0x8000, v134
v_xor_b32 v135, 0x8000, v135
MAavv 16,116,36
v_xor_b32 v136, 0x8000, v136
v_xor_b32 v137, 0x8000, v137
MAavv 20,120,36
v_xor_b32 v138, 0x8000, v138
v_xor_b32 v139, 0x8000, v139
MAavv 24,124,36
Tv 68,132,0
MAavv 28,128,36
Tv 70,132,2048
MAavv 32,100,40
Rv 4,142,0
MAavv 36,104,40
Tv 72,133,0
MAavv 40,108,40
Tv 74,133,2048
MAavv 44,112,40
Rv 8,142,2048
MAavv 48,116,40
s_mov_b64 s[24:25], s[36:37]
s_mov_b64 s[26:27], s[38:39]
s_mov_b32 s48, s58
MAavv 52,120,40
Tv 76,134,0
s_mov_b32 s49, s54
s_mov_b32 s52, s56
MAavv 56,124,40
v_mov_b32 v143, v151
v_mov_b32 v144, v152
MAavv 60,128,40
Tv 78,134,2048
v_mov_b32 v145, v153
MAavv 64,100,44
v_mov_b32 v146, v154
MAavv 68,104,44
Rv 12,142,4096
MAavv 72,108,44
Tv 80,135,0
MAavv 76,112,44
s_add_u32 s61, s61, 32768
s_cmp_ge_u32 s61, 163840
s_cselect_b32 s64, 98304, 0
MAavv 80,116,44
Tv 82,135,2048
s_sub_u32 s61, s61, s64
s_mov_b32 m0, s61
MAavv 84,120,44
buffer_load_dwordx4 v143, s[24:27], s48 offen lds
MAavv 88,124,44
Rv 16,142,6144
MAavv 92,128,44
buffer_load_dwordx4 v144, s[24:27], s48 offen offset:1024 lds
MAavv 96,100,48
Tv 84,136,0
MAavv 100,104,48
Tv 86,136,2048
MAavv 104,108,48
buffer_load_dwordx4 v145, s[24:27], s48 offen offset:2048 lds
MAavv 108,112,48
Rv 20,142,8192
MAavv 112,116,48
buffer_load_dwordx4 v146, s[24:27], s48 offen offset:3072 lds
MAavv 116,120,48
Tv 88,137,0
MAavv 120,124,48
s_add_u32 s50, s50, s51
s_cmp_eq_u32 s50, s53
s_cselect_b32 s50, 0, s50
MAavv 124,128,48
Tv 90,137,2048
MAavv 128,100,52
Rv 24,142,10240
MAavv 132,104,52
s_add_u32 s62, s62, 16384
s_and_b32 s62, s62, 0xffff
s_mov_b32 m0, s62
MAavv 136,108,52
Tv 92,138,0
MAavv 140,112,52
buffer_load_dwordx4 v147, s[28:31], s50 offen lds
MAavv 144,116,52
Tv 94,138,2048
MAavv 148,120,52
MAavv 152,124,52
Rv 28,142,12288
MAavv 156,128,52
Tv 96,139,0
MAavv 160,100,56
buffer_load_dwordx4 v148, s[28:31], s50 offen offset:1024 lds
MAavv 164,104,56
Tv 98,139,2048
MAavv 168,108,56
MAavv 172,112,56
Rv 32,142,14336
MAavv 176,116,56
MAavv 180,120,56
MAavv 184,124,56
buffer_load_dwordx4 v149, s[28:31], s50 offen offset:2048 lds
MAavv 188,128,56
MAavv 192,100,60
MAavv 196,104,60
MAavv 200,108,60
MAavv 204,112,60
MAavv 208,116,60
MAavv 212,120,60
buffer_load_dwordx4 v150, s[28:31], s50 offen offset:3072 lds
MAavv 216,124,60
MAavv 220,128,60
MAavv 224,100,64
MAavv 228,104,64
MAavv 232,108,64
MAavv 236,112,64
MAavv 240,116,64
MAavv 244,120,64
MAavv 248,124,64
MAavv 252,128,64
s_waitcnt vmcnt(16) lgkmcnt(0)
s_barrier
MAavv 0,68,4
Tv 100,132,16384
MAavv 4,72,4
Tv 102,132,18432
MAavv 8,76,4
s_add_u32 s65, s48, s49
s_add_u32 m0, s61, 4096
MAavv 12,80,4
Rv 36,141,0
MAavv 16,84,4
buffer_load_dwordx4 v143, s[24:27], s65 offen lds
MAavv 20,88,4
Tv 104,133,16384
MAavv 24,92,4
MAavv 28,96,4
Tv 106,133,18432
MAavv 32,68,8
Rv 40,141,2048
MAavv 36,72,8
MAavv 40,76,8
Tv 108,134,16384
MAavv 44,80,8
buffer_load_dwordx4 v144, s[24:27], s65 offen offset:1024 lds
MAavv 48,84,8
Tv 110,134,18432
MAavv 52,88,8
MAavv 56,92,8
Rv 44,141,4096
MAavv 60,96,8
Tv 112,135,16384
MAavv 64,68,12
MAavv 68,72,12
Tv 114,135,18432
MAavv 72,76,12
buffer_load_dwordx4 v145, s[24:27], s65 offen offset:2048 lds
MAavv 76,80,12
Rv 48,141,6144
MAavv 80,84,12
MAavv 84,88,12
Tv 116,136,16384
MAavv 88,92,12
Tv 118,136,18432
MAavv 92,96,12
MAavv 96,68,16
Rv 52,141,8192
MAavv 100,72,16
buffer_load_dwordx4 v146, s[24:27], s65 offen offset:3072 lds
MAavv 104,76,16
Tv 120,137,16384
MAavv 108,80,16
s_mov_b64 s[28:29], s[40:41]
s_mov_b64 s[30:31], s[42:43]
s_mov_b32 s50, s59
MAavv 112,84,16
Tv 122,137,18432
s_mov_b32 s51, s55
s_mov_b32 s53, s57
MAavv 116,88,16
Rv 56,141,10240
v_mov_b32 v147, v155
MAavv 120,92,16
v_mov_b32 v148, v156
v_mov_b32 v149, v157
MAavv 124,96,16
Tv 124,138,16384
v_mov_b32 v150, v158
MAavv 128,68,20
s_add_u32 s62, s62, 16384
s_and_b32 s62, s62, 0xffff
s_mov_b32 m0, s62
MAavv 132,72,20
Tv 126,138,18432
MAavv 136,76,20
buffer_load_dwordx4 v147, s[28:31], s50 offen lds
MAavv 140,80,20
Rv 60,141,12288
MAavv 144,84,20
Tv 128,139,16384
MAavv 148,88,20
MAavv 152,92,20
Tv 130,139,18432
MAavv 156,96,20
buffer_load_dwordx4 v148, s[28:31], s50 offen offset:1024 lds
MAavv 160,68,24
Rv 64,141,14336
MAavv 164,72,24
MAavv 168,76,24
MAavv 172,80,24
MAavv 176,84,24
MAavv 180,88,24
MAavv 184,92,24
buffer_load_dwordx4 v149, s[28:31], s50 offen offset:2048 lds
MAavv 188,96,24
MAavv 192,68,28
MAavv 196,72,28
MAavv 200,76,28
MAavv 204,80,28
MAavv 208,84,28
MAavv 212,88,28
buffer_load_dwordx4 v150, s[28:31], s50 offen offset:3072 lds
MAavv 216,92,28
MAavv 220,96,28
MAavv 224,68,32
MAavv 228,72,32
MAavv 232,76,32
MAavv 236,80,32
MAavv 240,84,32
MAavv 244,88,32
MAavv 248,92,32
MAavv 252,96,32
s_waitcnt vmcnt(16) lgkmcnt(0)
s_barrier
MAavv 0,100,36
s_add_u32 s63, s63, 32768
s_cmp_eq_u32 s63, 163840
s_cselect_b32 s63, 65536, s63
MAavv 4,104,36
v_add_u32 v142, s63, v140
v_xor_b32 v141, 64, v142
MAavv 8,108,36
v_xor_b32 v132, 0x8000, v132
v_xor_b32 v133, 0x8000, v133
MAavv 12,112,36
v_xor_b32 v134, 0x8000, v134
v_xor_b32 v135, 0x8000, v135
MAavv 16,116,36
v_xor_b32 v136, 0x8000, v136
v_xor_b32 v137, 0x8000, v137
MAavv 20,120,36
v_xor_b32 v138, 0x8000, v138
v_xor_b32 v139, 0x8000, v139
MAavv 24,124,36
Tv 68,132,0
MAavv 28,128,36
Tv 70,132,2048
MAavv 32,100,40
Rv 4,142,0
MAavv 36,104,40
Tv 72,133,0
MAavv 40,108,40
Tv 74,133,2048
MAavv 44,112,40
Rv 8,142,2048
MAavv 48,116,40
s_add_u32 s48, s48, 128
s_cmp_eq_u32 s48, s52
s_cselect_b32 s48, 0, s48
MAavv 52,120,40
Tv 76,134,0
MAavv 56,124,40
s_add_u32 s61, s61, 32768
s_cmp_ge_u32 s61, 163840
s_cselect_b32 s64, 98304, 0
MAavv 60,128,40
Tv 78,134,2048
s_sub_u32 s61, s61, s64
s_mov_b32 m0, s61
MAavv 64,100,44
buffer_load_dwordx4 v143, s[24:27], s48 offen lds
MAavv 68,104,44
Rv 12,142,4096
MAavv 72,108,44
Tv 80,135,0
MAavv 76,112,44
buffer_load_dwordx4 v144, s[24:27], s48 offen offset:1024 lds
MAavv 80,116,44
Tv 82,135,2048
MAavv 84,120,44
buffer_load_dwordx4 v145, s[24:27], s48 offen offset:2048 lds
MAavv 88,124,44
Rv 16,142,6144
MAavv 92,128,44
MAavv 96,100,48
Tv 84,136,0
MAavv 100,104,48
Tv 86,136,2048
MAavv 104,108,48
buffer_load_dwordx4 v146, s[24:27], s48 offen offset:3072 lds
MAavv 108,112,48
Rv 20,142,8192
MAavv 112,116,48
s_add_u32 s50, s50, s51
s_cmp_eq_u32 s50, s53
s_cselect_b32 s50, 0, s50
MAavv 116,120,48
Tv 88,137,0
MAavv 120,124,48
s_add_u32 s62, s62, 16384
s_and_b32 s62, s62, 0xffff
s_mov_b32 m0, s62
MAavv 124,128,48
Tv 90,137,2048
MAavv 128,100,52
Rv 24,142,10240
MAavv 132,104,52
buffer_load_dwordx4 v147, s[28:31], s50 offen lds
MAavv 136,108,52
Tv 92,138,0
MAavv 140,112,52
MAavv 144,116,52
Tv 94,138,2048
MAavv 148,120,52
MAavv 152,124,52
Rv 28,142,12288
MAavv 156,128,52
Tv 96,139,0
MAavv 160,100,56
buffer_load_dwordx4 v148, s[28:31], s50 offen offset:1024 lds
MAavv 164,104,56
Tv 98,139,2048
MAavv 168,108,56
MAavv 172,112,56
Rv 32,142,14336
MAavv 176,116,56
MAavv 180,120,56
MAavv 184,124,56
buffer_load_dwordx4 v149, s[28:31], s50 offen offset:2048 lds
MAavv 188,128,56
MAavv 192,100,60
MAavv 196,104,60
MAavv 200,108,60
MAavv 204,112,60
MAavv 208,116,60
MAavv 212,120,60
buffer_load_dwordx4 v150, s[28:31], s50 offen offset:3072 lds
MAavv 216,124,60
MAavv 220,128,60
MAavv 224,100,64
MAavv 228,104,64
MAavv 232,108,64
MAavv 236,112,64
MAavv 240,116,64
MAavv 244,120,64
MAavv 248,124,64
MAavv 252,128,64
s_waitcnt vmcnt(16) lgkmcnt(0)
s_barrier
MAavv 0,68,4
Tv 100,132,16384
MAavv 4,72,4
Tv 102,132,18432
MAavv 8,76,4
Rv 36,141,0
s_add_u32 s65, s48, s49
s_add_u32 m0, s61, 4096
MAavv 12,80,4
Tv 104,133,16384
MAavv 16,84,4
Tv 106,133,18432
MAavv 20,88,4
Rv 40,141,2048
MAavv 24,92,4
Tv 108,134,16384
MAavv 28,96,4
Tv 110,134,18432
MAavv 32,68,8
Rv 44,141,4096
MAavv 36,72,8
Tv 112,135,16384
MAavv 40,76,8
Tv 114,135,18432
MAavv 44,80,8
Rv 48,141,6144
MAavv 48,84,8
buffer_load_dwordx4 v143, s[24:27], s65 offen lds
MAavv 52,88,8
Tv 116,136,16384
MAavv 56,92,8
Tv 118,136,18432
MAavv 60,96,8
Rv 52,141,8192
MAavv 64,68,12
Tv 120,137,16384
MAavv 68,72,12
Tv 122,137,18432
MAavv 72,76,12
Rv 56,141,10240
MAavv 76,80,12
Tv 124,138,16384
MAavv 80,84,12
Tv 126,138,18432
MAavv 84,88,12
Rv 60,141,12288
MAavv 88,92,12
Tv 128,139,16384
MAavv 92,96,12
Tv 130,139,18432
MAavv 96,68,16
Rv 64,141,14336
MAavv 100,72,16
buffer_load_dwordx4 v144, s[24:27], s65 offen offset:1024 lds
MAavv 104,76,16
buffer_load_dwordx4 v145, s[24:27], s65 offen offset:2048 lds
MAavv 108,80,16
buffer_load_dwordx4 v146, s[24:27], s65 offen offset:3072 lds
MAavv 112,84,16
s_add_u32 s50, s50, s51
s_cmp_eq_u32 s50, s53
s_cselect_b32 s50, 0, s50
MAavv 116,88,16
s_add_u32 s62, s62, 16384
s_and_b32 s62, s62, 0xffff
s_mov_b32 m0, s62
MAavv 120,92,16
buffer_load_dwordx4 v147, s[28:31], s50 offen lds
MAavv 124,96,16
s_waitcnt vmcnt(13) lgkmcnt(0)
s_barrier
buffer_load_dwordx4 v148, s[28:31], s50 offen offset:1024 lds
MAavv 0,100,36
s_add_u32 s63, s63, 32768
s_cmp_eq_u32 s63, 163840
s_cselect_b32 s63, 65536, s63
MAavv 4,104,36
v_add_u32 v142, s63, v140
v_xor_b32 v141, 64, v142
MAavv 8,108,36
buffer_load_dwordx4 v149, s[28:31], s50 offen offset:2048 lds
MAavv 12,112,36
buffer_load_dwordx4 v150, s[28:31], s50 offen offset:3072 lds
MAavv 16,116,36
v_xor_b32 v132, 0x8000, v132
v_xor_b32 v133, 0x8000, v133
MAavv 20,120,36
v_xor_b32 v134, 0x8000, v134
v_xor_b32 v135, 0x8000, v135
MAavv 24,124,36
v_xor_b32 v136, 0x8000, v136
v_xor_b32 v137, 0x8000, v137
MAavv 28,128,36
v_xor_b32 v138, 0x8000, v138
v_xor_b32 v139, 0x8000, v139
MAavv 32,100,40
s_add_u32 s48, s48, 128
s_cmp_eq_u32 s48, s52
s_cselect_b32 s48, 0, s48
MAavv 36,104,40
s_add_u32 s61, s61, 32768
s_cmp_ge_u32 s61, 163840
s_cselect_b32 s64, 98304, 0
MAavv 40,108,40
s_sub_u32 s61, s61, s64
s_mov_b32 m0, s61
Rv 4,142,0
MAavv 44,112,40
buffer_load_dwordx4 v143, s[24:27], s48 offen lds
Rv 8,142,2048
MAavv 48,116,40
buffer_load_dwordx4 v144, s[24:27], s48 offen offset:1024 lds
Rv 12,142,4096
MAavv 52,120,40
buffer_load_dwordx4 v145, s[24:27], s48 offen offset:2048 lds
Rv 16,142,6144
MAavv 56,124,40
buffer_load_dwordx4 v146, s[24:27], s48 offen offset:3072 lds
AR 172,0
AR 173,1
MAavv 60,128,40
s_add_u32 s50, s50, s51
s_cmp_eq_u32 s50, s53
s_cselect_b32 s50, 0, s50
AR 174,2
AR 175,3
MAavv 64,100,44
s_add_u32 s62, s62, 16384
s_and_b32 s62, s62, 0xffff
s_mov_b32 m0, s62
AR 176,4
AR 177,5
MAavv 68,104,44
buffer_load_dwordx4 v147, s[28:31], s50 offen lds
AR 178,6
AR 179,7
MAavv 72,108,44
buffer_load_dwordx4 v148, s[28:31], s50 offen offset:1024 lds
v_mul_f32 v172, s78, v172
v_mul_f32 v173, s78, v173
MAavv 76,112,44
buffer_load_dwordx4 v149, s[28:31], s50 offen offset:2048 lds
v_mul_f32 v174, s78, v174
v_mul_f32 v175, s78, v175
MAavv 80,116,44
buffer_load_dwordx4 v150, s[28:31], s50 offen offset:3072 lds
v_mul_f32 v176, s78, v176
v_mul_f32 v177, s78, v177
MAavv 84,120,44
v_mul_f32 v178, s78, v178
v_mul_f32 v179, s78, v179
CV 204,172,173
CV 205,174,175
MAavv 88,124,44
CV 206,176,177
CV 207,178,179
s_mul_i32 s60, s71, 0
s_add_u32 s60, s60, s70
buffer_store_dwordx4 v[204:207], v159, s[44:47], s60 offen
MAavv 92,128,44
AR 180,8
AR 181,9
AR 182,10
AR 183,11
MAavv 96,100,48
AR 184,12
AR 185,13
AR 186,14
AR 187,15
MAavv 100,104,48
v_mul_f32 v180, s78, v180
v_mul_f32 v181, s78, v181
v_mul_f32 v182, s78, v182
v_mul_f32 v183, s78, v183
MAavv 104,108,48
v_mul_f32 v184, s78, v184
v_mul_f32 v185, s78, v185
v_mul_f32 v186, s78, v186
v_mul_f32 v187, s78, v187
MAavv 108,112,48
CV 208,180,181
CV 209,182,183
CV 210,184,185
CV 211,186,187
MAavv 112,116,48
buffer_store_dwordx4 v[208:211], v159, s[44:47], s60 offen offset:1024
AR 188,16
AR 189,17
MAavv 116,120,48
AR 190,18
AR 191,19
AR 192,20
AR 193,21
MAavv 120,124,48
AR 194,22
AR 195,23
v_mul_f32 v188, s78, v188
v_mul_f32 v189, s78, v189
MAavv 124,128,48
v_mul_f32 v190, s78, v190
v_mul_f32 v191, s78, v191
v_mul_f32 v192, s78, v192
v_mul_f32 v193, s78, v193
MAavv 128,68,20
v_mul_f32 v194, s78, v194
v_mul_f32 v195, s78, v195
CV 212,188,189
CV 213,190,191
MAavv 132,72,20
CV 214,192,193
CV 215,194,195
buffer_store_dwordx4 v[212:215], v159, s[44:47], s60 offen offset:2048
MAavv 136,76,20
AR 196,24
AR 197,25
AR 198,26
AR 199,27
MAavv 140,80,20
AR 200,28
AR 201,29
AR 202,30
AR 203,31
MAavv 144,84,20
v_mul_f32 v196, s78, v196
v_mul_f32 v197, s78, v197
v_mul_f32 v198, s78, v198
v_mul_f32 v199, s78, v199
MAavv 148,88,20
v_mul_f32 v200, s78, v200
v_mul_f32 v201, s78, v201
v_mul_f32 v202, s78, v202
v_mul_f32 v203, s78, v203
MAavv 152,92,20
CV 216,196,197
CV 217,198,199
CV 218,200,201
CV 219,202,203
MAavv 156,96,20
buffer_store_dwordx4 v[216:219], v159, s[44:47], s60 offen offset:3072
AR 172,32
AR 173,33
MAavv 160,68,24
AR 174,34
AR 175,35
AR 176,36
AR 177,37
MAavv 164,72,24
AR 178,38
AR 179,39
v_mul_f32 v172, s78, v172
v_mul_f32 v173, s78, v173
MAavv 168,76,24
v_mul_f32 v174, s78, v174
v_mul_f32 v175, s78, v175
v_mul_f32 v176, s78, v176
v_mul_f32 v177, s78, v177
MAavv 172,80,24
v_mul_f32 v178, s78, v178
v_mul_f32 v179, s78, v179
CV 220,172,173
CV 221,174,175
MAavv 176,84,24
CV 222,176,177
CV 223,178,179
s_mul_i32 s60, s71, 1
s_add_u32 s60, s60, s70
buffer_store_dwordx4 v[220:223], v159, s[44:47], s60 offen
MAavv 180,88,24
AR 180,40
AR 181,41
AR 182,42
AR 183,43
MAavv 184,92,24
AR 184,44
AR 185,45
AR 186,46
AR 187,47
MAavv 188,96,24
v_mul_f32 v180, s78, v180
v_mul_f32 v181, s78, v181
v_mul_f32 v182, s78, v182
v_mul_f32 v183, s78, v183
MAavv 192,68,28
v_mul_f32 v184, s78, v184
v_mul_f32 v185, s78, v185
v_mul_f32 v186, s78, v186
v_mul_f32 v187, s78, v187
MAavv 196,72,28
CV 224,180,181
CV 225,182,183
CV 226,184,185
CV 227,186,187
MAavv 200,76,28
buffer_store_dwordx4 v[224:227], v159, s[44:47], s60 offen offset:1024
AR 188,48
AR 189,49
MAavv 204,80,28
AR 190,50
AR 191,51
AR 192,52
AR 193,53
MAavv 208,84,28
AR 194,54
AR 195,55
v_mul_f32 v188, s78, v188
v_mul_f32 v189, s78, v189
MAavv 212,88,28
v_mul_f32 v190, s78, v190
v_mul_f32 v191, s78, v191
v_mul_f32 v192, s78, v192
v_mul_f32 v193, s78, v193
MAavv 216,92,28
v_mul_f32 v194, s78, v194
v_mul_f32 v195, s78, v195
CV 228,188,189
CV 229,190,191
MAavv 220,96,28
CV 230,192,193
CV 231,194,195
buffer_store_dwordx4 v[228:231], v159, s[44:47], s60 offen offset:2048
MAavv 224,68,32
AR 196,56
AR 197,57
AR 198,58
AR 199,59
MAavv 228,72,32
AR 200,60
AR 201,61
AR 202,62
AR 203,63
MAavv 232,76,32
v_mul_f32 v196, s78, v196
v_mul_f32 v197, s78, v197
v_mul_f32 v198, s78, v198
v_mul_f32 v199, s78, v199
MAavv 236,80,32
v_mul_f32 v200, s78, v200
v_mul_f32 v201, s78, v201
v_mul_f32 v202, s78, v202
v_mul_f32 v203, s78, v203
MAavv 240,84,32
CV 232,196,197
CV 233,198,199
CV 234,200,201
CV 235,202,203
MAavv 244,88,32
buffer_store_dwordx4 v[232:235], v159, s[44:47], s60 offen offset:3072
AR 172,64
AR 173,65
MAavv 248,92,32
AR 174,66
AR 175,67
AR 176,68
AR 177,69
MAavv 252,96,32
AR 178,70
AR 179,71
v_mul_f32 v172, s78, v172
v_mul_f32 v173, s78, v173
MAavv 128,100,52
Tv 68,132,0
v_mul_f32 v174, s78, v174
v_mul_f32 v175, s78, v175
MAavv 132,104,52
Tv 70,132,2048
v_mul_f32 v176, s78, v176
v_mul_f32 v177, s78, v177
MAavv 136,108,52
v_mul_f32 v178, s78, v178
v_mul_f32 v179, s78, v179
CV 204,172,173
CV 205,174,175
MAavv 140,112,52
Tv 72,133,0
CV 206,176,177
CV 207,178,179
MAavv 144,116,52
Tv 74,133,2048
s_mul_i32 s60, s71, 2
s_add_u32 s60, s60, s70
buffer_store_dwordx4 v[204:207], v159, s[44:47], s60 offen
MAavv 148,120,52
AR 180,72
AR 181,73
AR 182,74
AR 183,75
MAavv 152,124,52
Tv 76,134,0
AR 184,76
AR 185,77
MAavv 156,128,52
Tv 78,134,2048
AR 186,78
AR 187,79
MAavv 160,100,56
v_mul_f32 v180, s78, v180
v_mul_f32 v181, s78, v181
v_mul_f32 v182, s78, v182
v_mul_f32 v183, s78, v183
MAavv 164,104,56
Tv 80,135,0
v_mul_f32 v184, s78, v184
v_mul_f32 v185, s78, v185
MAavv 168,108,56
Tv 82,135,2048
v_mul_f32 v186, s78, v186
v_mul_f32 v187, s78, v187
MAavv 172,112,56
CV 208,180,181
CV 209,182,183
CV 210,184,185
CV 211,186,187
MAavv 176,116,56
Tv 84,136,0
buffer_store_dwordx4 v[208:211], v159, s[44:47], s60 offen offset:1024
MAavv 180,120,56
Tv 86,136,2048
AR 188,80
AR 189,81
MAavv 184,124,56
Rv 20,142,8192
AR 190,82
AR 191,83
MAavv 188,128,56
Tv 88,137,0
AR 192,84
AR 193,85
MAavv 192,100,60
Tv 90,137,2048
AR 194,86
AR 195,87
MAavv 196,104,60
Rv 24,142,10240
v_mul_f32 v188, s78, v188
v_mul_f32 v189, s78, v189
MAavv 200,108,60
Tv 92,138,0
v_mul_f32 v190, s78, v190
v_mul_f32 v191, s78, v191
MAavv 204,112,60
Tv 94,138,2048
v_mul_f32 v192, s78, v192
v_mul_f32 v193, s78, v193
MAavv 208,116,60
Rv 28,142,12288
v_mul_f32 v194, s78, v194
v_mul_f32 v195, s78, v195
MAavv 212,120,60
Tv 96,139,0
CV 212,188,189
CV 213,190,191
MAavv 216,124,60
Tv 98,139,2048
CV 214,192,193
CV 215,194,195
MAavv 220,128,60
Rv 32,142,14336
buffer_store_dwordx4 v[212:215], v159, s[44:47], s60 offen offset:2048
MAavv 224,100,64
AR 196,88
AR 197,89
AR 198,90
AR 199,91
MAavv 228,104,64
AR 200,92
AR 201,93
AR 202,94
AR 203,95
MAavv 232,108,64
v_mul_f32 v196, s78, v196
v_mul_f32 v197, s78, v197
v_mul_f32 v198, s78, v198
v_mul_f32 v199, s78, v199
MAavv 236,112,64
v_mul_f32 v200, s78, v200
v_mul_f32 v201, s78, v201
v_mul_f32 v202, s78, v202
v_mul_f32 v203, s78, v203
MAavv 240,116,64
CV 216,196,197
CV 217,198,199
CV 218,200,201
CV 219,202,203
MAavv 244,120,64
buffer_store_dwordx4 v[216:219], v159, s[44:47], s60 offen offset:3072
AR 172,96
AR 173,97
AR 174,98
AR 175,99
AR 176,100
AR 177,101
AR 178,102
AR 179,103
v_mul_f32 v172, s78, v172
v_mul_f32 v173, s78, v173
v_mul_f32 v174, s78, v174
v_mul_f32 v175, s78, v175
v_mul_f32 v176, s78, v176
v_mul_f32 v177, s78, v177
v_mul_f32 v178, s78, v178
v_mul_f32 v179, s78, v179
CV 220,172,173
CV 221,174,175
CV 222,176,177
CV 223,178,179
s_mul_i32 s60, s71, 3
s_add_u32 s60, s60, s70
buffer_store_dwordx4 v[220:223], v159, s[44:47], s60 offen
AR 180,104
AR 181,105
AR 182,106
AR 183,107
AR 184,108
AR 185,109
AR 186,110
AR 187,111
v_mul_f32 v180, s78, v180
v_mul_f32 v181, s78, v181
v_mul_f32 v182, s78, v182
v_mul_f32 v183, s78, v183
v_mul_f32 v184, s78, v184
v_mul_f32 v185, s78, v185
v_mul_f32 v186, s78, v186
v_mul_f32 v187, s78, v187
CV 224,180,181
CV 225,182,183
CV 226,184,185
CV 227,186,187
buffer_store_dwordx4 v[224:227], v159, s[44:47], s60 offen offset:1024
AR 188,112
AR 189,113
AR 190,114
AR 191,115
AR 192,116
AR 193,117
AR 194,118
AR 195,119
v_mul_f32 v188, s78, v188
v_mul_f32 v189, s78, v189
v_mul_f32 v190, s78, v190
v_mul_f32 v191, s78, v191
v_mul_f32 v192, s78, v192
v_mul_f32 v193, s78, v193
v_mul_f32 v194, s78, v194
v_mul_f32 v195, s78, v195
CV 228,188,189
CV 229,190,191
CV 230,192,193
CV 231,194,195
buffer_store_dwordx4 v[228:231], v159, s[44:47], s60 offen offset:2048
AR 196,120
AR 197,121
AR 198,122
AR 199,123
AR 200,124
AR 201,125
AR 202,126
AR 203,127
v_mul_f32 v196, s78, v196
v_mul_f32 v197, s78, v197
v_mul_f32 v198, s78, v198
v_mul_f32 v199, s78, v199
v_mul_f32 v200, s78, v200
v_mul_f32 v201, s78, v201
v_mul_f32 v202, s78, v202
v_mul_f32 v203, s78, v203
CV 232,196,197
CV 233,198,199
CV 234,200,201
CV 235,202,203
buffer_store_dwordx4 v[232:235], v159, s[44:47], s60 offen offset:3072
MAavv 248,124,64
AR 172,128
AR 173,129
AR 174,130
AR 175,131
AR 176,132
AR 177,133
AR 178,134
AR 179,135
v_mul_f32 v172, s78, v172
v_mul_f32 v173, s78, v173
v_mul_f32 v174, s78, v174
v_mul_f32 v175, s78, v175
v_mul_f32 v176, s78, v176
v_mul_f32 v177, s78, v177
v_mul_f32 v178, s78, v178
v_mul_f32 v179, s78, v179
CV 204,172,173
CV 205,174,175
CV 206,176,177
CV 207,178,179
s_mul_i32 s60, s71, 4
s_add_u32 s60, s60, s70
buffer_store_dwordx4 v[204:207], v159, s[44:47], s60 offen
AR 180,136
AR 181,137
AR 182,138
AR 183,139
AR 184,140
AR 185,141
AR 186,142
AR 187,143
v_mul_f32 v180, s78, v180
v_mul_f32 v181, s78, v181
v_mul_f32 v182, s78, v182
v_mul_f32 v183, s78, v183
v_mul_f32 v184, s78, v184
v_mul_f32 v185, s78, v185
v_mul_f32 v186, s78, v186
v_mul_f32 v187, s78, v187
CV 208,180,181
CV 209,182,183
CV 210,184,185
CV 211,186,187
buffer_store_dwordx4 v[208:211], v159, s[44:47], s60 offen offset:1024
AR 188,144
AR 189,145
AR 190,146
AR 191,147
AR 192,148
AR 193,149
AR 194,150
AR 195,151
v_mul_f32 v188, s78, v188
v_mul_f32 v189, s78, v189
v_mul_f32 v190, s78, v190
v_mul_f32 v191, s78, v191
v_mul_f32 v192, s78, v192
v_mul_f32 v193, s78, v193
v_mul_f32 v194, s78, v194
v_mul_f32 v195, s78, v195
CV 212,188,189
CV 213,190,191
CV 214,192,193
CV 215,194,195
buffer_store_dwordx4 v[212:215], v159, s[44:47], s60 offen offset:2048
AR 196,152
AR 197,153
AR 198,154
AR 199,155
AR 200,156
AR 201,157
AR 202,158
AR 203,159
v_mul_f32 v196, s78, v196
v_mul_f32 v197, s78, v197
v_mul_f32 v198, s78, v198
v_mul_f32 v199, s78, v199
v_mul_f32 v200, s78, v200
v_mul_f32 v201, s78, v201
v_mul_f32 v202, s78, v202
v_mul_f32 v203, s78, v203
CV 216,196,197
CV 217,198,199
CV 218,200,201
CV 219,202,203
buffer_store_dwordx4 v[216:219], v159, s[44:47], s60 offen offset:3072
AR 172,160
AR 173,161
AR 174,162
AR 175,163
AR 176,164
AR 177,165
AR 178,166
AR 179,167
v_mul_f32 v172, s78, v172
v_mul_f32 v173, s78, v173
v_mul_f32 v174, s78, v174
v_mul_f32 v175, s78, v175
v_mul_f32 v176, s78, v176
v_mul_f32 v177, s78, v177
v_mul_f32 v178, s78, v178
v_mul_f32 v179, s78, v179
CV 220,172,173
CV 221,174,175
CV 222,176,177
CV 223,178,179
s_mul_i32 s60, s71, 5
s_add_u32 s60, s60, s70
buffer_store_dwordx4 v[220:223], v159, s[44:47], s60 offen
AR 180,168
AR 181,169
AR 182,170
AR 183,171
AR 184,172
AR 185,173
AR 186,174
AR 187,175
v_mul_f32 v180, s78, v180
v_mul_f32 v181, s78, v181
v_mul_f32 v182, s78, v182
v_mul_f32 v183, s78, v183
v_mul_f32 v184, s78, v184
v_mul_f32 v185, s78, v185
v_mul_f32 v186, s78, v186
v_mul_f32 v187, s78, v187
CV 224,180,181
CV 225,182,183
CV 226,184,185
CV 227,186,187
buffer_store_dwordx4 v[224:227], v159, s[44:47], s60 offen offset:1024
AR 188,176
AR 189,177
AR 190,178
AR 191,179
AR 192,180
AR 193,181
AR 194,182
AR 195,183
v_mul_f32 v188, s78, v188
v_mul_f32 v189, s78, v189
v_mul_f32 v190, s78, v190
v_mul_f32 v191, s78, v191
v_mul_f32 v192, s78, v192
v_mul_f32 v193, s78, v193
v_mul_f32 v194, s78, v194
v_mul_f32 v195, s78, v195
CV 228,188,189
CV 229,190,191
CV 230,192,193
CV 231,194,195
buffer_store_dwordx4 v[228:231], v159, s[44:47], s60 offen offset:2048
AR 196,184
AR 197,185
AR 198,186
AR 199,187
AR 200,188
AR 201,189
AR 202,190
AR 203,191
v_mul_f32 v196, s78, v196
v_mul_f32 v197, s78, v197
v_mul_f32 v198, s78, v198
v_mul_f32 v199, s78, v199
v_mul_f32 v200, s78, v200
v_mul_f32 v201, s78, v201
v_mul_f32 v202, s78, v202
v_mul_f32 v203, s78, v203
CV 232,196,197
CV 233,198,199
CV 234,200,201
CV 235,202,203
buffer_store_dwordx4 v[232:235], v159, s[44:47], s60 offen offset:3072
MAavv 252,128,64
AR 172,192
AR 173,193
AR 174,194
AR 175,195
AR 176,196
AR 177,197
AR 178,198
AR 179,199
v_mul_f32 v172, s78, v172
v_mul_f32 v173, s78, v173
v_mul_f32 v174, s78, v174
v_mul_f32 v175, s78, v175
v_mul_f32 v176, s78, v176
v_mul_f32 v177, s78, v177
v_mul_f32 v178, s78, v178
v_mul_f32 v179, s78, v179
CV 204,172,173
CV 205,174,175
CV 206,176,177
CV 207,178,179
s_mul_i32 s60, s71, 6
s_add_u32 s60, s60, s70
buffer_store_dwordx4 v[204:207], v159, s[44:47], s60 offen
AR 180,200
AR 181,201
AR 182,202
AR 183,203
AR 184,204
AR 185,205
AR 186,206
AR 187,207
v_mul_f32 v180, s78, v180
v_mul_f32 v181, s78, v181
v_mul_f32 v182, s78, v182
v_mul_f32 v183, s78, v183
v_mul_f32 v184, s78, v184
v_mul_f32 v185, s78, v185
v_mul_f32 v186, s78, v186
v_mul_f32 v187, s78, v187
CV 208,180,181
CV 209,182,183
CV 210,184,185
CV 211,186,187
buffer_store_dwordx4 v[208:211], v159, s[44:47], s60 offen offset:1024
AR 188,208
AR 189,209
AR 190,210
AR 191,211
AR 192,212
AR 193,213
AR 194,214
AR 195,215
v_mul_f32 v188, s78, v188
v_mul_f32 v189, s78, v189
v_mul_f32 v190, s78, v190
v_mul_f32 v191, s78, v191
v_mul_f32 v192, s78, v192
v_mul_f32 v193, s78, v193
v_mul_f32 v194, s78, v194
v_mul_f32 v195, s78, v195
CV 212,188,189
CV 213,190,191
CV 214,192,193
CV 215,194,195
buffer_store_dwordx4 v[212:215], v159, s[44:47], s60 offen offset:2048
AR 196,216
AR 197,217
AR 198,218
AR 199,219
AR 200,220
AR 201,221
AR 202,222
AR 203,223
v_mul_f32 v196, s78, v196
v_mul_f32 v197, s78, v197
v_mul_f32 v198, s78, v198
v_mul_f32 v199, s78, v199
v_mul_f32 v200, s78, v200
v_mul_f32 v201, s78, v201
v_mul_f32 v202, s78, v202
v_mul_f32 v203, s78, v203
CV 216,196,197
CV 217,198,199
CV 218,200,201
CV 219,202,203
buffer_store_dwordx4 v[216:219], v159, s[44:47], s60 offen offset:3072
AR 172,224
AR 173,225
AR 174,226
AR 175,227
AR 176,228
AR 177,229
AR 178,230
AR 179,231
v_mul_f32 v172, s78, v172
v_mul_f32 v173, s78, v173
v_mul_f32 v174, s78, v174
v_mul_f32 v175, s78, v175
v_mul_f32 v176, s78, v176
v_mul_f32 v177, s78, v177
v_mul_f32 v178, s78, v178
v_mul_f32 v179, s78, v179
CV 220,172,173
CV 221,174,175
CV 222,176,177
CV 223,178,179
s_mul_i32 s60, s71, 7
s_add_u32 s60, s60, s70
buffer_store_dwordx4 v[220:223], v159, s[44:47], s60 offen
AR 180,232
AR 181,233
AR 182,234
AR 183,235
AR 184,236
AR 185,237
AR 186,238
AR 187,239
v_mul_f32 v180, s78, v180
v_mul_f32 v181, s78, v181
)ASM"
R"ASM(v_mul_f32 v182, s78, v182
v_mul_f32 v183, s78, v183
v_mul_f32 v184, s78, v184
v_mul_f32 v185, s78, v185
v_mul_f32 v186, s78, v186
v_mul_f32 v187, s78, v187
CV 224,180,181
CV 225,182,183
CV 226,184,185
CV 227,186,187
buffer_store_dwordx4 v[224:227], v159, s[44:47], s60 offen offset:1024
AR 188,240
AR 189,241
AR 190,242
AR 191,243
AR 192,244
AR 193,245
AR 194,246
AR 195,247
v_mul_f32 v188, s78, v188
v_mul_f32 v189, s78, v189
v_mul_f32 v190, s78, v190
v_mul_f32 v191, s78, v191
v_mul_f32 v192, s78, v192
v_mul_f32 v193, s78, v193
v_mul_f32 v194, s78, v194
v_mul_f32 v195, s78, v195
CV 228,188,189
CV 229,190,191
CV 230,192,193
CV 231,194,195
buffer_store_dwordx4 v[228:231], v159, s[44:47], s60 offen offset:2048
AR 196,248
AR 197,249
AR 198,250
AR 199,251
AR 200,252
AR 201,253
AR 202,254
AR 203,255
v_mul_f32 v196, s78, v196
v_mul_f32 v197, s78, v197
v_mul_f32 v198, s78, v198
v_mul_f32 v199, s78, v199
v_mul_f32 v200, s78, v200
v_mul_f32 v201, s78, v201
v_mul_f32 v202, s78, v202
v_mul_f32 v203, s78, v203
CV 232,196,197
CV 233,198,199
CV 234,200,201
CV 235,202,203
buffer_store_dwordx4 v[232:235], v159, s[44:47], s60 offen offset:3072
s_mov_b32 s38, 0
s_mov_b32 s42, 0
s_branch Lk1_u2
Lk1_u1K:
s_waitcnt vmcnt(16) lgkmcnt(0)
s_barrier
MZavv 0,68,4
Tv 100,132,16384
MZavv 4,72,4
Tv 102,132,18432
MZavv 8,76,4
s_add_u32 s65, s48, s49
s_add_u32 m0, s61, 4096
MZavv 12,80,4
Rv 36,141,0
MZavv 16,84,4
buffer_load_dwordx4 v143, s[24:27], s65 offen lds
MZavv 20,88,4
Tv 104,133,16384
MZavv 24,92,4
MZavv 28,96,4
Tv 106,133,18432
MZavv 32,68,8
Rv 40,141,2048
MZavv 36,72,8
MZavv 40,76,8
Tv 108,134,16384
MZavv 44,80,8
buffer_load_dwordx4 v144, s[24:27], s65 offen offset:1024 lds
MZavv 48,84,8
Tv 110,134,18432
MZavv 52,88,8
MZavv 56,92,8
Rv 44,141,4096
MZavv 60,96,8
Tv 112,135,16384
MZavv 64,68,12
MZavv 68,72,12
Tv 114,135,18432
MZavv 72,76,12
buffer_load_dwordx4 v145, s[24:27], s65 offen offset:2048 lds
MZavv 76,80,12
Rv 48,141,6144
MZavv 80,84,12
MZavv 84,88,12
Tv 116,136,16384
MZavv 88,92,12
Tv 118,136,18432
MZavv 92,96,12
MZavv 96,68,16
Rv 52,141,8192
MZavv 100,72,16
buffer_load_dwordx4 v146, s[24:27], s65 offen offset:3072 lds
MZavv 104,76,16
Tv 120,137,16384
MZavv 108,80,16
s_add_u32 s50, s50, s51
s_cmp_eq_u32 s50, s53
s_cselect_b32 s50, 0, s50
MZavv 112,84,16
Tv 122,137,18432
MZavv 116,88,16
Rv 56,141,10240
MZavv 120,92,16
s_add_u32 s62, s62, 16384
s_and_b32 s62, s62, 0xffff
s_mov_b32 m0, s62
MZavv 124,96,16
Tv 124,138,16384
MZavv 128,68,20
buffer_load_dwordx4 v147, s[28:31], s50 offen lds
MZavv 132,72,20
Tv 126,138,18432
MZavv 136,76,20
MZavv 140,80,20
Rv 60,141,12288
MZavv 144,84,20
Tv 128,139,16384
MZavv 148,88,20
MZavv 152,92,20
Tv 130,139,18432
MZavv 156,96,20
buffer_load_dwordx4 v148, s[28:31], s50 offen offset:1024 lds
MZavv 160,68,24
Rv 64,141,14336
MZavv 164,72,24
MZavv 168,76,24
MZavv 172,80,24
MZavv 176,84,24
MZavv 180,88,24
MZavv 184,92,24
buffer_load_dwordx4 v149, s[28:31], s50 offen offset:2048 lds
MZavv 188,96,24
MZavv 192,68,28
MZavv 196,72,28
MZavv 200,76,28
MZavv 204,80,28
MZavv 208,84,28
MZavv 212,88,28
buffer_load_dwordx4 v150, s[28:31], s50 offen offset:3072 lds
MZavv 216,92,28
MZavv 220,96,28
MZavv 224,68,32
MZavv 228,72,32
MZavv 232,76,32
MZavv 236,80,32
MZavv 240,84,32
MZavv 244,88,32
MZavv 248,92,32
MZavv 252,96,32
s_waitcnt vmcnt(16) lgkmcnt(0)
s_barrier
MAavv 0,100,36
s_add_u32 s63, s63, 32768
s_cmp_eq_u32 s63, 163840
s_cselect_b32 s63, 65536, s63
MAavv 4,104,36
v_add_u32 v142, s63, v140
v_xor_b32 v141, 64, v142
MAavv 8,108,36
v_xor_b32 v132, 0x8000, v132
v_xor_b32 v133, 0x8000, v133
MAavv 12,112,36
v_xor_b32 v134, 0x8000, v134
v_xor_b32 v135, 0x8000, v135
MAavv 16,116,36
v_xor_b32 v136, 0x8000, v136
v_xor_b32 v137, 0x8000, v137
MAavv 20,120,36
v_xor_b32 v138, 0x8000, v138
v_xor_b32 v139, 0x8000, v139
MAavv 24,124,36
Tv 68,132,0
MAavv 28,128,36
Tv 70,132,2048
MAavv 32,100,40
Rv 4,142,0
MAavv 36,104,40
Tv 72,133,0
MAavv 40,108,40
Tv 74,133,2048
MAavv 44,112,40
Rv 8,142,2048
MAavv 48,116,40
s_add_u32 s48, s48, 128
s_cmp_eq_u32 s48, s52
s_cselect_b32 s48, 0, s48
MAavv 52,120,40
Tv 76,134,0
MAavv 56,124,40
s_add_u32 s61, s61, 32768
s_cmp_ge_u32 s61, 163840
s_cselect_b32 s64, 98304, 0
MAavv 60,128,40
Tv 78,134,2048
s_sub_u32 s61, s61, s64
s_mov_b32 m0, s61
MAavv 64,100,44
buffer_load_dwordx4 v143, s[24:27], s48 offen lds
MAavv 68,104,44
Rv 12,142,4096
MAavv 72,108,44
Tv 80,135,0
MAavv 76,112,44
buffer_load_dwordx4 v144, s[24:27], s48 offen offset:1024 lds
MAavv 80,116,44
Tv 82,135,2048
MAavv 84,120,44
buffer_load_dwordx4 v145, s[24:27], s48 offen offset:2048 lds
MAavv 88,124,44
Rv 16,142,6144
MAavv 92,128,44
MAavv 96,100,48
Tv 84,136,0
MAavv 100,104,48
Tv 86,136,2048
MAavv 104,108,48
buffer_load_dwordx4 v146, s[24:27], s48 offen offset:3072 lds
MAavv 108,112,48
Rv 20,142,8192
MAavv 112,116,48
s_add_u32 s50, s50, s51
s_cmp_eq_u32 s50, s53
s_cselect_b32 s50, 0, s50
MAavv 116,120,48
Tv 88,137,0
MAavv 120,124,48
s_add_u32 s62, s62, 16384
s_and_b32 s62, s62, 0xffff
s_mov_b32 m0, s62
MAavv 124,128,48
Tv 90,137,2048
MAavv 128,100,52
Rv 24,142,10240
MAavv 132,104,52
buffer_load_dwordx4 v147, s[28:31], s50 offen lds
MAavv 136,108,52
Tv 92,138,0
MAavv 140,112,52
MAavv 144,116,52
Tv 94,138,2048
MAavv 148,120,52
MAavv 152,124,52
Rv 28,142,12288
MAavv 156,128,52
Tv 96,139,0
MAavv 160,100,56
buffer_load_dwordx4 v148, s[28:31], s50 offen offset:1024 lds
MAavv 164,104,56
Tv 98,139,2048
MAavv 168,108,56
MAavv 172,112,56
Rv 32,142,14336
MAavv 176,116,56
MAavv 180,120,56
MAavv 184,124,56
buffer_load_dwordx4 v149, s[28:31], s50 offen offset:2048 lds
MAavv 188,128,56
MAavv 192,100,60
MAavv 196,104,60
MAavv 200,108,60
MAavv 204,112,60
MAavv 208,116,60
MAavv 212,120,60
buffer_load_dwordx4 v150, s[28:31], s50 offen offset:3072 lds
MAavv 216,124,60
MAavv 220,128,60
MAavv 224,100,64
MAavv 228,104,64
MAavv 232,108,64
MAavv 236,112,64
MAavv 240,116,64
MAavv 244,120,64
MAavv 248,124,64
MAavv 252,128,64
s_waitcnt vmcnt(16) lgkmcnt(0)
s_barrier
MAavv 0,68,4
Tv 100,132,16384
MAavv 4,72,4
Tv 102,132,18432
MAavv 8,76,4
s_add_u32 s65, s48, s49
s_add_u32 m0, s61, 4096
MAavv 12,80,4
Rv 36,141,0
MAavv 16,84,4
buffer_load_dwordx4 v143, s[24:27], s65 offen lds
MAavv 20,88,4
Tv 104,133,16384
MAavv 24,92,4
MAavv 28,96,4
Tv 106,133,18432
MAavv 32,68,8
Rv 40,141,2048
MAavv 36,72,8
MAavv 40,76,8
Tv 108,134,16384
MAavv 44,80,8
buffer_load_dwordx4 v144, s[24:27], s65 offen offset:1024 lds
MAavv 48,84,8
Tv 110,134,18432
MAavv 52,88,8
MAavv 56,92,8
Rv 44,141,4096
MAavv 60,96,8
Tv 112,135,16384
MAavv 64,68,12
MAavv 68,72,12
Tv 114,135,18432
MAavv 72,76,12
buffer_load_dwordx4 v145, s[24:27], s65 offen offset:2048 lds
MAavv 76,80,12
Rv 48,141,6144
MAavv 80,84,12
MAavv 84,88,12
Tv 116,136,16384
MAavv 88,92,12
Tv 118,136,18432
MAavv 92,96,12
MAavv 96,68,16
Rv 52,141,8192
MAavv 100,72,16
buffer_load_dwordx4 v146, s[24:27], s65 offen offset:3072 lds
MAavv 104,76,16
Tv 120,137,16384
MAavv 108,80,16
s_add_u32 s50, s50, s51
s_cmp_eq_u32 s50, s53
s_cselect_b32 s50, 0, s50
MAavv 112,84,16
Tv 122,137,18432
MAavv 116,88,16
Rv 56,141,10240
MAavv 120,92,16
s_add_u32 s62, s62, 16384
s_and_b32 s62, s62, 0xffff
s_mov_b32 m0, s62
MAavv 124,96,16
Tv 124,138,16384
MAavv 128,68,20
buffer_load_dwordx4 v147, s[28:31], s50 offen lds
MAavv 132,72,20
Tv 126,138,18432
MAavv 136,76,20
MAavv 140,80,20
Rv 60,141,12288
MAavv 144,84,20
Tv 128,139,16384
MAavv 148,88,20
MAavv 152,92,20
Tv 130,139,18432
MAavv 156,96,20
buffer_load_dwordx4 v148, s[28:31], s50 offen offset:1024 lds
MAavv 160,68,24
Rv 64,141,14336
MAavv 164,72,24
MAavv 168,76,24
MAavv 172,80,24
MAavv 176,84,24
MAavv 180,88,24
MAavv 184,92,24
buffer_load_dwordx4 v149, s[28:31], s50 offen offset:2048 lds
MAavv 188,96,24
MAavv 192,68,28
MAavv 196,72,28
MAavv 200,76,28
MAavv 204,80,28
MAavv 208,84,28
MAavv 212,88,28
buffer_load_dwordx4 v150, s[28:31], s50 offen offset:3072 lds
MAavv 216,92,28
MAavv 220,96,28
MAavv 224,68,32
MAavv 228,72,32
MAavv 232,76,32
MAavv 236,80,32
MAavv 240,84,32
MAavv 244,88,32
MAavv 248,92,32
MAavv 252,96,32
s_waitcnt vmcnt(16) lgkmcnt(0)
s_barrier
MAavv 0,100,36
s_add_u32 s63, s63, 32768
s_cmp_eq_u32 s63, 163840
s_cselect_b32 s63, 65536, s63
MAavv 4,104,36
v_add_u32 v142, s63, v140
v_xor_b32 v141, 64, v142
MAavv 8,108,36
v_xor_b32 v132, 0x8000, v132
v_xor_b32 v133, 0x8000, v133
MAavv 12,112,36
v_xor_b32 v134, 0x8000, v134
v_xor_b32 v135, 0x8000, v135
MAavv 16,116,36
v_xor_b32 v136, 0x8000, v136
v_xor_b32 v137, 0x8000, v137
MAavv 20,120,36
v_xor_b32 v138, 0x8000, v138
v_xor_b32 v139, 0x8000, v139
MAavv 24,124,36
Tv 68,132,0
MAavv 28,128,36
Tv 70,132,2048
MAavv 32,100,40
Rv 4,142,0
MAavv 36,104,40
Tv 72,133,0
MAavv 40,108,40
Tv 74,133,2048
MAavv 44,112,40
Rv 8,142,2048
MAavv 48,116,40
s_add_u32 s48, s48, 128
s_cmp_eq_u32 s48, s52
s_cselect_b32 s48, 0, s48
MAavv 52,120,40
Tv 76,134,0
MAavv 56,124,40
s_add_u32 s61, s61, 32768
s_cmp_ge_u32 s61, 163840
s_cselect_b32 s64, 98304, 0
MAavv 60,128,40
Tv 78,134,2048
s_sub_u32 s61, s61, s64
s_mov_b32 m0, s61
MAavv 64,100,44
buffer_load_dwordx4 v143, s[24:27], s48 offen lds
MAavv 68,104,44
Rv 12,142,4096
MAavv 72,108,44
Tv 80,135,0
MAavv 76,112,44
buffer_load_dwordx4 v144, s[24:27], s48 offen offset:1024 lds
MAavv 80,116,44
Tv 82,135,2048
MAavv 84,120,44
buffer_load_dwordx4 v145, s[24:27], s48 offen offset:2048 lds
MAavv 88,124,44
Rv 16,142,6144
MAavv 92,128,44
MAavv 96,100,48
Tv 84,136,0
MAavv 100,104,48
Tv 86,136,2048
MAavv 104,108,48
buffer_load_dwordx4 v146, s[24:27], s48 offen offset:3072 lds
MAavv 108,112,48
Rv 20,142,8192
MAavv 112,116,48
s_add_u32 s50, s50, s51
s_cmp_eq_u32 s50, s53
s_cselect_b32 s50, 0, s50
MAavv 116,120,48
Tv 88,137,0
MAavv 120,124,48
s_add_u32 s62, s62, 16384
s_and_b32 s62, s62, 0xffff
s_mov_b32 m0, s62
MAavv 124,128,48
Tv 90,137,2048
MAavv 128,100,52
Rv 24,142,10240
MAavv 132,104,52
buffer_load_dwordx4 v147, s[28:31], s50 offen lds
MAavv 136,108,52
Tv 92,138,0
MAavv 140,112,52
MAavv 144,116,52
Tv 94,138,2048
MAavv 148,120,52
MAavv 152,124,52
Rv 28,142,12288
MAavv 156,128,52
Tv 96,139,0
MAavv 160,100,56
buffer_load_dwordx4 v148, s[28:31], s50 offen offset:1024 lds
MAavv 164,104,56
Tv 98,139,2048
MAavv 168,108,56
MAavv 172,112,56
Rv 32,142,14336
MAavv 176,116,56
MAavv 180,120,56
MAavv 184,124,56
buffer_load_dwordx4 v149, s[28:31], s50 offen offset:2048 lds
MAavv 188,128,56
MAavv 192,100,60
MAavv 196,104,60
MAavv 200,108,60
MAavv 204,112,60
MAavv 208,116,60
MAavv 212,120,60
buffer_load_dwordx4 v150, s[28:31], s50 offen offset:3072 lds
MAavv 216,124,60
MAavv 220,128,60
MAavv 224,100,64
MAavv 228,104,64
MAavv 232,108,64
MAavv 236,112,64
MAavv 240,116,64
MAavv 244,120,64
MAavv 248,124,64
MAavv 252,128,64
s_waitcnt vmcnt(16) lgkmcnt(0)
s_barrier
MAavv 0,68,4
Tv 100,132,16384
MAavv 4,72,4
Tv 102,132,18432
MAavv 8,76,4
s_add_u32 s65, s48, s49
s_add_u32 m0, s61, 4096
MAavv 12,80,4
Rv 36,141,0
MAavv 16,84,4
buffer_load_dwordx4 v143, s[24:27], s65 offen lds
MAavv 20,88,4
Tv 104,133,16384
MAavv 24,92,4
MAavv 28,96,4
Tv 106,133,18432
MAavv 32,68,8
Rv 40,141,2048
MAavv 36,72,8
MAavv 40,76,8
Tv 108,134,16384
MAavv 44,80,8
buffer_load_dwordx4 v144, s[24:27], s65 offen offset:1024 lds
MAavv 48,84,8
Tv 110,134,18432
MAavv 52,88,8
MAavv 56,92,8
Rv 44,141,4096
MAavv 60,96,8
Tv 112,135,16384
MAavv 64,68,12
MAavv 68,72,12
Tv 114,135,18432
MAavv 72,76,12
buffer_load_dwordx4 v145, s[24:27], s65 offen offset:2048 lds
MAavv 76,80,12
Rv 48,141,6144
MAavv 80,84,12
MAavv 84,88,12
Tv 116,136,16384
MAavv 88,92,12
Tv 118,136,18432
MAavv 92,96,12
MAavv 96,68,16
Rv 52,141,8192
MAavv 100,72,16
buffer_load_dwordx4 v146, s[24:27], s65 offen offset:3072 lds
MAavv 104,76,16
Tv 120,137,16384
MAavv 108,80,16
s_add_u32 s50, s50, s51
s_cmp_eq_u32 s50, s53
s_cselect_b32 s50, 0, s50
MAavv 112,84,16
Tv 122,137,18432
MAavv 116,88,16
Rv 56,141,10240
MAavv 120,92,16
s_add_u32 s62, s62, 16384
s_and_b32 s62, s62, 0xffff
s_mov_b32 m0, s62
MAavv 124,96,16
Tv 124,138,16384
MAavv 128,68,20
buffer_load_dwordx4 v147, s[28:31], s50 offen lds
MAavv 132,72,20
Tv 126,138,18432
MAavv 136,76,20
MAavv 140,80,20
Rv 60,141,12288
MAavv 144,84,20
Tv 128,139,16384
MAavv 148,88,20
MAavv 152,92,20
Tv 130,139,18432
MAavv 156,96,20
buffer_load_dwordx4 v148, s[28:31], s50 offen offset:1024 lds
MAavv 160,68,24
Rv 64,141,14336
MAavv 164,72,24
MAavv 168,76,24
MAavv 172,80,24
MAavv 176,84,24
MAavv 180,88,24
MAavv 184,92,24
buffer_load_dwordx4 v149, s[28:31], s50 offen offset:2048 lds
MAavv 188,96,24
MAavv 192,68,28
MAavv 196,72,28
MAavv 200,76,28
MAavv 204,80,28
MAavv 208,84,28
MAavv 212,88,28
buffer_load_dwordx4 v150, s[28:31], s50 offen offset:3072 lds
MAavv 216,92,28
MAavv 220,96,28
MAavv 224,68,32
MAavv 228,72,32
MAavv 232,76,32
MAavv 236,80,32
MAavv 240,84,32
MAavv 244,88,32
MAavv 248,92,32
MAavv 252,96,32
s_waitcnt vmcnt(16) lgkmcnt(0)
s_barrier
MAavv 0,100,36
s_add_u32 s63, s63, 32768
s_cmp_eq_u32 s63, 163840
s_cselect_b32 s63, 65536, s63
MAavv 4,104,36
v_add_u32 v142, s63, v140
v_xor_b32 v141, 64, v142
MAavv 8,108,36
v_xor_b32 v132, 0x8000, v132
v_xor_b32 v133, 0x8000, v133
MAavv 12,112,36
v_xor_b32 v134, 0x8000, v134
v_xor_b32 v135, 0x8000, v135
MAavv 16,116,36
v_xor_b32 v136, 0x8000, v136
v_xor_b32 v137, 0x8000, v137
MAavv 20,120,36
v_xor_b32 v138, 0x8000, v138
v_xor_b32 v139, 0x8000, v139
MAavv 24,124,36
Tv 68,132,0
MAavv 28,128,36
Tv 70,132,2048
MAavv 32,100,40
Rv 4,142,0
MAavv 36,104,40
Tv 72,133,0
MAavv 40,108,40
Tv 74,133,2048
MAavv 44,112,40
Rv 8,142,2048
MAavv 48,116,40
s_add_u32 s48, s48, 128
s_cmp_eq_u32 s48, s52
s_cselect_b32 s48, 0, s48
MAavv 52,120,40
Tv 76,134,0
MAavv 56,124,40
s_add_u32 s61, s61, 32768
s_cmp_ge_u32 s61, 163840
s_cselect_b32 s64, 98304, 0
MAavv 60,128,40
Tv 78,134,2048
s_sub_u32 s61, s61, s64
s_mov_b32 m0, s61
MAavv 64,100,44
buffer_load_dwordx4 v143, s[24:27], s48 offen lds
MAavv 68,104,44
Rv 12,142,4096
MAavv 72,108,44
Tv 80,135,0
MAavv 76,112,44
buffer_load_dwordx4 v144, s[24:27], s48 offen offset:1024 lds
MAavv 80,116,44
Tv 82,135,2048
MAavv 84,120,44
buffer_load_dwordx4 v145, s[24:27], s48 offen offset:2048 lds
MAavv 88,124,44
Rv 16,142,6144
MAavv 92,128,44
MAavv 96,100,48
Tv 84,136,0
MAavv 100,104,48
Tv 86,136,2048
MAavv 104,108,48
buffer_load_dwordx4 v146, s[24:27], s48 offen offset:3072 lds
MAavv 108,112,48
Rv 20,142,8192
MAavv 112,116,48
s_add_u32 s50, s50, s51
s_cmp_eq_u32 s50, s53
s_cselect_b32 s50, 0, s50
MAavv 116,120,48
Tv 88,137,0
MAavv 120,124,48
s_add_u32 s62, s62, 16384
s_and_b32 s62, s62, 0xffff
s_mov_b32 m0, s62
MAavv 124,128,48
Tv 90,137,2048
MAavv 128,100,52
Rv 24,142,10240
MAavv 132,104,52
buffer_load_dwordx4 v147, s[28:31], s50 offen lds
MAavv 136,108,52
Tv 92,138,0
MAavv 140,112,52
MAavv 144,116,52
Tv 94,138,2048
MAavv 148,120,52
MAavv 152,124,52
Rv 28,142,12288
MAavv 156,128,52
Tv 96,139,0
MAavv 160,100,56
buffer_load_dwordx4 v148, s[28:31], s50 offen offset:1024 lds
MAavv 164,104,56
Tv 98,139,2048
MAavv 168,108,56
MAavv 172,112,56
Rv 32,142,14336
MAavv 176,116,56
MAavv 180,120,56
MAavv 184,124,56
buffer_load_dwordx4 v149, s[28:31], s50 offen offset:2048 lds
MAavv 188,128,56
MAavv 192,100,60
MAavv 196,104,60
MAavv 200,108,60
MAavv 204,112,60
MAavv 208,116,60
MAavv 212,120,60
buffer_load_dwordx4 v150, s[28:31], s50 offen offset:3072 lds
MAavv 216,124,60
MAavv 220,128,60
MAavv 224,100,64
MAavv 228,104,64
MAavv 232,108,64
MAavv 236,112,64
MAavv 240,116,64
MAavv 244,120,64
MAavv 248,124,64
MAavv 252,128,64
s_waitcnt vmcnt(16) lgkmcnt(0)
s_barrier
MAavv 0,68,4
Tv 100,132,16384
MAavv 4,72,4
Tv 102,132,18432
MAavv 8,76,4
s_add_u32 s65, s48, s49
s_add_u32 m0, s61, 4096
MAavv 12,80,4
Rv 36,141,0
MAavv 16,84,4
buffer_load_dwordx4 v143, s[24:27], s65 offen lds
MAavv 20,88,4
Tv 104,133,16384
MAavv 24,92,4
MAavv 28,96,4
Tv 106,133,18432
MAavv 32,68,8
Rv 40,141,2048
MAavv 36,72,8
MAavv 40,76,8
Tv 108,134,16384
MAavv 44,80,8
buffer_load_dwordx4 v144, s[24:27], s65 offen offset:1024 lds
MAavv 48,84,8
Tv 110,134,18432
MAavv 52,88,8
MAavv 56,92,8
Rv 44,141,4096
MAavv 60,96,8
Tv 112,135,16384
MAavv 64,68,12
MAavv 68,72,12
Tv 114,135,18432
MAavv 72,76,12
buffer_load_dwordx4 v145, s[24:27], s65 offen offset:2048 lds
MAavv 76,80,12
Rv 48,141,6144
MAavv 80,84,12
MAavv 84,88,12
Tv 116,136,16384
MAavv 88,92,12
Tv 118,136,18432
MAavv 92,96,12
MAavv 96,68,16
Rv 52,141,8192
MAavv 100,72,16
buffer_load_dwordx4 v146, s[24:27], s65 offen offset:3072 lds
MAavv 104,76,16
Tv 120,137,16384
MAavv 108,80,16
s_add_u32 s50, s50, s51
s_cmp_eq_u32 s50, s53
s_cselect_b32 s50, 0, s50
MAavv 112,84,16
Tv 122,137,18432
MAavv 116,88,16
Rv 56,141,10240
MAavv 120,92,16
s_add_u32 s62, s62, 16384
s_and_b32 s62, s62, 0xffff
s_mov_b32 m0, s62
MAavv 124,96,16
Tv 124,138,16384
MAavv 128,68,20
buffer_load_dwordx4 v147, s[28:31], s50 offen lds
MAavv 132,72,20
Tv 126,138,18432
MAavv 136,76,20
MAavv 140,80,20
Rv 60,141,12288
MAavv 144,84,20
Tv 128,139,16384
MAavv 148,88,20
MAavv 152,92,20
Tv 130,139,18432
MAavv 156,96,20
buffer_load_dwordx4 v148, s[28:31], s50 offen offset:1024 lds
MAavv 160,68,24
Rv 64,141,14336
MAavv 164,72,24
MAavv 168,76,24
MAavv 172,80,24
MAavv 176,84,24
MAavv 180,88,24
MAavv 184,92,24
buffer_load_dwordx4 v149, s[28:31], s50 offen offset:2048 lds
MAavv 188,96,24
MAavv 192,68,28
MAavv 196,72,28
MAavv 200,76,28
MAavv 204,80,28
MAavv 208,84,28
MAavv 212,88,28
buffer_load_dwordx4 v150, s[28:31], s50 offen offset:3072 lds
MAavv 216,92,28
MAavv 220,96,28
MAavv 224,68,32
MAavv 228,72,32
MAavv 232,76,32
MAavv 236,80,32
MAavv 240,84,32
MAavv 244,88,32
MAavv 248,92,32
MAavv 252,96,32
s_waitcnt vmcnt(16) lgkmcnt(0)
s_barrier
MAavv 0,100,36
s_add_u32 s63, s63, 32768
s_cmp_eq_u32 s63, 163840
s_cselect_b32 s63, 65536, s63
MAavv 4,104,36
v_add_u32 v142, s63, v140
v_xor_b32 v141, 64, v142
MAavv 8,108,36
v_xor_b32 v132, 0x8000, v132
v_xor_b32 v133, 0x8000, v133
MAavv 12,112,36
v_xor_b32 v134, 0x8000, v134
v_xor_b32 v135, 0x8000, v135
MAavv 16,116,36
v_xor_b32 v136, 0x8000, v136
v_xor_b32 v137, 0x8000, v137
MAavv 20,120,36
v_xor_b32 v138, 0x8000, v138
v_xor_b32 v139, 0x8000, v139
MAavv 24,124,36
Tv 68,132,0
MAavv 28,128,36
Tv 70,132,2048
MAavv 32,100,40
Rv 4,142,0
MAavv 36,104,40
Tv 72,133,0
MAavv 40,108,40
Tv 74,133,2048
MAavv 44,112,40
Rv 8,142,2048
MAavv 48,116,40
s_add_u32 s48, s48, 128
s_cmp_eq_u32 s48, s52
s_cselect_b32 s48, 0, s48
MAavv 52,120,40
Tv 76,134,0
MAavv 56,124,40
s_add_u32 s61, s61, 32768
s_cmp_ge_u32 s61, 163840
s_cselect_b32 s64, 98304, 0
MAavv 60,128,40
Tv 78,134,2048
s_sub_u32 s61, s61, s64
s_mov_b32 m0, s61
MAavv 64,100,44
buffer_load_dwordx4 v143, s[24:27], s48 offen lds
MAavv 68,104,44
Rv 12,142,4096
MAavv 72,108,44
Tv 80,135,0
MAavv 76,112,44
buffer_load_dwordx4 v144, s[24:27], s48 offen offset:1024 lds
MAavv 80,116,44
Tv 82,135,2048
MAavv 84,120,44
buffer_load_dwordx4 v145, s[24:27], s48 offen offset:2048 lds
MAavv 88,124,44
Rv 16,142,6144
MAavv 92,128,44
MAavv 96,100,48
Tv 84,136,0
MAavv 100,104,48
Tv 86,136,2048
MAavv 104,108,48
buffer_load_dwordx4 v146, s[24:27], s48 offen offset:3072 lds
MAavv 108,112,48
Rv 20,142,8192
MAavv 112,116,48
s_add_u32 s50, s50, s51
s_cmp_eq_u32 s50, s53
s_cselect_b32 s50, 0, s50
MAavv 116,120,48
Tv 88,137,0
MAavv 120,124,48
s_add_u32 s62, s62, 16384
s_and_b32 s62, s62, 0xffff
s_mov_b32 m0, s62
MAavv 124,128,48
Tv 90,137,2048
MAavv 128,100,52
Rv 24,142,10240
MAavv 132,104,52
buffer_load_dwordx4 v147, s[28:31], s50 offen lds
MAavv 136,108,52
Tv 92,138,0
MAavv 140,112,52
MAavv 144,116,52
Tv 94,138,2048
MAavv 148,120,52
MAavv 152,124,52
Rv 28,142,12288
MAavv 156,128,52
Tv 96,139,0
MAavv 160,100,56
buffer_load_dwordx4 v148, s[28:31], s50 offen offset:1024 lds
MAavv 164,104,56
Tv 98,139,2048
MAavv 168,108,56
MAavv 172,112,56
Rv 32,142,14336
MAavv 176,116,56
MAavv 180,120,56
MAavv 184,124,56
buffer_load_dwordx4 v149, s[28:31], s50 offen offset:2048 lds
MAavv 188,128,56
MAavv 192,100,60
MAavv 196,104,60
MAavv 200,108,60
MAavv 204,112,60
MAavv 208,116,60
MAavv 212,120,60
buffer_load_dwordx4 v150, s[28:31], s50 offen offset:3072 lds
MAavv 216,124,60
MAavv 220,128,60
MAavv 224,100,64
MAavv 228,104,64
MAavv 232,108,64
MAavv 236,112,64
MAavv 240,116,64
MAavv 244,120,64
MAavv 248,124,64
MAavv 252,128,64
s_waitcnt vmcnt(16) lgkmcnt(0)
s_barrier
MAavv 0,68,4
Tv 100,132,16384
MAavv 4,72,4
Tv 102,132,18432
MAavv 8,76,4
s_add_u32 s65, s48, s49
s_add_u32 m0, s61, 4096
MAavv 12,80,4
Rv 36,141,0
MAavv 16,84,4
buffer_load_dwordx4 v143, s[24:27], s65 offen lds
MAavv 20,88,4
Tv 104,133,16384
MAavv 24,92,4
MAavv 28,96,4
Tv 106,133,18432
MAavv 32,68,8
Rv 40,141,2048
MAavv 36,72,8
MAavv 40,76,8
Tv 108,134,16384
MAavv 44,80,8
buffer_load_dwordx4 v144, s[24:27], s65 offen offset:1024 lds
MAavv 48,84,8
Tv 110,134,18432
MAavv 52,88,8
MAavv 56,92,8
Rv 44,141,4096
MAavv 60,96,8
Tv 112,135,16384
MAavv 64,68,12
MAavv 68,72,12
Tv 114,135,18432
MAavv 72,76,12
buffer_load_dwordx4 v145, s[24:27], s65 offen offset:2048 lds
MAavv 76,80,12
Rv 48,141,6144
MAavv 80,84,12
MAavv 84,88,12
Tv 116,136,16384
MAavv 88,92,12
Tv 118,136,18432
MAavv 92,96,12
MAavv 96,68,16
Rv 52,141,8192
MAavv 100,72,16
buffer_load_dwordx4 v146, s[24:27], s65 offen offset:3072 lds
MAavv 104,76,16
Tv 120,137,16384
MAavv 108,80,16
s_add_u32 s50, s50, s51
s_cmp_eq_u32 s50, s53
s_cselect_b32 s50, 0, s50
MAavv 112,84,16
Tv 122,137,18432
MAavv 116,88,16
Rv 56,141,10240
MAavv 120,92,16
s_add_u32 s62, s62, 16384
s_and_b32 s62, s62, 0xffff
s_mov_b32 m0, s62
MAavv 124,96,16
Tv 124,138,16384
MAavv 128,68,20
buffer_load_dwordx4 v147, s[28:31], s50 offen lds
MAavv 132,72,20
Tv 126,138,18432
MAavv 136,76,20
MAavv 140,80,20
Rv 60,141,12288
MAavv 144,84,20
Tv 128,139,16384
MAavv 148,88,20
MAavv 152,92,20
Tv 130,139,18432
MAavv 156,96,20
buffer_load_dwordx4 v148, s[28:31], s50 offen offset:1024 lds
MAavv 160,68,24
Rv 64,141,14336
MAavv 164,72,24
MAavv 168,76,24
MAavv 172,80,24
MAavv 176,84,24
MAavv 180,88,24
MAavv 184,92,24
buffer_load_dwordx4 v149, s[28:31], s50 offen offset:2048 lds
MAavv 188,96,24
MAavv 192,68,28
MAavv 196,72,28
MAavv 200,76,28
MAavv 204,80,28
MAavv 208,84,28
MAavv 212,88,28
buffer_load_dwordx4 v150, s[28:31], s50 offen offset:3072 lds
MAavv 216,92,28
MAavv 220,96,28
MAavv 224,68,32
MAavv 228,72,32
MAavv 232,76,32
MAavv 236,80,32
MAavv 240,84,32
MAavv 244,88,32
MAavv 248,92,32
MAavv 252,96,32
s_waitcnt vmcnt(16) lgkmcnt(0)
s_barrier
MAavv 0,100,36
s_add_u32 s63, s63, 32768
s_cmp_eq_u32 s63, 163840
s_cselect_b32 s63, 65536, s63
MAavv 4,104,36
v_add_u32 v142, s63, v140
v_xor_b32 v141, 64, v142
MAavv 8,108,36
v_xor_b32 v132, 0x8000, v132
v_xor_b32 v133, 0x8000, v133
MAavv 12,112,36
v_xor_b32 v134, 0x8000, v134
v_xor_b32 v135, 0x8000, v135
MAavv 16,116,36
v_xor_b32 v136, 0x8000, v136
v_xor_b32 v137, 0x8000, v137
MAavv 20,120,36
v_xor_b32 v138, 0x8000, v138
v_xor_b32 v139, 0x8000, v139
MAavv 24,124,36
Tv 68,132,0
MAavv 28,128,36
Tv 70,132,2048
MAavv 32,100,40
Rv 4,142,0
MAavv 36,104,40
Tv 72,133,0
MAavv 40,108,40
Tv 74,133,2048
MAavv 44,112,40
Rv 8,142,2048
MAavv 48,116,40
s_mov_b64 s[24:25], s[36:37]
s_mov_b64 s[26:27], s[38:39]
s_mov_b32 s48, s58
MAavv 52,120,40
Tv 76,134,0
s_mov_b32 s49, s54
s_mov_b32 s52, s56
MAavv 56,124,40
v_mov_b32 v143, v151
v_mov_b32 v144, v152
MAavv 60,128,40
Tv 78,134,2048
v_mov_b32 v145, v153
MAavv 64,100,44
v_mov_b32 v146, v154
MAavv 68,104,44
Rv 12,142,4096
MAavv 72,108,44
Tv 80,135,0
MAavv 76,112,44
s_add_u32 s61, s61, 32768
s_cmp_ge_u32 s61, 163840
s_cselect_b32 s64, 98304, 0
MAavv 80,116,44
Tv 82,135,2048
s_sub_u32 s61, s61, s64
s_mov_b32 m0, s61
MAavv 84,120,44
buffer_load_dwordx4 v143, s[24:27], s48 offen lds
MAavv 88,124,44
Rv 16,142,6144
MAavv 92,128,44
buffer_load_dwordx4 v144, s[24:27], s48 offen offset:1024 lds
MAavv 96,100,48
Tv 84,136,0
MAavv 100,104,48
Tv 86,136,2048
MAavv 104,108,48
buffer_load_dwordx4 v145, s[24:27], s48 offen offset:2048 lds
MAavv 108,112,48
Rv 20,142,8192
MAavv 112,116,48
buffer_load_dwordx4 v146, s[24:27], s48 offen offset:3072 lds
MAavv 116,120,48
Tv 88,137,0
MAavv 120,124,48
s_add_u32 s50, s50, s51
s_cmp_eq_u32 s50, s53
s_cselect_b32 s50, 0, s50
MAavv 124,128,48
Tv 90,137,2048
MAavv 128,100,52
Rv 24,142,10240
MAavv 132,104,52
s_add_u32 s62, s62, 16384
s_and_b32 s62, s62, 0xffff
s_mov_b32 m0, s62
MAavv 136,108,52
Tv 92,138,0
MAavv 140,112,52
buffer_load_dwordx4 v147, s[28:31], s50 offen lds
MAavv 144,116,52
Tv 94,138,2048
MAavv 148,120,52
MAavv 152,124,52
Rv 28,142,12288
MAavv 156,128,52
Tv 96,139,0
MAavv 160,100,56
buffer_load_dwordx4 v148, s[28:31], s50 offen offset:1024 lds
MAavv 164,104,56
Tv 98,139,2048
MAavv 168,108,56
MAavv 172,112,56
Rv 32,142,14336
MAavv 176,116,56
MAavv 180,120,56
MAavv 184,124,56
buffer_load_dwordx4 v149, s[28:31], s50 offen offset:2048 lds
MAavv 188,128,56
MAavv 192,100,60
MAavv 196,104,60
MAavv 200,108,60
MAavv 204,112,60
MAavv 208,116,60
MAavv 212,120,60
buffer_load_dwordx4 v150, s[28:31], s50 offen offset:3072 lds
MAavv 216,124,60
MAavv 220,128,60
MAavv 224,100,64
MAavv 228,104,64
MAavv 232,108,64
MAavv 236,112,64
MAavv 240,116,64
MAavv 244,120,64
MAavv 248,124,64
MAavv 252,128,64
s_waitcnt vmcnt(16) lgkmcnt(0)
s_barrier
MAavv 0,68,4
Tv 100,132,16384
MAavv 4,72,4
Tv 102,132,18432
MAavv 8,76,4
s_add_u32 s65, s48, s49
s_add_u32 m0, s61, 4096
MAavv 12,80,4
Rv 36,141,0
MAavv 16,84,4
buffer_load_dwordx4 v143, s[24:27], s65 offen lds
MAavv 20,88,4
Tv 104,133,16384
MAavv 24,92,4
MAavv 28,96,4
Tv 106,133,18432
MAavv 32,68,8
Rv 40,141,2048
MAavv 36,72,8
MAavv 40,76,8
Tv 108,134,16384
MAavv 44,80,8
buffer_load_dwordx4 v144, s[24:27], s65 offen offset:1024 lds
MAavv 48,84,8
Tv 110,134,18432
MAavv 52,88,8
MAavv 56,92,8
Rv 44,141,4096
MAavv 60,96,8
Tv 112,135,16384
MAavv 64,68,12
MAavv 68,72,12
Tv 114,135,18432
MAavv 72,76,12
buffer_load_dwordx4 v145, s[24:27], s65 offen offset:2048 lds
MAavv 76,80,12
Rv 48,141,6144
MAavv 80,84,12
MAavv 84,88,12
Tv 116,136,16384
MAavv 88,92,12
Tv 118,136,18432
MAavv 92,96,12
MAavv 96,68,16
Rv 52,141,8192
MAavv 100,72,16
buffer_load_dwordx4 v146, s[24:27], s65 offen offset:3072 lds
MAavv 104,76,16
Tv 120,137,16384
MAavv 108,80,16
s_mov_b64 s[28:29], s[40:41]
s_mov_b64 s[30:31], s[42:43]
s_mov_b32 s50, s59
MAavv 112,84,16
Tv 122,137,18432
s_mov_b32 s51, s55
s_mov_b32 s53, s57
MAavv 116,88,16
Rv 56,141,10240
v_mov_b32 v147, v155
MAavv 120,92,16
v_mov_b32 v148, v156
v_mov_b32 v149, v157
MAavv 124,96,16
Tv 124,138,16384
v_mov_b32 v150, v158
MAavv 128,68,20
s_add_u32 s62, s62, 16384
s_and_b32 s62, s62, 0xffff
s_mov_b32 m0, s62
MAavv 132,72,20
Tv 126,138,18432
MAavv 136,76,20
buffer_load_dwordx4 v147, s[28:31], s50 offen lds
MAavv 140,80,20
Rv 60,141,12288
MAavv 144,84,20
Tv 128,139,16384
MAavv 148,88,20
MAavv 152,92,20
Tv 130,139,18432
MAavv 156,96,20
buffer_load_dwordx4 v148, s[28:31], s50 offen offset:1024 lds
MAavv 160,68,24
Rv 64,141,14336
MAavv 164,72,24
MAavv 168,76,24
MAavv 172,80,24
MAavv 176,84,24
MAavv 180,88,24
MAavv 184,92,24
buffer_load_dwordx4 v149, s[28:31], s50 offen offset:2048 lds
MAavv 188,96,24
MAavv 192,68,28
MAavv 196,72,28
MAavv 200,76,28
MAavv 204,80,28
MAavv 208,84,28
MAavv 212,88,28
buffer_load_dwordx4 v150, s[28:31], s50 offen offset:3072 lds
MAavv 216,92,28
MAavv 220,96,28
MAavv 224,68,32
MAavv 228,72,32
MAavv 232,76,32
MAavv 236,80,32
MAavv 240,84,32
MAavv 244,88,32
MAavv 248,92,32
MAavv 252,96,32
s_waitcnt vmcnt(16) lgkmcnt(0)
s_barrier
MAavv 0,100,36
s_add_u32 s63, s63, 32768
s_cmp_eq_u32 s63, 163840
s_cselect_b32 s63, 65536, s63
MAavv 4,104,36
v_add_u32 v142, s63, v140
v_xor_b32 v141, 64, v142
MAavv 8,108,36
v_xor_b32 v132, 0x8000, v132
v_xor_b32 v133, 0x8000, v133
MAavv 12,112,36
v_xor_b32 v134, 0x8000, v134
v_xor_b32 v135, 0x8000, v135
MAavv 16,116,36
v_xor_b32 v136, 0x8000, v136
v_xor_b32 v137, 0x8000, v137
MAavv 20,120,36
v_xor_b32 v138, 0x8000, v138
v_xor_b32 v139, 0x8000, v139
MAavv 24,124,36
Tv 68,132,0
MAavv 28,128,36
Tv 70,132,2048
MAavv 32,100,40
Rv 4,142,0
MAavv 36,104,40
Tv 72,133,0
MAavv 40,108,40
Tv 74,133,2048
MAavv 44,112,40
Rv 8,142,2048
MAavv 48,116,40
s_add_u32 s48, s48, 128
s_cmp_eq_u32 s48, s52
s_cselect_b32 s48, 0, s48
MAavv 52,120,40
Tv 76,134,0
MAavv 56,124,40
s_add_u32 s61, s61, 32768
s_cmp_ge_u32 s61, 163840
s_cselect_b32 s64, 98304, 0
MAavv 60,128,40
Tv 78,134,2048
s_sub_u32 s61, s61, s64
s_mov_b32 m0, s61
MAavv 64,100,44
buffer_load_dwordx4 v143, s[24:27], s48 offen lds
MAavv 68,104,44
Rv 12,142,4096
MAavv 72,108,44
Tv 80,135,0
MAavv 76,112,44
buffer_load_dwordx4 v144, s[24:27], s48 offen offset:1024 lds
MAavv 80,116,44
Tv 82,135,2048
MAavv 84,120,44
buffer_load_dwordx4 v145, s[24:27], s48 offen offset:2048 lds
MAavv 88,124,44
Rv 16,142,6144
MAavv 92,128,44
MAavv 96,100,48
Tv 84,136,0
MAavv 100,104,48
Tv 86,136,2048
MAavv 104,108,48
buffer_load_dwordx4 v146, s[24:27], s48 offen offset:3072 lds
MAavv 108,112,48
Rv 20,142,8192
MAavv 112,116,48
s_add_u32 s50, s50, s51
s_cmp_eq_u32 s50, s53
s_cselect_b32 s50, 0, s50
MAavv 116,120,48
Tv 88,137,0
MAavv 120,124,48
s_add_u32 s62, s62, 16384
s_and_b32 s62, s62, 0xffff
s_mov_b32 m0, s62
MAavv 124,128,48
Tv 90,137,2048
MAavv 128,100,52
Rv 24,142,10240
MAavv 132,104,52
buffer_load_dwordx4 v147, s[28:31], s50 offen lds
MAavv 136,108,52
Tv 92,138,0
MAavv 140,112,52
MAavv 144,116,52
Tv 94,138,2048
MAavv 148,120,52
MAavv 152,124,52
Rv 28,142,12288
MAavv 156,128,52
Tv 96,139,0
MAavv 160,100,56
buffer_load_dwordx4 v148, s[28:31], s50 offen offset:1024 lds
MAavv 164,104,56
Tv 98,139,2048
MAavv 168,108,56
MAavv 172,112,56
Rv 32,142,14336
MAavv 176,116,56
MAavv 180,120,56
MAavv 184,124,56
buffer_load_dwordx4 v149, s[28:31], s50 offen offset:2048 lds
MAavv 188,128,56
MAavv 192,100,60
MAavv 196,104,60
MAavv 200,108,60
MAavv 204,112,60
MAavv 208,116,60
MAavv 212,120,60
buffer_load_dwordx4 v150, s[28:31], s50 offen offset:3072 lds
MAavv 216,124,60
MAavv 220,128,60
MAavv 224,100,64
MAavv 228,104,64
MAavv 232,108,64
MAavv 236,112,64
MAavv 240,116,64
MAavv 244,120,64
MAavv 248,124,64
MAavv 252,128,64
s_waitcnt vmcnt(16) lgkmcnt(0)
s_barrier
MAavv 0,68,4
Tv 100,132,16384
MAavv 4,72,4
Tv 102,132,18432
MAavv 8,76,4
Rv 36,141,0
s_add_u32 s65, s48, s49
s_add_u32 m0, s61, 4096
MAavv 12,80,4
Tv 104,133,16384
MAavv 16,84,4
Tv 106,133,18432
MAavv 20,88,4
Rv 40,141,2048
MAavv 24,92,4
Tv 108,134,16384
MAavv 28,96,4
Tv 110,134,18432
MAavv 32,68,8
Rv 44,141,4096
MAavv 36,72,8
Tv 112,135,16384
MAavv 40,76,8
Tv 114,135,18432
MAavv 44,80,8
Rv 48,141,6144
MAavv 48,84,8
)ASM"
R"ASM(buffer_load_dwordx4 v143, s[24:27], s65 offen lds
MAavv 52,88,8
Tv 116,136,16384
MAavv 56,92,8
Tv 118,136,18432
MAavv 60,96,8
Rv 52,141,8192
MAavv 64,68,12
Tv 120,137,16384
MAavv 68,72,12
Tv 122,137,18432
MAavv 72,76,12
Rv 56,141,10240
MAavv 76,80,12
Tv 124,138,16384
MAavv 80,84,12
Tv 126,138,18432
MAavv 84,88,12
Rv 60,141,12288
MAavv 88,92,12
Tv 128,139,16384
MAavv 92,96,12
Tv 130,139,18432
MAavv 96,68,16
Rv 64,141,14336
MAavv 100,72,16
buffer_load_dwordx4 v144, s[24:27], s65 offen offset:1024 lds
MAavv 104,76,16
buffer_load_dwordx4 v145, s[24:27], s65 offen offset:2048 lds
MAavv 108,80,16
buffer_load_dwordx4 v146, s[24:27], s65 offen offset:3072 lds
MAavv 112,84,16
s_add_u32 s50, s50, s51
s_cmp_eq_u32 s50, s53
s_cselect_b32 s50, 0, s50
MAavv 116,88,16
s_add_u32 s62, s62, 16384
s_and_b32 s62, s62, 0xffff
s_mov_b32 m0, s62
MAavv 120,92,16
buffer_load_dwordx4 v147, s[28:31], s50 offen lds
MAavv 124,96,16
s_waitcnt vmcnt(13) lgkmcnt(0)
s_barrier
buffer_load_dwordx4 v148, s[28:31], s50 offen offset:1024 lds
MAavv 0,100,36
s_add_u32 s63, s63, 32768
s_cmp_eq_u32 s63, 163840
s_cselect_b32 s63, 65536, s63
MAavv 4,104,36
v_add_u32 v142, s63, v140
v_xor_b32 v141, 64, v142
MAavv 8,108,36
buffer_load_dwordx4 v149, s[28:31], s50 offen offset:2048 lds
MAavv 12,112,36
buffer_load_dwordx4 v150, s[28:31], s50 offen offset:3072 lds
MAavv 16,116,36
v_xor_b32 v132, 0x8000, v132
v_xor_b32 v133, 0x8000, v133
MAavv 20,120,36
v_xor_b32 v134, 0x8000, v134
v_xor_b32 v135, 0x8000, v135
MAavv 24,124,36
v_xor_b32 v136, 0x8000, v136
v_xor_b32 v137, 0x8000, v137
MAavv 28,128,36
v_xor_b32 v138, 0x8000, v138
v_xor_b32 v139, 0x8000, v139
MAavv 32,100,40
s_add_u32 s48, s48, 128
s_cmp_eq_u32 s48, s52
s_cselect_b32 s48, 0, s48
MAavv 36,104,40
s_add_u32 s61, s61, 32768
s_cmp_ge_u32 s61, 163840
s_cselect_b32 s64, 98304, 0
MAavv 40,108,40
s_sub_u32 s61, s61, s64
s_mov_b32 m0, s61
Rv 4,142,0
MAavv 44,112,40
buffer_load_dwordx4 v143, s[24:27], s48 offen lds
Rv 8,142,2048
MAavv 48,116,40
buffer_load_dwordx4 v144, s[24:27], s48 offen offset:1024 lds
Rv 12,142,4096
MAavv 52,120,40
buffer_load_dwordx4 v145, s[24:27], s48 offen offset:2048 lds
Rv 16,142,6144
MAavv 56,124,40
buffer_load_dwordx4 v146, s[24:27], s48 offen offset:3072 lds
AR 172,0
AR 173,1
MAavv 60,128,40
s_add_u32 s50, s50, s51
s_cmp_eq_u32 s50, s53
s_cselect_b32 s50, 0, s50
AR 174,2
AR 175,3
MAavv 64,100,44
s_add_u32 s62, s62, 16384
s_and_b32 s62, s62, 0xffff
s_mov_b32 m0, s62
AR 176,4
AR 177,5
MAavv 68,104,44
buffer_load_dwordx4 v147, s[28:31], s50 offen lds
AR 178,6
AR 179,7
MAavv 72,108,44
buffer_load_dwordx4 v148, s[28:31], s50 offen offset:1024 lds
CV 204,172,173
CV 205,174,175
MAavv 76,112,44
buffer_load_dwordx4 v149, s[28:31], s50 offen offset:2048 lds
CV 206,176,177
CV 207,178,179
MAavv 80,116,44
buffer_load_dwordx4 v150, s[28:31], s50 offen offset:3072 lds
AR 180,8
AR 181,9
MAavv 84,120,44
s_mul_i32 s60, s71, 0
s_add_u32 s60, s60, s70
buffer_store_dwordx4 v[204:207], v159, s[44:47], s60 offen
AR 182,10
AR 183,11
MAavv 88,124,44
AR 184,12
AR 185,13
AR 186,14
AR 187,15
MAavv 92,128,44
CV 208,180,181
CV 209,182,183
CV 210,184,185
CV 211,186,187
MAavv 96,100,48
buffer_store_dwordx4 v[208:211], v159, s[44:47], s60 offen offset:1024
AR 188,16
AR 189,17
MAavv 100,104,48
AR 190,18
AR 191,19
AR 192,20
AR 193,21
MAavv 104,108,48
AR 194,22
AR 195,23
CV 212,188,189
CV 213,190,191
MAavv 108,112,48
CV 214,192,193
CV 215,194,195
buffer_store_dwordx4 v[212:215], v159, s[44:47], s60 offen offset:2048
MAavv 112,116,48
AR 196,24
AR 197,25
AR 198,26
AR 199,27
MAavv 116,120,48
AR 200,28
AR 201,29
AR 202,30
AR 203,31
MAavv 120,124,48
CV 216,196,197
CV 217,198,199
CV 218,200,201
CV 219,202,203
MAavv 124,128,48
buffer_store_dwordx4 v[216:219], v159, s[44:47], s60 offen offset:3072
AR 172,32
AR 173,33
MAavv 128,68,20
AR 174,34
AR 175,35
AR 176,36
AR 177,37
MAavv 132,72,20
AR 178,38
AR 179,39
CV 220,172,173
CV 221,174,175
MAavv 136,76,20
CV 222,176,177
CV 223,178,179
s_mul_i32 s60, s71, 1
s_add_u32 s60, s60, s70
buffer_store_dwordx4 v[220:223], v159, s[44:47], s60 offen
MAavv 140,80,20
AR 180,40
AR 181,41
AR 182,42
AR 183,43
MAavv 144,84,20
AR 184,44
AR 185,45
AR 186,46
AR 187,47
MAavv 148,88,20
CV 224,180,181
CV 225,182,183
CV 226,184,185
CV 227,186,187
MAavv 152,92,20
buffer_store_dwordx4 v[224:227], v159, s[44:47], s60 offen offset:1024
AR 188,48
AR 189,49
MAavv 156,96,20
AR 190,50
AR 191,51
AR 192,52
AR 193,53
MAavv 160,68,24
AR 194,54
AR 195,55
CV 228,188,189
CV 229,190,191
MAavv 164,72,24
CV 230,192,193
CV 231,194,195
buffer_store_dwordx4 v[228:231], v159, s[44:47], s60 offen offset:2048
MAavv 168,76,24
AR 196,56
AR 197,57
AR 198,58
AR 199,59
MAavv 172,80,24
AR 200,60
AR 201,61
AR 202,62
AR 203,63
MAavv 176,84,24
CV 232,196,197
CV 233,198,199
CV 234,200,201
CV 235,202,203
MAavv 180,88,24
buffer_store_dwordx4 v[232:235], v159, s[44:47], s60 offen offset:3072
AR 172,64
AR 173,65
MAavv 184,92,24
AR 174,66
AR 175,67
AR 176,68
AR 177,69
MAavv 188,96,24
AR 178,70
AR 179,71
CV 204,172,173
CV 205,174,175
MAavv 192,68,28
CV 206,176,177
CV 207,178,179
s_mul_i32 s60, s71, 2
s_add_u32 s60, s60, s70
buffer_store_dwordx4 v[204:207], v159, s[44:47], s60 offen
MAavv 196,72,28
AR 180,72
AR 181,73
AR 182,74
AR 183,75
MAavv 200,76,28
AR 184,76
AR 185,77
AR 186,78
AR 187,79
MAavv 204,80,28
CV 208,180,181
CV 209,182,183
CV 210,184,185
CV 211,186,187
MAavv 208,84,28
buffer_store_dwordx4 v[208:211], v159, s[44:47], s60 offen offset:1024
AR 188,80
AR 189,81
MAavv 212,88,28
AR 190,82
AR 191,83
AR 192,84
AR 193,85
MAavv 216,92,28
AR 194,86
AR 195,87
CV 212,188,189
CV 213,190,191
MAavv 220,96,28
CV 214,192,193
CV 215,194,195
buffer_store_dwordx4 v[212:215], v159, s[44:47], s60 offen offset:2048
MAavv 224,68,32
AR 196,88
AR 197,89
AR 198,90
AR 199,91
MAavv 228,72,32
AR 200,92
AR 201,93
AR 202,94
AR 203,95
MAavv 232,76,32
CV 216,196,197
CV 217,198,199
CV 218,200,201
CV 219,202,203
MAavv 236,80,32
buffer_store_dwordx4 v[216:219], v159, s[44:47], s60 offen offset:3072
AR 172,96
AR 173,97
MAavv 240,84,32
AR 174,98
AR 175,99
AR 176,100
AR 177,101
MAavv 244,88,32
AR 178,102
AR 179,103
CV 220,172,173
CV 221,174,175
MAavv 248,92,32
CV 222,176,177
CV 223,178,179
s_mul_i32 s60, s71, 3
s_add_u32 s60, s60, s70
buffer_store_dwordx4 v[220:223], v159, s[44:47], s60 offen
MAavv 252,96,32
AR 180,104
AR 181,105
AR 182,106
AR 183,107
MAavv 128,100,52
Tv 68,132,0
AR 184,108
AR 185,109
MAavv 132,104,52
Tv 70,132,2048
AR 186,110
AR 187,111
MAavv 136,108,52
CV 224,180,181
CV 225,182,183
CV 226,184,185
CV 227,186,187
MAavv 140,112,52
Tv 72,133,0
buffer_store_dwordx4 v[224:227], v159, s[44:47], s60 offen offset:1024
MAavv 144,116,52
Tv 74,133,2048
AR 188,112
AR 189,113
MAavv 148,120,52
AR 190,114
AR 191,115
AR 192,116
AR 193,117
MAavv 152,124,52
Tv 76,134,0
AR 194,118
AR 195,119
MAavv 156,128,52
Tv 78,134,2048
CV 228,188,189
CV 229,190,191
MAavv 160,100,56
CV 230,192,193
CV 231,194,195
buffer_store_dwordx4 v[228:231], v159, s[44:47], s60 offen offset:2048
MAavv 164,104,56
Tv 80,135,0
AR 196,120
AR 197,121
MAavv 168,108,56
Tv 82,135,2048
AR 198,122
AR 199,123
MAavv 172,112,56
AR 200,124
AR 201,125
AR 202,126
AR 203,127
MAavv 176,116,56
Tv 84,136,0
CV 232,196,197
CV 233,198,199
MAavv 180,120,56
Tv 86,136,2048
CV 234,200,201
CV 235,202,203
MAavv 184,124,56
Rv 20,142,8192
buffer_store_dwordx4 v[232:235], v159, s[44:47], s60 offen offset:3072
MAavv 188,128,56
Tv 88,137,0
AR 172,128
AR 173,129
MAavv 192,100,60
Tv 90,137,2048
AR 174,130
AR 175,131
MAavv 196,104,60
Rv 24,142,10240
AR 176,132
AR 177,133
MAavv 200,108,60
Tv 92,138,0
AR 178,134
AR 179,135
MAavv 204,112,60
Tv 94,138,2048
CV 204,172,173
CV 205,174,175
MAavv 208,116,60
Rv 28,142,12288
CV 206,176,177
CV 207,178,179
MAavv 212,120,60
Tv 96,139,0
s_mul_i32 s60, s71, 4
s_add_u32 s60, s60, s70
buffer_store_dwordx4 v[204:207], v159, s[44:47], s60 offen
MAavv 216,124,60
Tv 98,139,2048
AR 180,136
AR 181,137
MAavv 220,128,60
Rv 32,142,14336
AR 182,138
AR 183,139
MAavv 224,100,64
AR 184,140
AR 185,141
AR 186,142
AR 187,143
MAavv 228,104,64
CV 208,180,181
CV 209,182,183
CV 210,184,185
CV 211,186,187
MAavv 232,108,64
buffer_store_dwordx4 v[208:211], v159, s[44:47], s60 offen offset:1024
AR 188,144
AR 189,145
MAavv 236,112,64
AR 190,146
AR 191,147
AR 192,148
AR 193,149
MAavv 240,116,64
AR 194,150
AR 195,151
CV 212,188,189
CV 213,190,191
MAavv 244,120,64
CV 214,192,193
CV 215,194,195
buffer_store_dwordx4 v[212:215], v159, s[44:47], s60 offen offset:2048
MAavv 248,124,64
AR 196,152
AR 197,153
AR 198,154
AR 199,155
AR 200,156
AR 201,157
AR 202,158
AR 203,159
CV 216,196,197
CV 217,198,199
CV 218,200,201
CV 219,202,203
buffer_store_dwordx4 v[216:219], v159, s[44:47], s60 offen offset:3072
AR 172,160
AR 173,161
AR 174,162
AR 175,163
AR 176,164
AR 177,165
AR 178,166
AR 179,167
CV 220,172,173
CV 221,174,175
CV 222,176,177
CV 223,178,179
s_mul_i32 s60, s71, 5
s_add_u32 s60, s60, s70
buffer_store_dwordx4 v[220:223], v159, s[44:47], s60 offen
AR 180,168
AR 181,169
AR 182,170
AR 183,171
AR 184,172
AR 185,173
AR 186,174
AR 187,175
CV 224,180,181
CV 225,182,183
CV 226,184,185
CV 227,186,187
buffer_store_dwordx4 v[224:227], v159, s[44:47], s60 offen offset:1024
AR 188,176
AR 189,177
AR 190,178
AR 191,179
AR 192,180
AR 193,181
AR 194,182
AR 195,183
CV 228,188,189
CV 229,190,191
CV 230,192,193
CV 231,194,195
buffer_store_dwordx4 v[228:231], v159, s[44:47], s60 offen offset:2048
AR 196,184
AR 197,185
AR 198,186
AR 199,187
AR 200,188
AR 201,189
AR 202,190
AR 203,191
CV 232,196,197
CV 233,198,199
CV 234,200,201
CV 235,202,203
buffer_store_dwordx4 v[232:235], v159, s[44:47], s60 offen offset:3072
MAavv 252,128,64
AR 172,192
AR 173,193
AR 174,194
AR 175,195
AR 176,196
AR 177,197
AR 178,198
AR 179,199
CV 204,172,173
CV 205,174,175
CV 206,176,177
CV 207,178,179
s_mul_i32 s60, s71, 6
s_add_u32 s60, s60, s70
buffer_store_dwordx4 v[204:207], v159, s[44:47], s60 offen
AR 180,200
AR 181,201
AR 182,202
AR 183,203
AR 184,204
AR 185,205
AR 186,206
AR 187,207
CV 208,180,181
CV 209,182,183
CV 210,184,185
CV 211,186,187
buffer_store_dwordx4 v[208:211], v159, s[44:47], s60 offen offset:1024
AR 188,208
AR 189,209
AR 190,210
AR 191,211
AR 192,212
AR 193,213
AR 194,214
AR 195,215
CV 212,188,189
CV 213,190,191
CV 214,192,193
CV 215,194,195
buffer_store_dwordx4 v[212:215], v159, s[44:47], s60 offen offset:2048
AR 196,216
AR 197,217
AR 198,218
AR 199,219
AR 200,220
AR 201,221
AR 202,222
AR 203,223
CV 216,196,197
CV 217,198,199
CV 218,200,201
CV 219,202,203
buffer_store_dwordx4 v[216:219], v159, s[44:47], s60 offen offset:3072
AR 172,224
AR 173,225
AR 174,226
AR 175,227
AR 176,228
AR 177,229
AR 178,230
AR 179,231
CV 220,172,173
CV 221,174,175
CV 222,176,177
CV 223,178,179
s_mul_i32 s60, s71, 7
s_add_u32 s60, s60, s70
buffer_store_dwordx4 v[220:223], v159, s[44:47], s60 offen
AR 180,232
AR 181,233
AR 182,234
AR 183,235
AR 184,236
AR 185,237
AR 186,238
AR 187,239
CV 224,180,181
CV 225,182,183
CV 226,184,185
CV 227,186,187
buffer_store_dwordx4 v[224:227], v159, s[44:47], s60 offen offset:1024
AR 188,240
AR 189,241
AR 190,242
AR 191,243
AR 192,244
AR 193,245
AR 194,246
AR 195,247
CV 228,188,189
CV 229,190,191
CV 230,192,193
CV 231,194,195
buffer_store_dwordx4 v[228:231], v159, s[44:47], s60 offen offset:2048
AR 196,248
AR 197,249
AR 198,250
AR 199,251
AR 200,252
AR 201,253
AR 202,254
AR 203,255
CV 232,196,197
CV 233,198,199
CV 234,200,201
CV 235,202,203
buffer_store_dwordx4 v[232:235], v159, s[44:47], s60 offen offset:3072
s_mov_b32 s38, 0
s_mov_b32 s42, 0
s_branch Lk1_u2
Lk1_u1V:
s_waitcnt vmcnt(16) lgkmcnt(0)
s_barrier
MZavv 0,4,68
Tv 100,132,16384
MZavv 4,4,72
Tv 102,132,18432
MZavv 8,4,76
s_add_u32 s65, s48, s49
s_add_u32 m0, s61, 4096
MZavv 12,4,80
Rv 36,141,0
MZavv 16,4,84
buffer_load_dwordx4 v143, s[24:27], s65 offen lds
MZavv 20,4,88
Tv 104,133,16384
MZavv 24,4,92
MZavv 28,4,96
Tv 106,133,18432
MZavv 32,8,68
Rv 40,141,2048
MZavv 36,8,72
MZavv 40,8,76
Tv 108,134,16384
MZavv 44,8,80
buffer_load_dwordx4 v144, s[24:27], s65 offen offset:1024 lds
MZavv 48,8,84
Tv 110,134,18432
MZavv 52,8,88
MZavv 56,8,92
Rv 44,141,4096
MZavv 60,8,96
Tv 112,135,16384
MZavv 64,12,68
MZavv 68,12,72
Tv 114,135,18432
MZavv 72,12,76
buffer_load_dwordx4 v145, s[24:27], s65 offen offset:2048 lds
MZavv 76,12,80
Rv 48,141,6144
MZavv 80,12,84
MZavv 84,12,88
Tv 116,136,16384
MZavv 88,12,92
Tv 118,136,18432
MZavv 92,12,96
MZavv 96,16,68
Rv 52,141,8192
MZavv 100,16,72
buffer_load_dwordx4 v146, s[24:27], s65 offen offset:3072 lds
MZavv 104,16,76
Tv 120,137,16384
MZavv 108,16,80
s_add_u32 s50, s50, s51
s_cmp_eq_u32 s50, s53
s_cselect_b32 s50, 0, s50
MZavv 112,16,84
Tv 122,137,18432
MZavv 116,16,88
Rv 56,141,10240
MZavv 120,16,92
s_add_u32 s62, s62, 16384
s_and_b32 s62, s62, 0xffff
s_mov_b32 m0, s62
MZavv 124,16,96
Tv 124,138,16384
MZavv 128,20,68
buffer_load_dwordx4 v147, s[28:31], s50 offen lds
MZavv 132,20,72
Tv 126,138,18432
MZavv 136,20,76
MZavv 140,20,80
Rv 60,141,12288
MZavv 144,20,84
Tv 128,139,16384
MZavv 148,20,88
MZavv 152,20,92
Tv 130,139,18432
MZavv 156,20,96
buffer_load_dwordx4 v148, s[28:31], s50 offen offset:1024 lds
MZavv 160,24,68
Rv 64,141,14336
MZavv 164,24,72
MZavv 168,24,76
MZavv 172,24,80
MZavv 176,24,84
MZavv 180,24,88
MZavv 184,24,92
buffer_load_dwordx4 v149, s[28:31], s50 offen offset:2048 lds
MZavv 188,24,96
MZavv 192,28,68
MZavv 196,28,72
MZavv 200,28,76
MZavv 204,28,80
MZavv 208,28,84
MZavv 212,28,88
buffer_load_dwordx4 v150, s[28:31], s50 offen offset:3072 lds
MZavv 216,28,92
MZavv 220,28,96
MZavv 224,32,68
MZavv 228,32,72
MZavv 232,32,76
MZavv 236,32,80
MZavv 240,32,84
MZavv 244,32,88
MZavv 248,32,92
MZavv 252,32,96
s_waitcnt vmcnt(16) lgkmcnt(0)
s_barrier
MAavv 0,36,100
s_add_u32 s63, s63, 32768
s_cmp_eq_u32 s63, 163840
s_cselect_b32 s63, 65536, s63
MAavv 4,36,104
v_add_u32 v142, s63, v140
v_xor_b32 v141, 64, v142
MAavv 8,36,108
v_xor_b32 v132, 0x8000, v132
v_xor_b32 v133, 0x8000, v133
MAavv 12,36,112
v_xor_b32 v134, 0x8000, v134
v_xor_b32 v135, 0x8000, v135
MAavv 16,36,116
v_xor_b32 v136, 0x8000, v136
v_xor_b32 v137, 0x8000, v137
MAavv 20,36,120
v_xor_b32 v138, 0x8000, v138
v_xor_b32 v139, 0x8000, v139
MAavv 24,36,124
Tv 68,132,0
MAavv 28,36,128
Tv 70,132,2048
MAavv 32,40,100
Rv 4,142,0
MAavv 36,40,104
Tv 72,133,0
MAavv 40,40,108
Tv 74,133,2048
MAavv 44,40,112
Rv 8,142,2048
MAavv 48,40,116
s_add_u32 s48, s48, 128
s_cmp_eq_u32 s48, s52
s_cselect_b32 s48, 0, s48
MAavv 52,40,120
Tv 76,134,0
MAavv 56,40,124
s_add_u32 s61, s61, 32768
s_cmp_ge_u32 s61, 163840
s_cselect_b32 s64, 98304, 0
MAavv 60,40,128
Tv 78,134,2048
s_sub_u32 s61, s61, s64
s_mov_b32 m0, s61
MAavv 64,44,100
buffer_load_dwordx4 v143, s[24:27], s48 offen lds
MAavv 68,44,104
Rv 12,142,4096
MAavv 72,44,108
Tv 80,135,0
MAavv 76,44,112
buffer_load_dwordx4 v144, s[24:27], s48 offen offset:1024 lds
MAavv 80,44,116
Tv 82,135,2048
MAavv 84,44,120
buffer_load_dwordx4 v145, s[24:27], s48 offen offset:2048 lds
MAavv 88,44,124
Rv 16,142,6144
MAavv 92,44,128
MAavv 96,48,100
Tv 84,136,0
MAavv 100,48,104
Tv 86,136,2048
MAavv 104,48,108
buffer_load_dwordx4 v146, s[24:27], s48 offen offset:3072 lds
MAavv 108,48,112
Rv 20,142,8192
MAavv 112,48,116
s_add_u32 s50, s50, s51
s_cmp_eq_u32 s50, s53
s_cselect_b32 s50, 0, s50
MAavv 116,48,120
Tv 88,137,0
MAavv 120,48,124
s_add_u32 s62, s62, 16384
s_and_b32 s62, s62, 0xffff
s_mov_b32 m0, s62
MAavv 124,48,128
Tv 90,137,2048
MAavv 128,52,100
Rv 24,142,10240
MAavv 132,52,104
buffer_load_dwordx4 v147, s[28:31], s50 offen lds
MAavv 136,52,108
Tv 92,138,0
MAavv 140,52,112
MAavv 144,52,116
Tv 94,138,2048
MAavv 148,52,120
MAavv 152,52,124
Rv 28,142,12288
MAavv 156,52,128
Tv 96,139,0
MAavv 160,56,100
buffer_load_dwordx4 v148, s[28:31], s50 offen offset:1024 lds
MAavv 164,56,104
Tv 98,139,2048
MAavv 168,56,108
MAavv 172,56,112
Rv 32,142,14336
MAavv 176,56,116
MAavv 180,56,120
MAavv 184,56,124
buffer_load_dwordx4 v149, s[28:31], s50 offen offset:2048 lds
MAavv 188,56,128
MAavv 192,60,100
MAavv 196,60,104
MAavv 200,60,108
MAavv 204,60,112
MAavv 208,60,116
MAavv 212,60,120
buffer_load_dwordx4 v150, s[28:31], s50 offen offset:3072 lds
MAavv 216,60,124
MAavv 220,60,128
MAavv 224,64,100
MAavv 228,64,104
MAavv 232,64,108
MAavv 236,64,112
MAavv 240,64,116
MAavv 244,64,120
MAavv 248,64,124
MAavv 252,64,128
s_waitcnt vmcnt(16) lgkmcnt(0)
s_barrier
MAavv 0,4,68
Tv 100,132,16384
MAavv 4,4,72
Tv 102,132,18432
MAavv 8,4,76
s_add_u32 s65, s48, s49
s_add_u32 m0, s61, 4096
MAavv 12,4,80
Rv 36,141,0
MAavv 16,4,84
buffer_load_dwordx4 v143, s[24:27], s65 offen lds
MAavv 20,4,88
Tv 104,133,16384
MAavv 24,4,92
MAavv 28,4,96
Tv 106,133,18432
MAavv 32,8,68
Rv 40,141,2048
MAavv 36,8,72
MAavv 40,8,76
Tv 108,134,16384
MAavv 44,8,80
buffer_load_dwordx4 v144, s[24:27], s65 offen offset:1024 lds
MAavv 48,8,84
Tv 110,134,18432
MAavv 52,8,88
MAavv 56,8,92
Rv 44,141,4096
MAavv 60,8,96
Tv 112,135,16384
MAavv 64,12,68
MAavv 68,12,72
Tv 114,135,18432
MAavv 72,12,76
buffer_load_dwordx4 v145, s[24:27], s65 offen offset:2048 lds
MAavv 76,12,80
Rv 48,141,6144
MAavv 80,12,84
MAavv 84,12,88
Tv 116,136,16384
MAavv 88,12,92
Tv 118,136,18432
MAavv 92,12,96
MAavv 96,16,68
Rv 52,141,8192
MAavv 100,16,72
buffer_load_dwordx4 v146, s[24:27], s65 offen offset:3072 lds
MAavv 104,16,76
Tv 120,137,16384
MAavv 108,16,80
s_add_u32 s50, s50, s51
s_cmp_eq_u32 s50, s53
s_cselect_b32 s50, 0, s50
MAavv 112,16,84
Tv 122,137,18432
MAavv 116,16,88
Rv 56,141,10240
MAavv 120,16,92
s_add_u32 s62, s62, 16384
s_and_b32 s62, s62, 0xffff
s_mov_b32 m0, s62
MAavv 124,16,96
Tv 124,138,16384
MAavv 128,20,68
buffer_load_dwordx4 v147, s[28:31], s50 offen lds
MAavv 132,20,72
Tv 126,138,18432
MAavv 136,20,76
MAavv 140,20,80
Rv 60,141,12288
MAavv 144,20,84
Tv 128,139,16384
MAavv 148,20,88
MAavv 152,20,92
Tv 130,139,18432
MAavv 156,20,96
buffer_load_dwordx4 v148, s[28:31], s50 offen offset:1024 lds
MAavv 160,24,68
Rv 64,141,14336
MAavv 164,24,72
MAavv 168,24,76
MAavv 172,24,80
MAavv 176,24,84
MAavv 180,24,88
MAavv 184,24,92
buffer_load_dwordx4 v149, s[28:31], s50 offen offset:2048 lds
MAavv 188,24,96
MAavv 192,28,68
MAavv 196,28,72
MAavv 200,28,76
MAavv 204,28,80
MAavv 208,28,84
MAavv 212,28,88
buffer_load_dwordx4 v150, s[28:31], s50 offen offset:3072 lds
MAavv 216,28,92
MAavv 220,28,96
MAavv 224,32,68
MAavv 228,32,72
MAavv 232,32,76
MAavv 236,32,80
MAavv 240,32,84
MAavv 244,32,88
MAavv 248,32,92
MAavv 252,32,96
s_waitcnt vmcnt(16) lgkmcnt(0)
s_barrier
MAavv 0,36,100
s_add_u32 s63, s63, 32768
s_cmp_eq_u32 s63, 163840
s_cselect_b32 s63, 65536, s63
MAavv 4,36,104
v_add_u32 v142, s63, v140
v_xor_b32 v141, 64, v142
MAavv 8,36,108
v_xor_b32 v132, 0x8000, v132
v_xor_b32 v133, 0x8000, v133
MAavv 12,36,112
v_xor_b32 v134, 0x8000, v134
v_xor_b32 v135, 0x8000, v135
MAavv 16,36,116
v_xor_b32 v136, 0x8000, v136
v_xor_b32 v137, 0x8000, v137
MAavv 20,36,120
v_xor_b32 v138, 0x8000, v138
v_xor_b32 v139, 0x8000, v139
MAavv 24,36,124
Tv 68,132,0
MAavv 28,36,128
Tv 70,132,2048
MAavv 32,40,100
Rv 4,142,0
MAavv 36,40,104
Tv 72,133,0
MAavv 40,40,108
Tv 74,133,2048
MAavv 44,40,112
Rv 8,142,2048
MAavv 48,40,116
s_add_u32 s48, s48, 128
s_cmp_eq_u32 s48, s52
s_cselect_b32 s48, 0, s48
MAavv 52,40,120
Tv 76,134,0
MAavv 56,40,124
s_add_u32 s61, s61, 32768
s_cmp_ge_u32 s61, 163840
s_cselect_b32 s64, 98304, 0
MAavv 60,40,128
Tv 78,134,2048
s_sub_u32 s61, s61, s64
s_mov_b32 m0, s61
MAavv 64,44,100
buffer_load_dwordx4 v143, s[24:27], s48 offen lds
MAavv 68,44,104
Rv 12,142,4096
MAavv 72,44,108
Tv 80,135,0
MAavv 76,44,112
buffer_load_dwordx4 v144, s[24:27], s48 offen offset:1024 lds
MAavv 80,44,116
Tv 82,135,2048
MAavv 84,44,120
buffer_load_dwordx4 v145, s[24:27], s48 offen offset:2048 lds
MAavv 88,44,124
Rv 16,142,6144
MAavv 92,44,128
MAavv 96,48,100
Tv 84,136,0
MAavv 100,48,104
Tv 86,136,2048
MAavv 104,48,108
buffer_load_dwordx4 v146, s[24:27], s48 offen offset:3072 lds
MAavv 108,48,112
Rv 20,142,8192
MAavv 112,48,116
s_add_u32 s50, s50, s51
s_cmp_eq_u32 s50, s53
s_cselect_b32 s50, 0, s50
MAavv 116,48,120
Tv 88,137,0
MAavv 120,48,124
s_add_u32 s62, s62, 16384
s_and_b32 s62, s62, 0xffff
s_mov_b32 m0, s62
MAavv 124,48,128
Tv 90,137,2048
MAavv 128,52,100
Rv 24,142,10240
MAavv 132,52,104
buffer_load_dwordx4 v147, s[28:31], s50 offen lds
MAavv 136,52,108
Tv 92,138,0
MAavv 140,52,112
MAavv 144,52,116
Tv 94,138,2048
MAavv 148,52,120
MAavv 152,52,124
Rv 28,142,12288
MAavv 156,52,128
Tv 96,139,0
MAavv 160,56,100
buffer_load_dwordx4 v148, s[28:31], s50 offen offset:1024 lds
MAavv 164,56,104
Tv 98,139,2048
MAavv 168,56,108
MAavv 172,56,112
Rv 32,142,14336
MAavv 176,56,116
MAavv 180,56,120
MAavv 184,56,124
buffer_load_dwordx4 v149, s[28:31], s50 offen offset:2048 lds
MAavv 188,56,128
MAavv 192,60,100
MAavv 196,60,104
MAavv 200,60,108
MAavv 204,60,112
MAavv 208,60,116
MAavv 212,60,120
buffer_load_dwordx4 v150, s[28:31], s50 offen offset:3072 lds
MAavv 216,60,124
MAavv 220,60,128
MAavv 224,64,100
MAavv 228,64,104
MAavv 232,64,108
MAavv 236,64,112
MAavv 240,64,116
MAavv 244,64,120
MAavv 248,64,124
MAavv 252,64,128
s_waitcnt vmcnt(16) lgkmcnt(0)
s_barrier
MAavv 0,4,68
Tv 100,132,16384
MAavv 4,4,72
Tv 102,132,18432
MAavv 8,4,76
s_add_u32 s65, s48, s49
s_add_u32 m0, s61, 4096
MAavv 12,4,80
Rv 36,141,0
MAavv 16,4,84
buffer_load_dwordx4 v143, s[24:27], s65 offen lds
MAavv 20,4,88
Tv 104,133,16384
MAavv 24,4,92
MAavv 28,4,96
Tv 106,133,18432
MAavv 32,8,68
Rv 40,141,2048
MAavv 36,8,72
MAavv 40,8,76
Tv 108,134,16384
MAavv 44,8,80
buffer_load_dwordx4 v144, s[24:27], s65 offen offset:1024 lds
MAavv 48,8,84
Tv 110,134,18432
MAavv 52,8,88
MAavv 56,8,92
Rv 44,141,4096
MAavv 60,8,96
Tv 112,135,16384
MAavv 64,12,68
MAavv 68,12,72
Tv 114,135,18432
MAavv 72,12,76
buffer_load_dwordx4 v145, s[24:27], s65 offen offset:2048 lds
MAavv 76,12,80
Rv 48,141,6144
MAavv 80,12,84
MAavv 84,12,88
Tv 116,136,16384
MAavv 88,12,92
Tv 118,136,18432
MAavv 92,12,96
MAavv 96,16,68
Rv 52,141,8192
MAavv 100,16,72
buffer_load_dwordx4 v146, s[24:27], s65 offen offset:3072 lds
MAavv 104,16,76
Tv 120,137,16384
MAavv 108,16,80
s_add_u32 s50, s50, s51
s_cmp_eq_u32 s50, s53
s_cselect_b32 s50, 0, s50
MAavv 112,16,84
Tv 122,137,18432
MAavv 116,16,88
Rv 56,141,10240
MAavv 120,16,92
s_add_u32 s62, s62, 16384
s_and_b32 s62, s62, 0xffff
s_mov_b32 m0, s62
MAavv 124,16,96
Tv 124,138,16384
MAavv 128,20,68
buffer_load_dwordx4 v147, s[28:31], s50 offen lds
MAavv 132,20,72
Tv 126,138,18432
MAavv 136,20,76
MAavv 140,20,80
Rv 60,141,12288
MAavv 144,20,84
Tv 128,139,16384
MAavv 148,20,88
MAavv 152,20,92
Tv 130,139,18432
MAavv 156,20,96
buffer_load_dwordx4 v148, s[28:31], s50 offen offset:1024 lds
MAavv 160,24,68
Rv 64,141,14336
MAavv 164,24,72
MAavv 168,24,76
MAavv 172,24,80
MAavv 176,24,84
MAavv 180,24,88
MAavv 184,24,92
buffer_load_dwordx4 v149, s[28:31], s50 offen offset:2048 lds
MAavv 188,24,96
MAavv 192,28,68
MAavv 196,28,72
MAavv 200,28,76
MAavv 204,28,80
MAavv 208,28,84
MAavv 212,28,88
buffer_load_dwordx4 v150, s[28:31], s50 offen offset:3072 lds
MAavv 216,28,92
MAavv 220,28,96
MAavv 224,32,68
MAavv 228,32,72
MAavv 232,32,76
MAavv 236,32,80
MAavv 240,32,84
MAavv 244,32,88
MAavv 248,32,92
MAavv 252,32,96
s_waitcnt vmcnt(16) lgkmcnt(0)
s_barrier
MAavv 0,36,100
s_add_u32 s63, s63, 32768
s_cmp_eq_u32 s63, 163840
s_cselect_b32 s63, 65536, s63
MAavv 4,36,104
v_add_u32 v142, s63, v140
v_xor_b32 v141, 64, v142
MAavv 8,36,108
v_xor_b32 v132, 0x8000, v132
v_xor_b32 v133, 0x8000, v133
MAavv 12,36,112
v_xor_b32 v134, 0x8000, v134
v_xor_b32 v135, 0x8000, v135
MAavv 16,36,116
v_xor_b32 v136, 0x8000, v136
v_xor_b32 v137, 0x8000, v137
MAavv 20,36,120
v_xor_b32 v138, 0x8000, v138
v_xor_b32 v139, 0x8000, v139
MAavv 24,36,124
Tv 68,132,0
MAavv 28,36,128
Tv 70,132,2048
MAavv 32,40,100
Rv 4,142,0
MAavv 36,40,104
Tv 72,133,0
MAavv 40,40,108
Tv 74,133,2048
MAavv 44,40,112
Rv 8,142,2048
MAavv 48,40,116
s_add_u32 s48, s48, 128
s_cmp_eq_u32 s48, s52
s_cselect_b32 s48, 0, s48
MAavv 52,40,120
Tv 76,134,0
MAavv 56,40,124
s_add_u32 s61, s61, 32768
s_cmp_ge_u32 s61, 163840
s_cselect_b32 s64, 98304, 0
MAavv 60,40,128
Tv 78,134,2048
s_sub_u32 s61, s61, s64
s_mov_b32 m0, s61
MAavv 64,44,100
buffer_load_dwordx4 v143, s[24:27], s48 offen lds
MAavv 68,44,104
Rv 12,142,4096
MAavv 72,44,108
Tv 80,135,0
MAavv 76,44,112
buffer_load_dwordx4 v144, s[24:27], s48 offen offset:1024 lds
MAavv 80,44,116
Tv 82,135,2048
MAavv 84,44,120
buffer_load_dwordx4 v145, s[24:27], s48 offen offset:2048 lds
MAavv 88,44,124
Rv 16,142,6144
MAavv 92,44,128
MAavv 96,48,100
Tv 84,136,0
MAavv 100,48,104
Tv 86,136,2048
MAavv 104,48,108
buffer_load_dwordx4 v146, s[24:27], s48 offen offset:3072 lds
MAavv 108,48,112
Rv 20,142,8192
MAavv 112,48,116
s_add_u32 s50, s50, s51
s_cmp_eq_u32 s50, s53
s_cselect_b32 s50, 0, s50
MAavv 116,48,120
Tv 88,137,0
MAavv 120,48,124
s_add_u32 s62, s62, 16384
s_and_b32 s62, s62, 0xffff
s_mov_b32 m0, s62
MAavv 124,48,128
Tv 90,137,2048
MAavv 128,52,100
Rv 24,142,10240
MAavv 132,52,104
buffer_load_dwordx4 v147, s[28:31], s50 offen lds
MAavv 136,52,108
Tv 92,138,0
MAavv 140,52,112
MAavv 144,52,116
Tv 94,138,2048
MAavv 148,52,120
MAavv 152,52,124
Rv 28,142,12288
MAavv 156,52,128
Tv 96,139,0
MAavv 160,56,100
buffer_load_dwordx4 v148, s[28:31], s50 offen offset:1024 lds
MAavv 164,56,104
Tv 98,139,2048
MAavv 168,56,108
MAavv 172,56,112
Rv 32,142,14336
MAavv 176,56,116
MAavv 180,56,120
MAavv 184,56,124
buffer_load_dwordx4 v149, s[28:31], s50 offen offset:2048 lds
MAavv 188,56,128
MAavv 192,60,100
MAavv 196,60,104
MAavv 200,60,108
MAavv 204,60,112
MAavv 208,60,116
MAavv 212,60,120
buffer_load_dwordx4 v150, s[28:31], s50 offen offset:3072 lds
MAavv 216,60,124
MAavv 220,60,128
MAavv 224,64,100
MAavv 228,64,104
MAavv 232,64,108
MAavv 236,64,112
MAavv 240,64,116
MAavv 244,64,120
MAavv 248,64,124
MAavv 252,64,128
s_waitcnt vmcnt(16) lgkmcnt(0)
s_barrier
MAavv 0,4,68
Tv 100,132,16384
MAavv 4,4,72
Tv 102,132,18432
MAavv 8,4,76
s_add_u32 s65, s48, s49
s_add_u32 m0, s61, 4096
MAavv 12,4,80
Rv 36,141,0
MAavv 16,4,84
buffer_load_dwordx4 v143, s[24:27], s65 offen lds
MAavv 20,4,88
Tv 104,133,16384
MAavv 24,4,92
MAavv 28,4,96
Tv 106,133,18432
MAavv 32,8,68
Rv 40,141,2048
MAavv 36,8,72
MAavv 40,8,76
Tv 108,134,16384
MAavv 44,8,80
buffer_load_dwordx4 v144, s[24:27], s65 offen offset:1024 lds
MAavv 48,8,84
Tv 110,134,18432
MAavv 52,8,88
MAavv 56,8,92
Rv 44,141,4096
MAavv 60,8,96
Tv 112,135,16384
MAavv 64,12,68
MAavv 68,12,72
Tv 114,135,18432
MAavv 72,12,76
buffer_load_dwordx4 v145, s[24:27], s65 offen offset:2048 lds
MAavv 76,12,80
Rv 48,141,6144
MAavv 80,12,84
MAavv 84,12,88
Tv 116,136,16384
MAavv 88,12,92
Tv 118,136,18432
MAavv 92,12,96
MAavv 96,16,68
Rv 52,141,8192
MAavv 100,16,72
buffer_load_dwordx4 v146, s[24:27], s65 offen offset:3072 lds
MAavv 104,16,76
Tv 120,137,16384
MAavv 108,16,80
s_add_u32 s50, s50, s51
s_cmp_eq_u32 s50, s53
s_cselect_b32 s50, 0, s50
MAavv 112,16,84
Tv 122,137,18432
MAavv 116,16,88
Rv 56,141,10240
MAavv 120,16,92
s_add_u32 s62, s62, 16384
s_and_b32 s62, s62, 0xffff
s_mov_b32 m0, s62
MAavv 124,16,96
Tv 124,138,16384
MAavv 128,20,68
buffer_load_dwordx4 v147, s[28:31], s50 offen lds
MAavv 132,20,72
Tv 126,138,18432
MAavv 136,20,76
MAavv 140,20,80
Rv 60,141,12288
MAavv 144,20,84
Tv 128,139,16384
MAavv 148,20,88
MAavv 152,20,92
Tv 130,139,18432
MAavv 156,20,96
buffer_load_dwordx4 v148, s[28:31], s50 offen offset:1024 lds
MAavv 160,24,68
Rv 64,141,14336
MAavv 164,24,72
MAavv 168,24,76
MAavv 172,24,80
MAavv 176,24,84
MAavv 180,24,88
MAavv 184,24,92
buffer_load_dwordx4 v149, s[28:31], s50 offen offset:2048 lds
MAavv 188,24,96
MAavv 192,28,68
MAavv 196,28,72
MAavv 200,28,76
MAavv 204,28,80
MAavv 208,28,84
MAavv 212,28,88
buffer_load_dwordx4 v150, s[28:31], s50 offen offset:3072 lds
MAavv 216,28,92
MAavv 220,28,96
MAavv 224,32,68
MAavv 228,32,72
MAavv 232,32,76
MAavv 236,32,80
MAavv 240,32,84
MAavv 244,32,88
MAavv 248,32,92
MAavv 252,32,96
s_waitcnt vmcnt(16) lgkmcnt(0)
s_barrier
MAavv 0,36,100
s_add_u32 s63, s63, 32768
s_cmp_eq_u32 s63, 163840
s_cselect_b32 s63, 65536, s63
MAavv 4,36,104
v_add_u32 v142, s63, v140
v_xor_b32 v141, 64, v142
MAavv 8,36,108
v_xor_b32 v132, 0x8000, v132
v_xor_b32 v133, 0x8000, v133
MAavv 12,36,112
v_xor_b32 v134, 0x8000, v134
v_xor_b32 v135, 0x8000, v135
MAavv 16,36,116
v_xor_b32 v136, 0x8000, v136
v_xor_b32 v137, 0x8000, v137
MAavv 20,36,120
v_xor_b32 v138, 0x8000, v138
v_xor_b32 v139, 0x8000, v139
MAavv 24,36,124
Tv 68,132,0
MAavv 28,36,128
Tv 70,132,2048
MAavv 32,40,100
Rv 4,142,0
MAavv 36,40,104
Tv 72,133,0
MAavv 40,40,108
Tv 74,133,2048
MAavv 44,40,112
Rv 8,142,2048
MAavv 48,40,116
s_add_u32 s48, s48, 128
s_cmp_eq_u32 s48, s52
s_cselect_b32 s48, 0, s48
MAavv 52,40,120
Tv 76,134,0
MAavv 56,40,124
s_add_u32 s61, s61, 32768
s_cmp_ge_u32 s61, 163840
s_cselect_b32 s64, 98304, 0
MAavv 60,40,128
Tv 78,134,2048
s_sub_u32 s61, s61, s64
s_mov_b32 m0, s61
MAavv 64,44,100
buffer_load_dwordx4 v143, s[24:27], s48 offen lds
MAavv 68,44,104
Rv 12,142,4096
MAavv 72,44,108
Tv 80,135,0
MAavv 76,44,112
buffer_load_dwordx4 v144, s[24:27], s48 offen offset:1024 lds
MAavv 80,44,116
Tv 82,135,2048
MAavv 84,44,120
buffer_load_dwordx4 v145, s[24:27], s48 offen offset:2048 lds
MAavv 88,44,124
Rv 16,142,6144
MAavv 92,44,128
MAavv 96,48,100
Tv 84,136,0
MAavv 100,48,104
Tv 86,136,2048
MAavv 104,48,108
buffer_load_dwordx4 v146, s[24:27], s48 offen offset:3072 lds
MAavv 108,48,112
Rv 20,142,8192
MAavv 112,48,116
s_add_u32 s50, s50, s51
s_cmp_eq_u32 s50, s53
s_cselect_b32 s50, 0, s50
MAavv 116,48,120
Tv 88,137,0
MAavv 120,48,124
s_add_u32 s62, s62, 16384
s_and_b32 s62, s62, 0xffff
s_mov_b32 m0, s62
MAavv 124,48,128
Tv 90,137,2048
MAavv 128,52,100
Rv 24,142,10240
MAavv 132,52,104
buffer_load_dwordx4 v147, s[28:31], s50 offen lds
MAavv 136,52,108
Tv 92,138,0
MAavv 140,52,112
MAavv 144,52,116
Tv 94,138,2048
MAavv 148,52,120
MAavv 152,52,124
Rv 28,142,12288
MAavv 156,52,128
Tv 96,139,0
MAavv 160,56,100
buffer_load_dwordx4 v148, s[28:31], s50 offen offset:1024 lds
MAavv 164,56,104
Tv 98,139,2048
MAavv 168,56,108
MAavv 172,56,112
Rv 32,142,14336
MAavv 176,56,116
MAavv 180,56,120
MAavv 184,56,124
buffer_load_dwordx4 v149, s[28:31], s50 offen offset:2048 lds
MAavv 188,56,128
MAavv 192,60,100
MAavv 196,60,104
MAavv 200,60,108
MAavv 204,60,112
MAavv 208,60,116
MAavv 212,60,120
buffer_load_dwordx4 v150, s[28:31], s50 offen offset:3072 lds
)ASM"
R"ASM(MAavv 216,60,124
MAavv 220,60,128
MAavv 224,64,100
MAavv 228,64,104
MAavv 232,64,108
MAavv 236,64,112
MAavv 240,64,116
MAavv 244,64,120
MAavv 248,64,124
MAavv 252,64,128
s_waitcnt vmcnt(16) lgkmcnt(0)
s_barrier
MAavv 0,4,68
Tv 100,132,16384
MAavv 4,4,72
Tv 102,132,18432
MAavv 8,4,76
s_add_u32 s65, s48, s49
s_add_u32 m0, s61, 4096
MAavv 12,4,80
Rv 36,141,0
MAavv 16,4,84
buffer_load_dwordx4 v143, s[24:27], s65 offen lds
MAavv 20,4,88
Tv 104,133,16384
MAavv 24,4,92
MAavv 28,4,96
Tv 106,133,18432
MAavv 32,8,68
Rv 40,141,2048
MAavv 36,8,72
MAavv 40,8,76
Tv 108,134,16384
MAavv 44,8,80
buffer_load_dwordx4 v144, s[24:27], s65 offen offset:1024 lds
MAavv 48,8,84
Tv 110,134,18432
MAavv 52,8,88
MAavv 56,8,92
Rv 44,141,4096
MAavv 60,8,96
Tv 112,135,16384
MAavv 64,12,68
MAavv 68,12,72
Tv 114,135,18432
MAavv 72,12,76
buffer_load_dwordx4 v145, s[24:27], s65 offen offset:2048 lds
MAavv 76,12,80
Rv 48,141,6144
MAavv 80,12,84
MAavv 84,12,88
Tv 116,136,16384
MAavv 88,12,92
Tv 118,136,18432
MAavv 92,12,96
MAavv 96,16,68
Rv 52,141,8192
MAavv 100,16,72
buffer_load_dwordx4 v146, s[24:27], s65 offen offset:3072 lds
MAavv 104,16,76
Tv 120,137,16384
MAavv 108,16,80
s_add_u32 s50, s50, s51
s_cmp_eq_u32 s50, s53
s_cselect_b32 s50, 0, s50
MAavv 112,16,84
Tv 122,137,18432
MAavv 116,16,88
Rv 56,141,10240
MAavv 120,16,92
s_add_u32 s62, s62, 16384
s_and_b32 s62, s62, 0xffff
s_mov_b32 m0, s62
MAavv 124,16,96
Tv 124,138,16384
MAavv 128,20,68
buffer_load_dwordx4 v147, s[28:31], s50 offen lds
MAavv 132,20,72
Tv 126,138,18432
MAavv 136,20,76
MAavv 140,20,80
Rv 60,141,12288
MAavv 144,20,84
Tv 128,139,16384
MAavv 148,20,88
MAavv 152,20,92
Tv 130,139,18432
MAavv 156,20,96
buffer_load_dwordx4 v148, s[28:31], s50 offen offset:1024 lds
MAavv 160,24,68
Rv 64,141,14336
MAavv 164,24,72
MAavv 168,24,76
MAavv 172,24,80
MAavv 176,24,84
MAavv 180,24,88
MAavv 184,24,92
buffer_load_dwordx4 v149, s[28:31], s50 offen offset:2048 lds
MAavv 188,24,96
MAavv 192,28,68
MAavv 196,28,72
MAavv 200,28,76
MAavv 204,28,80
MAavv 208,28,84
MAavv 212,28,88
buffer_load_dwordx4 v150, s[28:31], s50 offen offset:3072 lds
MAavv 216,28,92
MAavv 220,28,96
MAavv 224,32,68
MAavv 228,32,72
MAavv 232,32,76
MAavv 236,32,80
MAavv 240,32,84
MAavv 244,32,88
MAavv 248,32,92
MAavv 252,32,96
s_waitcnt vmcnt(16) lgkmcnt(0)
s_barrier
MAavv 0,36,100
s_add_u32 s63, s63, 32768
s_cmp_eq_u32 s63, 163840
s_cselect_b32 s63, 65536, s63
MAavv 4,36,104
v_add_u32 v142, s63, v140
v_xor_b32 v141, 64, v142
MAavv 8,36,108
v_xor_b32 v132, 0x8000, v132
v_xor_b32 v133, 0x8000, v133
MAavv 12,36,112
v_xor_b32 v134, 0x8000, v134
v_xor_b32 v135, 0x8000, v135
MAavv 16,36,116
v_xor_b32 v136, 0x8000, v136
v_xor_b32 v137, 0x8000, v137
MAavv 20,36,120
v_xor_b32 v138, 0x8000, v138
v_xor_b32 v139, 0x8000, v139
MAavv 24,36,124
Tv 68,132,0
MAavv 28,36,128
Tv 70,132,2048
MAavv 32,40,100
Rv 4,142,0
MAavv 36,40,104
Tv 72,133,0
MAavv 40,40,108
Tv 74,133,2048
MAavv 44,40,112
Rv 8,142,2048
MAavv 48,40,116
s_mov_b64 s[24:25], s[36:37]
s_mov_b64 s[26:27], s[38:39]
s_mov_b32 s48, s58
MAavv 52,40,120
Tv 76,134,0
s_mov_b32 s49, s54
s_mov_b32 s52, s56
MAavv 56,40,124
v_mov_b32 v143, v151
v_mov_b32 v144, v152
MAavv 60,40,128
Tv 78,134,2048
v_mov_b32 v145, v153
MAavv 64,44,100
v_mov_b32 v146, v154
MAavv 68,44,104
Rv 12,142,4096
MAavv 72,44,108
Tv 80,135,0
MAavv 76,44,112
s_add_u32 s61, s61, 32768
s_cmp_ge_u32 s61, 163840
s_cselect_b32 s64, 98304, 0
MAavv 80,44,116
Tv 82,135,2048
s_sub_u32 s61, s61, s64
s_mov_b32 m0, s61
MAavv 84,44,120
buffer_load_dwordx4 v143, s[24:27], s48 offen lds
MAavv 88,44,124
Rv 16,142,6144
MAavv 92,44,128
buffer_load_dwordx4 v144, s[24:27], s48 offen offset:1024 lds
MAavv 96,48,100
Tv 84,136,0
MAavv 100,48,104
Tv 86,136,2048
MAavv 104,48,108
buffer_load_dwordx4 v145, s[24:27], s48 offen offset:2048 lds
MAavv 108,48,112
Rv 20,142,8192
MAavv 112,48,116
buffer_load_dwordx4 v146, s[24:27], s48 offen offset:3072 lds
MAavv 116,48,120
Tv 88,137,0
MAavv 120,48,124
s_add_u32 s50, s50, s51
s_cmp_eq_u32 s50, s53
s_cselect_b32 s50, 0, s50
MAavv 124,48,128
Tv 90,137,2048
MAavv 128,52,100
Rv 24,142,10240
MAavv 132,52,104
s_add_u32 s62, s62, 16384
s_and_b32 s62, s62, 0xffff
s_mov_b32 m0, s62
MAavv 136,52,108
Tv 92,138,0
MAavv 140,52,112
buffer_load_dwordx4 v147, s[28:31], s50 offen lds
MAavv 144,52,116
Tv 94,138,2048
MAavv 148,52,120
MAavv 152,52,124
Rv 28,142,12288
MAavv 156,52,128
Tv 96,139,0
MAavv 160,56,100
buffer_load_dwordx4 v148, s[28:31], s50 offen offset:1024 lds
MAavv 164,56,104
Tv 98,139,2048
MAavv 168,56,108
MAavv 172,56,112
Rv 32,142,14336
MAavv 176,56,116
MAavv 180,56,120
MAavv 184,56,124
buffer_load_dwordx4 v149, s[28:31], s50 offen offset:2048 lds
MAavv 188,56,128
MAavv 192,60,100
MAavv 196,60,104
MAavv 200,60,108
MAavv 204,60,112
MAavv 208,60,116
MAavv 212,60,120
buffer_load_dwordx4 v150, s[28:31], s50 offen offset:3072 lds
MAavv 216,60,124
MAavv 220,60,128
MAavv 224,64,100
MAavv 228,64,104
MAavv 232,64,108
MAavv 236,64,112
MAavv 240,64,116
MAavv 244,64,120
MAavv 248,64,124
MAavv 252,64,128
s_waitcnt vmcnt(16) lgkmcnt(0)
s_barrier
MAavv 0,4,68
Tv 100,132,16384
MAavv 4,4,72
Tv 102,132,18432
MAavv 8,4,76
s_add_u32 s65, s48, s49
s_add_u32 m0, s61, 4096
MAavv 12,4,80
Rv 36,141,0
MAavv 16,4,84
buffer_load_dwordx4 v143, s[24:27], s65 offen lds
MAavv 20,4,88
Tv 104,133,16384
MAavv 24,4,92
MAavv 28,4,96
Tv 106,133,18432
MAavv 32,8,68
Rv 40,141,2048
MAavv 36,8,72
MAavv 40,8,76
Tv 108,134,16384
MAavv 44,8,80
buffer_load_dwordx4 v144, s[24:27], s65 offen offset:1024 lds
MAavv 48,8,84
Tv 110,134,18432
MAavv 52,8,88
MAavv 56,8,92
Rv 44,141,4096
MAavv 60,8,96
Tv 112,135,16384
MAavv 64,12,68
MAavv 68,12,72
Tv 114,135,18432
MAavv 72,12,76
buffer_load_dwordx4 v145, s[24:27], s65 offen offset:2048 lds
MAavv 76,12,80
Rv 48,141,6144
MAavv 80,12,84
MAavv 84,12,88
Tv 116,136,16384
MAavv 88,12,92
Tv 118,136,18432
MAavv 92,12,96
MAavv 96,16,68
Rv 52,141,8192
MAavv 100,16,72
buffer_load_dwordx4 v146, s[24:27], s65 offen offset:3072 lds
MAavv 104,16,76
Tv 120,137,16384
MAavv 108,16,80
s_mov_b64 s[28:29], s[40:41]
s_mov_b64 s[30:31], s[42:43]
s_mov_b32 s50, s59
MAavv 112,16,84
Tv 122,137,18432
s_mov_b32 s51, s55
s_mov_b32 s53, s57
MAavv 116,16,88
Rv 56,141,10240
v_mov_b32 v147, v155
MAavv 120,16,92
v_mov_b32 v148, v156
v_mov_b32 v149, v157
MAavv 124,16,96
Tv 124,138,16384
v_mov_b32 v150, v158
MAavv 128,20,68
s_add_u32 s62, s62, 16384
s_and_b32 s62, s62, 0xffff
s_mov_b32 m0, s62
MAavv 132,20,72
Tv 126,138,18432
MAavv 136,20,76
buffer_load_dwordx4 v147, s[28:31], s50 offen lds
MAavv 140,20,80
Rv 60,141,12288
MAavv 144,20,84
Tv 128,139,16384
MAavv 148,20,88
MAavv 152,20,92
Tv 130,139,18432
MAavv 156,20,96
buffer_load_dwordx4 v148, s[28:31], s50 offen offset:1024 lds
MAavv 160,24,68
Rv 64,141,14336
MAavv 164,24,72
MAavv 168,24,76
MAavv 172,24,80
MAavv 176,24,84
MAavv 180,24,88
MAavv 184,24,92
buffer_load_dwordx4 v149, s[28:31], s50 offen offset:2048 lds
MAavv 188,24,96
MAavv 192,28,68
MAavv 196,28,72
MAavv 200,28,76
MAavv 204,28,80
MAavv 208,28,84
MAavv 212,28,88
buffer_load_dwordx4 v150, s[28:31], s50 offen offset:3072 lds
MAavv 216,28,92
MAavv 220,28,96
MAavv 224,32,68
MAavv 228,32,72
MAavv 232,32,76
MAavv 236,32,80
MAavv 240,32,84
MAavv 244,32,88
MAavv 248,32,92
MAavv 252,32,96
s_waitcnt vmcnt(16) lgkmcnt(0)
s_barrier
MAavv 0,36,100
s_add_u32 s63, s63, 32768
s_cmp_eq_u32 s63, 163840
s_cselect_b32 s63, 65536, s63
MAavv 4,36,104
v_add_u32 v142, s63, v140
v_xor_b32 v141, 64, v142
MAavv 8,36,108
v_xor_b32 v132, 0x8000, v132
v_xor_b32 v133, 0x8000, v133
MAavv 12,36,112
v_xor_b32 v134, 0x8000, v134
v_xor_b32 v135, 0x8000, v135
MAavv 16,36,116
v_xor_b32 v136, 0x8000, v136
v_xor_b32 v137, 0x8000, v137
MAavv 20,36,120
v_xor_b32 v138, 0x8000, v138
v_xor_b32 v139, 0x8000, v139
MAavv 24,36,124
Tv 68,132,0
MAavv 28,36,128
Tv 70,132,2048
MAavv 32,40,100
Rv 4,142,0
MAavv 36,40,104
Tv 72,133,0
MAavv 40,40,108
Tv 74,133,2048
MAavv 44,40,112
Rv 8,142,2048
MAavv 48,40,116
s_add_u32 s48, s48, 128
s_cmp_eq_u32 s48, s52
s_cselect_b32 s48, 0, s48
MAavv 52,40,120
Tv 76,134,0
MAavv 56,40,124
s_add_u32 s61, s61, 32768
s_cmp_ge_u32 s61, 163840
s_cselect_b32 s64, 98304, 0
MAavv 60,40,128
Tv 78,134,2048
s_sub_u32 s61, s61, s64
s_mov_b32 m0, s61
MAavv 64,44,100
buffer_load_dwordx4 v143, s[24:27], s48 offen lds
MAavv 68,44,104
Rv 12,142,4096
MAavv 72,44,108
Tv 80,135,0
MAavv 76,44,112
buffer_load_dwordx4 v144, s[24:27], s48 offen offset:1024 lds
MAavv 80,44,116
Tv 82,135,2048
MAavv 84,44,120
buffer_load_dwordx4 v145, s[24:27], s48 offen offset:2048 lds
MAavv 88,44,124
Rv 16,142,6144
MAavv 92,44,128
MAavv 96,48,100
Tv 84,136,0
MAavv 100,48,104
Tv 86,136,2048
MAavv 104,48,108
buffer_load_dwordx4 v146, s[24:27], s48 offen offset:3072 lds
MAavv 108,48,112
Rv 20,142,8192
MAavv 112,48,116
s_add_u32 s50, s50, s51
s_cmp_eq_u32 s50, s53
s_cselect_b32 s50, 0, s50
MAavv 116,48,120
Tv 88,137,0
MAavv 120,48,124
s_add_u32 s62, s62, 16384
s_and_b32 s62, s62, 0xffff
s_mov_b32 m0, s62
MAavv 124,48,128
Tv 90,137,2048
MAavv 128,52,100
Rv 24,142,10240
MAavv 132,52,104
buffer_load_dwordx4 v147, s[28:31], s50 offen lds
MAavv 136,52,108
Tv 92,138,0
MAavv 140,52,112
MAavv 144,52,116
Tv 94,138,2048
MAavv 148,52,120
MAavv 152,52,124
Rv 28,142,12288
MAavv 156,52,128
Tv 96,139,0
MAavv 160,56,100
buffer_load_dwordx4 v148, s[28:31], s50 offen offset:1024 lds
MAavv 164,56,104
Tv 98,139,2048
MAavv 168,56,108
MAavv 172,56,112
Rv 32,142,14336
MAavv 176,56,116
MAavv 180,56,120
MAavv 184,56,124
buffer_load_dwordx4 v149, s[28:31], s50 offen offset:2048 lds
MAavv 188,56,128
MAavv 192,60,100
MAavv 196,60,104
MAavv 200,60,108
MAavv 204,60,112
MAavv 208,60,116
MAavv 212,60,120
buffer_load_dwordx4 v150, s[28:31], s50 offen offset:3072 lds
MAavv 216,60,124
MAavv 220,60,128
MAavv 224,64,100
MAavv 228,64,104
MAavv 232,64,108
MAavv 236,64,112
MAavv 240,64,116
MAavv 244,64,120
MAavv 248,64,124
MAavv 252,64,128
s_waitcnt vmcnt(16) lgkmcnt(0)
s_barrier
MAavv 0,4,68
Tv 100,132,16384
MAavv 4,4,72
Tv 102,132,18432
MAavv 8,4,76
Rv 36,141,0
s_add_u32 s65, s48, s49
s_add_u32 m0, s61, 4096
MAavv 12,4,80
Tv 104,133,16384
MAavv 16,4,84
Tv 106,133,18432
MAavv 20,4,88
Rv 40,141,2048
MAavv 24,4,92
Tv 108,134,16384
MAavv 28,4,96
Tv 110,134,18432
MAavv 32,8,68
Rv 44,141,4096
MAavv 36,8,72
Tv 112,135,16384
MAavv 40,8,76
Tv 114,135,18432
MAavv 44,8,80
Rv 48,141,6144
MAavv 48,8,84
buffer_load_dwordx4 v143, s[24:27], s65 offen lds
MAavv 52,8,88
Tv 116,136,16384
MAavv 56,8,92
Tv 118,136,18432
MAavv 60,8,96
Rv 52,141,8192
MAavv 64,12,68
Tv 120,137,16384
MAavv 68,12,72
Tv 122,137,18432
MAavv 72,12,76
Rv 56,141,10240
MAavv 76,12,80
Tv 124,138,16384
MAavv 80,12,84
Tv 126,138,18432
MAavv 84,12,88
Rv 60,141,12288
MAavv 88,12,92
Tv 128,139,16384
MAavv 92,12,96
Tv 130,139,18432
MAavv 96,16,68
Rv 64,141,14336
MAavv 100,16,72
buffer_load_dwordx4 v144, s[24:27], s65 offen offset:1024 lds
MAavv 104,16,76
buffer_load_dwordx4 v145, s[24:27], s65 offen offset:2048 lds
MAavv 108,16,80
buffer_load_dwordx4 v146, s[24:27], s65 offen offset:3072 lds
MAavv 112,16,84
s_add_u32 s50, s50, s51
s_cmp_eq_u32 s50, s53
s_cselect_b32 s50, 0, s50
MAavv 116,16,88
s_add_u32 s62, s62, 16384
s_and_b32 s62, s62, 0xffff
s_mov_b32 m0, s62
MAavv 120,16,92
buffer_load_dwordx4 v147, s[28:31], s50 offen lds
MAavv 124,16,96
s_waitcnt vmcnt(13) lgkmcnt(0)
s_barrier
buffer_load_dwordx4 v148, s[28:31], s50 offen offset:1024 lds
MAavv 0,36,100
s_add_u32 s63, s63, 32768
s_cmp_eq_u32 s63, 163840
s_cselect_b32 s63, 65536, s63
MAavv 4,36,104
v_add_u32 v142, s63, v140
v_xor_b32 v141, 64, v142
MAavv 8,36,108
buffer_load_dwordx4 v149, s[28:31], s50 offen offset:2048 lds
MAavv 12,36,112
buffer_load_dwordx4 v150, s[28:31], s50 offen offset:3072 lds
MAavv 16,36,116
v_xor_b32 v132, 0x8000, v132
v_xor_b32 v133, 0x8000, v133
MAavv 20,36,120
v_xor_b32 v134, 0x8000, v134
v_xor_b32 v135, 0x8000, v135
MAavv 24,36,124
v_xor_b32 v136, 0x8000, v136
v_xor_b32 v137, 0x8000, v137
MAavv 28,36,128
v_xor_b32 v138, 0x8000, v138
v_xor_b32 v139, 0x8000, v139
MAavv 32,40,100
s_add_u32 s48, s48, 128
s_cmp_eq_u32 s48, s52
s_cselect_b32 s48, 0, s48
MAavv 36,40,104
s_add_u32 s61, s61, 32768
s_cmp_ge_u32 s61, 163840
s_cselect_b32 s64, 98304, 0
MAavv 40,40,108
s_sub_u32 s61, s61, s64
s_mov_b32 m0, s61
Rv 4,142,0
MAavv 44,40,112
buffer_load_dwordx4 v143, s[24:27], s48 offen lds
Rv 8,142,2048
MAavv 48,40,116
buffer_load_dwordx4 v144, s[24:27], s48 offen offset:1024 lds
Rv 12,142,4096
MAavv 52,40,120
buffer_load_dwordx4 v145, s[24:27], s48 offen offset:2048 lds
Rv 16,142,6144
MAavv 56,40,124
buffer_load_dwordx4 v146, s[24:27], s48 offen offset:3072 lds
MAavv 60,40,128
s_add_u32 s50, s50, s51
s_cmp_eq_u32 s50, s53
s_cselect_b32 s50, 0, s50
MAavv 64,44,100
s_add_u32 s62, s62, 16384
s_and_b32 s62, s62, 0xffff
s_mov_b32 m0, s62
MAavv 68,44,104
buffer_load_dwordx4 v147, s[28:31], s50 offen lds
MAavv 72,44,108
buffer_load_dwordx4 v148, s[28:31], s50 offen offset:1024 lds
MAavv 76,44,112
buffer_load_dwordx4 v149, s[28:31], s50 offen offset:2048 lds
AR 172,0
AR 173,1
MAavv 80,44,116
buffer_load_dwordx4 v150, s[28:31], s50 offen offset:3072 lds
AR 174,2
AR 175,3
MAavv 84,44,120
AR 176,32
AR 177,33
AR 178,34
AR 179,35
MAavv 88,44,124
CV 204,172,173
CV 205,174,175
CV 206,176,177
CV 207,178,179
MAavv 92,44,128
s_mul_i32 s60, s71, 0
s_add_u32 s60, s60, s70
buffer_store_dwordx4 v[204:207], v159, s[44:47], s60 offen
AR 180,4
AR 181,5
MAavv 96,48,100
AR 182,6
AR 183,7
AR 184,36
AR 185,37
MAavv 100,48,104
AR 186,38
AR 187,39
CV 208,180,181
CV 209,182,183
MAavv 104,48,108
CV 210,184,185
CV 211,186,187
buffer_store_dwordx4 v[208:211], v159, s[44:47], s60 offen offset:1024
MAavv 108,48,112
AR 188,8
AR 189,9
AR 190,10
AR 191,11
MAavv 112,48,116
AR 192,40
AR 193,41
AR 194,42
AR 195,43
MAavv 116,48,120
CV 212,188,189
CV 213,190,191
CV 214,192,193
CV 215,194,195
MAavv 120,48,124
buffer_store_dwordx4 v[212:215], v159, s[44:47], s60 offen offset:2048
AR 196,12
AR 197,13
MAavv 124,48,128
AR 198,14
AR 199,15
AR 200,44
AR 201,45
MAavv 128,20,68
AR 202,46
AR 203,47
CV 216,196,197
CV 217,198,199
MAavv 132,20,72
CV 218,200,201
CV 219,202,203
buffer_store_dwordx4 v[216:219], v159, s[44:47], s60 offen offset:3072
MAavv 136,20,76
AR 172,16
AR 173,17
AR 174,18
AR 175,19
MAavv 140,20,80
AR 176,48
AR 177,49
AR 178,50
AR 179,51
MAavv 144,20,84
CV 220,172,173
CV 221,174,175
CV 222,176,177
CV 223,178,179
MAavv 148,20,88
s_mul_i32 s60, s71, 0
s_add_u32 s60, s60, s70
s_add_u32 s60, s60, 4096
buffer_store_dwordx4 v[220:223], v159, s[44:47], s60 offen
MAavv 152,20,92
AR 180,20
AR 181,21
AR 182,22
AR 183,23
MAavv 156,20,96
AR 184,52
AR 185,53
AR 186,54
AR 187,55
MAavv 160,24,68
CV 224,180,181
CV 225,182,183
CV 226,184,185
CV 227,186,187
MAavv 164,24,72
buffer_store_dwordx4 v[224:227], v159, s[44:47], s60 offen offset:1024
AR 188,24
AR 189,25
MAavv 168,24,76
AR 190,26
AR 191,27
AR 192,56
AR 193,57
MAavv 172,24,80
AR 194,58
AR 195,59
CV 228,188,189
CV 229,190,191
MAavv 176,24,84
CV 230,192,193
CV 231,194,195
buffer_store_dwordx4 v[228:231], v159, s[44:47], s60 offen offset:2048
MAavv 180,24,88
AR 196,28
AR 197,29
AR 198,30
AR 199,31
MAavv 184,24,92
AR 200,60
AR 201,61
AR 202,62
AR 203,63
MAavv 188,24,96
CV 232,196,197
CV 233,198,199
CV 234,200,201
CV 235,202,203
MAavv 192,28,68
buffer_store_dwordx4 v[232:235], v159, s[44:47], s60 offen offset:3072
AR 172,64
AR 173,65
MAavv 196,28,72
AR 174,66
AR 175,67
AR 176,96
AR 177,97
MAavv 200,28,76
AR 178,98
AR 179,99
CV 204,172,173
CV 205,174,175
MAavv 204,28,80
CV 206,176,177
CV 207,178,179
s_mul_i32 s60, s71, 1
s_add_u32 s60, s60, s70
buffer_store_dwordx4 v[204:207], v159, s[44:47], s60 offen
MAavv 208,28,84
AR 180,68
AR 181,69
AR 182,70
AR 183,71
MAavv 212,28,88
AR 184,100
AR 185,101
AR 186,102
AR 187,103
MAavv 216,28,92
CV 208,180,181
CV 209,182,183
CV 210,184,185
CV 211,186,187
MAavv 220,28,96
buffer_store_dwordx4 v[208:211], v159, s[44:47], s60 offen offset:1024
AR 188,72
AR 189,73
MAavv 224,32,68
AR 190,74
AR 191,75
AR 192,104
AR 193,105
MAavv 228,32,72
AR 194,106
AR 195,107
CV 212,188,189
CV 213,190,191
MAavv 232,32,76
CV 214,192,193
CV 215,194,195
buffer_store_dwordx4 v[212:215], v159, s[44:47], s60 offen offset:2048
MAavv 236,32,80
AR 196,76
AR 197,77
AR 198,78
AR 199,79
MAavv 240,32,84
AR 200,108
AR 201,109
AR 202,110
AR 203,111
MAavv 244,32,88
CV 216,196,197
CV 217,198,199
CV 218,200,201
CV 219,202,203
MAavv 248,32,92
buffer_store_dwordx4 v[216:219], v159, s[44:47], s60 offen offset:3072
AR 172,80
AR 173,81
MAavv 252,32,96
AR 174,82
AR 175,83
AR 176,112
AR 177,113
MAavv 128,52,100
Tv 68,132,0
AR 178,114
AR 179,115
MAavv 132,52,104
Tv 70,132,2048
CV 220,172,173
CV 221,174,175
MAavv 136,52,108
CV 222,176,177
CV 223,178,179
AR 180,84
AR 181,85
MAavv 140,52,112
Tv 72,133,0
s_mul_i32 s60, s71, 1
s_add_u32 s60, s60, s70
s_add_u32 s60, s60, 4096
buffer_store_dwordx4 v[220:223], v159, s[44:47], s60 offen
MAavv 144,52,116
Tv 74,133,2048
AR 182,86
AR 183,87
MAavv 148,52,120
AR 184,116
AR 185,117
AR 186,118
AR 187,119
MAavv 152,52,124
Tv 76,134,0
CV 224,180,181
CV 225,182,183
MAavv 156,52,128
Tv 78,134,2048
CV 226,184,185
CV 227,186,187
MAavv 160,56,100
buffer_store_dwordx4 v[224:227], v159, s[44:47], s60 offen offset:1024
AR 188,88
AR 189,89
MAavv 164,56,104
Tv 80,135,0
AR 190,90
AR 191,91
MAavv 168,56,108
Tv 82,135,2048
AR 192,120
AR 193,121
MAavv 172,56,112
AR 194,122
AR 195,123
CV 228,188,189
CV 229,190,191
MAavv 176,56,116
Tv 84,136,0
CV 230,192,193
CV 231,194,195
MAavv 180,56,120
Tv 86,136,2048
buffer_store_dwordx4 v[228:231], v159, s[44:47], s60 offen offset:2048
MAavv 184,56,124
Rv 20,142,8192
AR 196,92
AR 197,93
MAavv 188,56,128
Tv 88,137,0
AR 198,94
AR 199,95
MAavv 192,60,100
Tv 90,137,2048
AR 200,124
AR 201,125
MAavv 196,60,104
Rv 24,142,10240
AR 202,126
AR 203,127
MAavv 200,60,108
Tv 92,138,0
CV 232,196,197
CV 233,198,199
MAavv 204,60,112
Tv 94,138,2048
CV 234,200,201
CV 235,202,203
MAavv 208,60,116
Rv 28,142,12288
buffer_store_dwordx4 v[232:235], v159, s[44:47], s60 offen offset:3072
MAavv 212,60,120
Tv 96,139,0
AR 172,128
AR 173,129
MAavv 216,60,124
Tv 98,139,2048
AR 174,130
AR 175,131
MAavv 220,60,128
Rv 32,142,14336
AR 176,160
AR 177,161
MAavv 224,64,100
AR 178,162
AR 179,163
CV 204,172,173
CV 205,174,175
MAavv 228,64,104
CV 206,176,177
CV 207,178,179
s_mul_i32 s60, s71, 2
s_add_u32 s60, s60, s70
buffer_store_dwordx4 v[204:207], v159, s[44:47], s60 offen
MAavv 232,64,108
AR 180,132
AR 181,133
AR 182,134
AR 183,135
MAavv 236,64,112
AR 184,164
AR 185,165
AR 186,166
AR 187,167
MAavv 240,64,116
CV 208,180,181
CV 209,182,183
CV 210,184,185
CV 211,186,187
MAavv 244,64,120
buffer_store_dwordx4 v[208:211], v159, s[44:47], s60 offen offset:1024
AR 188,136
AR 189,137
MAavv 248,64,124
AR 190,138
AR 191,139
AR 192,168
AR 193,169
AR 194,170
AR 195,171
CV 212,188,189
CV 213,190,191
CV 214,192,193
CV 215,194,195
buffer_store_dwordx4 v[212:215], v159, s[44:47], s60 offen offset:2048
AR 196,140
AR 197,141
AR 198,142
AR 199,143
AR 200,172
AR 201,173
AR 202,174
AR 203,175
CV 216,196,197
CV 217,198,199
CV 218,200,201
CV 219,202,203
buffer_store_dwordx4 v[216:219], v159, s[44:47], s60 offen offset:3072
AR 172,144
AR 173,145
AR 174,146
AR 175,147
AR 176,176
AR 177,177
AR 178,178
AR 179,179
CV 220,172,173
CV 221,174,175
CV 222,176,177
CV 223,178,179
s_mul_i32 s60, s71, 2
s_add_u32 s60, s60, s70
s_add_u32 s60, s60, 4096
buffer_store_dwordx4 v[220:223], v159, s[44:47], s60 offen
AR 180,148
AR 181,149
AR 182,150
AR 183,151
AR 184,180
AR 185,181
AR 186,182
AR 187,183
CV 224,180,181
CV 225,182,183
CV 226,184,185
CV 227,186,187
buffer_store_dwordx4 v[224:227], v159, s[44:47], s60 offen offset:1024
AR 188,152
AR 189,153
AR 190,154
AR 191,155
AR 192,184
AR 193,185
AR 194,186
AR 195,187
CV 228,188,189
CV 229,190,191
CV 230,192,193
CV 231,194,195
buffer_store_dwordx4 v[228:231], v159, s[44:47], s60 offen offset:2048
AR 196,156
AR 197,157
AR 198,158
AR 199,159
AR 200,188
AR 201,189
AR 202,190
AR 203,191
CV 232,196,197
CV 233,198,199
CV 234,200,201
CV 235,202,203
buffer_store_dwordx4 v[232:235], v159, s[44:47], s60 offen offset:3072
MAavv 252,64,128
AR 172,192
AR 173,193
AR 174,194
AR 175,195
AR 176,224
AR 177,225
AR 178,226
AR 179,227
CV 204,172,173
CV 205,174,175
CV 206,176,177
CV 207,178,179
s_mul_i32 s60, s71, 3
s_add_u32 s60, s60, s70
buffer_store_dwordx4 v[204:207], v159, s[44:47], s60 offen
AR 180,196
AR 181,197
AR 182,198
AR 183,199
AR 184,228
AR 185,229
AR 186,230
AR 187,231
CV 208,180,181
CV 209,182,183
CV 210,184,185
CV 211,186,187
buffer_store_dwordx4 v[208:211], v159, s[44:47], s60 offen offset:1024
AR 188,200
AR 189,201
AR 190,202
AR 191,203
AR 192,232
AR 193,233
AR 194,234
AR 195,235
CV 212,188,189
CV 213,190,191
CV 214,192,193
CV 215,194,195
buffer_store_dwordx4 v[212:215], v159, s[44:47], s60 offen offset:2048
AR 196,204
AR 197,205
AR 198,206
AR 199,207
AR 200,236
AR 201,237
AR 202,238
AR 203,239
CV 216,196,197
CV 217,198,199
CV 218,200,201
CV 219,202,203
buffer_store_dwordx4 v[216:219], v159, s[44:47], s60 offen offset:3072
AR 172,208
AR 173,209
AR 174,210
AR 175,211
AR 176,240
AR 177,241
AR 178,242
AR 179,243
CV 220,172,173
CV 221,174,175
CV 222,176,177
CV 223,178,179
s_mul_i32 s60, s71, 3
s_add_u32 s60, s60, s70
s_add_u32 s60, s60, 4096
buffer_store_dwordx4 v[220:223], v159, s[44:47], s60 offen
AR 180,212
AR 181,213
AR 182,214
AR 183,215
AR 184,244
AR 185,245
AR 186,246
AR 187,247
CV 224,180,181
CV 225,182,183
CV 226,184,185
CV 227,186,187
buffer_store_dwordx4 v[224:227], v159, s[44:47], s60 offen offset:1024
AR 188,216
AR 189,217
AR 190,218
AR 191,219
AR 192,248
AR 193,249
AR 194,250
AR 195,251
CV 228,188,189
CV 229,190,191
CV 230,192,193
CV 231,194,195
buffer_store_dwordx4 v[228:231], v159, s[44:47], s60 offen offset:2048
AR 196,220
AR 197,221
AR 198,222
AR 199,223
AR 200,252
AR 201,253
AR 202,254
AR 203,255
CV 232,196,197
CV 233,198,199
CV 234,200,201
CV 235,202,203
buffer_store_dwordx4 v[232:235], v159, s[44:47], s60 offen offset:3072
s_mov_b32 s38, 0
s_mov_b32 s42, 0
Lk1_u2:
s_cmp_eq_u32 s75, 1
s_cbranch_scc1 Lk1_u2V
s_cmp_lg_u32 s76, 0
s_cbranch_scc1 Lw1
s_waitcnt vmcnt(16)
Lw1:
s_waitcnt vmcnt(48) lgkmcnt(0)
s_barrier
MZavv 0,68,4
Tv 100,132,16384
MZavv 4,72,4
Tv 102,132,18432
MZavv 8,76,4
s_add_u32 s65, s48, s49
s_add_u32 m0, s61, 4096
MZavv 12,80,4
Rv 36,141,0
MZavv 16,84,4
buffer_load_dwordx4 v143, s[24:27], s65 offen lds
MZavv 20,88,4
Tv 104,133,16384
MZavv 24,92,4
MZavv 28,96,4
Tv 106,133,18432
MZavv 32,68,8
Rv 40,141,2048
MZavv 36,72,8
MZavv 40,76,8
Tv 108,134,16384
MZavv 44,80,8
buffer_load_dwordx4 v144, s[24:27], s65 offen offset:1024 lds
MZavv 48,84,8
Tv 110,134,18432
MZavv 52,88,8
MZavv 56,92,8
Rv 44,141,4096
MZavv 60,96,8
Tv 112,135,16384
MZavv 64,68,12
MZavv 68,72,12
Tv 114,135,18432
MZavv 72,76,12
buffer_load_dwordx4 v145, s[24:27], s65 offen offset:2048 lds
MZavv 76,80,12
Rv 48,141,6144
MZavv 80,84,12
MZavv 84,88,12
Tv 116,136,16384
MZavv 88,92,12
Tv 118,136,18432
MZavv 92,96,12
MZavv 96,68,16
Rv 52,141,8192
MZavv 100,72,16
buffer_load_dwordx4 v146, s[24:27], s65 offen offset:3072 lds
MZavv 104,76,16
Tv 120,137,16384
MZavv 108,80,16
s_add_u32 s50, s50, s51
s_cmp_eq_u32 s50, s53
s_cselect_b32 s50, 0, s50
MZavv 112,84,16
Tv 122,137,18432
MZavv 116,88,16
Rv 56,141,10240
MZavv 120,92,16
s_add_u32 s62, s62, 16384
s_and_b32 s62, s62, 0xffff
s_mov_b32 m0, s62
MZavv 124,96,16
Tv 124,138,16384
MZavv 128,68,20
buffer_load_dwordx4 v147, s[28:31], s50 offen lds
MZavv 132,72,20
Tv 126,138,18432
MZavv 136,76,20
MZavv 140,80,20
Rv 60,141,12288
MZavv 144,84,20
Tv 128,139,16384
MZavv 148,88,20
MZavv 152,92,20
Tv 130,139,18432
MZavv 156,96,20
buffer_load_dwordx4 v148, s[28:31], s50 offen offset:1024 lds
MZavv 160,68,24
Rv 64,141,14336
MZavv 164,72,24
MZavv 168,76,24
MZavv 172,80,24
MZavv 176,84,24
MZavv 180,88,24
MZavv 184,92,24
buffer_load_dwordx4 v149, s[28:31], s50 offen offset:2048 lds
MZavv 188,96,24
MZavv 192,68,28
MZavv 196,72,28
MZavv 200,76,28
MZavv 204,80,28
MZavv 208,84,28
MZavv 212,88,28
buffer_load_dwordx4 v150, s[28:31], s50 offen offset:3072 lds
MZavv 216,92,28
MZavv 220,96,28
MZavv 224,68,32
MZavv 228,72,32
MZavv 232,76,32
MZavv 236,80,32
MZavv 240,84,32
MZavv 244,88,32
MZavv 248,92,32
MZavv 252,96,32
s_cmp_lg_u32 s76, 0
s_cbranch_scc1 Lw2
s_waitcnt vmcnt(16)
Lw2:
s_waitcnt vmcnt(48) lgkmcnt(0)
s_barrier
MAavv 0,100,36
s_add_u32 s63, s63, 32768
s_cmp_eq_u32 s63, 163840
s_cselect_b32 s63, 65536, s63
MAavv 4,104,36
v_add_u32 v142, s63, v140
v_xor_b32 v141, 64, v142
MAavv 8,108,36
v_xor_b32 v132, 0x8000, v132
v_xor_b32 v133, 0x8000, v133
MAavv 12,112,36
v_xor_b32 v134, 0x8000, v134
v_xor_b32 v135, 0x8000, v135
MAavv 16,116,36
v_xor_b32 v136, 0x8000, v136
v_xor_b32 v137, 0x8000, v137
MAavv 20,120,36
v_xor_b32 v138, 0x8000, v138
v_xor_b32 v139, 0x8000, v139
MAavv 24,124,36
Tv 68,132,0
MAavv 28,128,36
Tv 70,132,2048
MAavv 32,100,40
Rv 4,142,0
MAavv 36,104,40
Tv 72,133,0
MAavv 40,108,40
Tv 74,133,2048
MAavv 44,112,40
Rv 8,142,2048
MAavv 48,116,40
s_add_u32 s48, s48, 128
s_cmp_eq_u32 s48, s52
s_cselect_b32 s48, 0, s48
MAavv 52,120,40
Tv 76,134,0
MAavv 56,124,40
s_add_u32 s61, s61, 32768
s_cmp_ge_u32 s61, 163840
s_cselect_b32 s64, 98304, 0
MAavv 60,128,40
Tv 78,134,2048
s_sub_u32 s61, s61, s64
s_mov_b32 m0, s61
MAavv 64,100,44
buffer_load_dwordx4 v143, s[24:27], s48 offen lds
MAavv 68,104,44
Rv 12,142,4096
MAavv 72,108,44
Tv 80,135,0
MAavv 76,112,44
buffer_load_dwordx4 v144, s[24:27], s48 offen offset:1024 lds
MAavv 80,116,44
Tv 82,135,2048
MAavv 84,120,44
buffer_load_dwordx4 v145, s[24:27], s48 offen offset:2048 lds
MAavv 88,124,44
Rv 16,142,6144
MAavv 92,128,44
MAavv 96,100,48
Tv 84,136,0
MAavv 100,104,48
Tv 86,136,2048
MAavv 104,108,48
buffer_load_dwordx4 v146, s[24:27], s48 offen offset:3072 lds
MAavv 108,112,48
Rv 20,142,8192
MAavv 112,116,48
s_add_u32 s50, s50, s51
s_cmp_eq_u32 s50, s53
s_cselect_b32 s50, 0, s50
MAavv 116,120,48
Tv 88,137,0
MAavv 120,124,48
s_add_u32 s62, s62, 16384
s_and_b32 s62, s62, 0xffff
s_mov_b32 m0, s62
MAavv 124,128,48
Tv 90,137,2048
MAavv 128,100,52
Rv 24,142,10240
MAavv 132,104,52
buffer_load_dwordx4 v147, s[28:31], s50 offen lds
MAavv 136,108,52
Tv 92,138,0
MAavv 140,112,52
MAavv 144,116,52
Tv 94,138,2048
MAavv 148,120,52
MAavv 152,124,52
Rv 28,142,12288
MAavv 156,128,52
Tv 96,139,0
MAavv 160,100,56
buffer_load_dwordx4 v148, s[28:31], s50 offen offset:1024 lds
MAavv 164,104,56
Tv 98,139,2048
MAavv 168,108,56
MAavv 172,112,56
Rv 32,142,14336
MAavv 176,116,56
MAavv 180,120,56
MAavv 184,124,56
buffer_load_dwordx4 v149, s[28:31], s50 offen offset:2048 lds
MAavv 188,128,56
MAavv 192,100,60
MAavv 196,104,60
MAavv 200,108,60
MAavv 204,112,60
MAavv 208,116,60
MAavv 212,120,60
buffer_load_dwordx4 v150, s[28:31], s50 offen offset:3072 lds
MAavv 216,124,60
MAavv 220,128,60
MAavv 224,100,64
MAavv 228,104,64
MAavv 232,108,64
MAavv 236,112,64
MAavv 240,116,64
MAavv 244,120,64
MAavv 248,124,64
MAavv 252,128,64
s_cmp_lg_u32 s76, 0
s_cbranch_scc1 Lw3
s_waitcnt vmcnt(16)
Lw3:
s_waitcnt vmcnt(48) lgkmcnt(0)
s_barrier
MAavv 0,68,4
Tv 100,132,16384
MAavv 4,72,4
Tv 102,132,18432
MAavv 8,76,4
s_add_u32 s65, s48, s49
s_add_u32 m0, s61, 4096
MAavv 12,80,4
Rv 36,141,0
MAavv 16,84,4
buffer_load_dwordx4 v143, s[24:27], s65 offen lds
MAavv 20,88,4
Tv 104,133,16384
MAavv 24,92,4
MAavv 28,96,4
Tv 106,133,18432
MAavv 32,68,8
Rv 40,141,2048
MAavv 36,72,8
MAavv 40,76,8
Tv 108,134,16384
MAavv 44,80,8
buffer_load_dwordx4 v144, s[24:27], s65 offen offset:1024 lds
MAavv 48,84,8
Tv 110,134,18432
MAavv 52,88,8
MAavv 56,92,8
Rv 44,141,4096
MAavv 60,96,8
Tv 112,135,16384
MAavv 64,68,12
MAavv 68,72,12
Tv 114,135,18432
MAavv 72,76,12
buffer_load_dwordx4 v145, s[24:27], s65 offen offset:2048 lds
MAavv 76,80,12
Rv 48,141,6144
MAavv 80,84,12
MAavv 84,88,12
Tv 116,136,16384
MAavv 88,92,12
Tv 118,136,18432
MAavv 92,96,12
MAavv 96,68,16
Rv 52,141,8192
MAavv 100,72,16
buffer_load_dwordx4 v146, s[24:27], s65 offen offset:3072 lds
MAavv 104,76,16
Tv 120,137,16384
MAavv 108,80,16
s_add_u32 s50, s50, s51
s_cmp_eq_u32 s50, s53
s_cselect_b32 s50, 0, s50
MAavv 112,84,16
Tv 122,137,18432
MAavv 116,88,16
Rv 56,141,10240
MAavv 120,92,16
s_add_u32 s62, s62, 16384
s_and_b32 s62, s62, 0xffff
s_mov_b32 m0, s62
MAavv 124,96,16
Tv 124,138,16384
MAavv 128,68,20
buffer_load_dwordx4 v147, s[28:31], s50 offen lds
MAavv 132,72,20
Tv 126,138,18432
MAavv 136,76,20
MAavv 140,80,20
Rv 60,141,12288
MAavv 144,84,20
Tv 128,139,16384
MAavv 148,88,20
MAavv 152,92,20
Tv 130,139,18432
MAavv 156,96,20
buffer_load_dwordx4 v148, s[28:31], s50 offen offset:1024 lds
MAavv 160,68,24
Rv 64,141,14336
MAavv 164,72,24
MAavv 168,76,24
MAavv 172,80,24
MAavv 176,84,24
MAavv 180,88,24
MAavv 184,92,24
buffer_load_dwordx4 v149, s[28:31], s50 offen offset:2048 lds
MAavv 188,96,24
MAavv 192,68,28
MAavv 196,72,28
MAavv 200,76,28
MAavv 204,80,28
MAavv 208,84,28
MAavv 212,88,28
buffer_load_dwordx4 v150, s[28:31], s50 offen offset:3072 lds
MAavv 216,92,28
MAavv 220,96,28
MAavv 224,68,32
MAavv 228,72,32
MAavv 232,76,32
MAavv 236,80,32
MAavv 240,84,32
MAavv 244,88,32
MAavv 248,92,32
MAavv 252,96,32
s_waitcnt vmcnt(16) lgkmcnt(0)
s_barrier
MAavv 0,100,36
s_add_u32 s63, s63, 32768
s_cmp_eq_u32 s63, 163840
s_cselect_b32 s63, 65536, s63
MAavv 4,104,36
v_add_u32 v142, s63, v140
v_xor_b32 v141, 64, v142
MAavv 8,108,36
v_xor_b32 v132, 0x8000, v132
v_xor_b32 v133, 0x8000, v133
MAavv 12,112,36
v_xor_b32 v134, 0x8000, v134
v_xor_b32 v135, 0x8000, v135
MAavv 16,116,36
v_xor_b32 v136, 0x8000, v136
v_xor_b32 v137, 0x8000, v137
MAavv 20,120,36
v_xor_b32 v138, 0x8000, v138
v_xor_b32 v139, 0x8000, v139
MAavv 24,124,36
Tv 68,132,0
MAavv 28,128,36
Tv 70,132,2048
MAavv 32,100,40
Rv 4,142,0
MAavv 36,104,40
Tv 72,133,0
MAavv 40,108,40
Tv 74,133,2048
MAavv 44,112,40
Rv 8,142,2048
MAavv 48,116,40
s_add_u32 s48, s48, 128
s_cmp_eq_u32 s48, s52
s_cselect_b32 s48, 0, s48
MAavv 52,120,40
Tv 76,134,0
MAavv 56,124,40
s_add_u32 s61, s61, 32768
s_cmp_ge_u32 s61, 163840
s_cselect_b32 s64, 98304, 0
MAavv 60,128,40
Tv 78,134,2048
s_sub_u32 s61, s61, s64
s_mov_b32 m0, s61
MAavv 64,100,44
buffer_load_dwordx4 v143, s[24:27], s48 offen lds
MAavv 68,104,44
Rv 12,142,4096
MAavv 72,108,44
Tv 80,135,0
MAavv 76,112,44
buffer_load_dwordx4 v144, s[24:27], s48 offen offset:1024 lds
MAavv 80,116,44
Tv 82,135,2048
MAavv 84,120,44
buffer_load_dwordx4 v145, s[24:27], s48 offen offset:2048 lds
)ASM"
R"ASM(MAavv 88,124,44
Rv 16,142,6144
MAavv 92,128,44
MAavv 96,100,48
Tv 84,136,0
MAavv 100,104,48
Tv 86,136,2048
MAavv 104,108,48
buffer_load_dwordx4 v146, s[24:27], s48 offen offset:3072 lds
MAavv 108,112,48
Rv 20,142,8192
MAavv 112,116,48
s_add_u32 s50, s50, s51
s_cmp_eq_u32 s50, s53
s_cselect_b32 s50, 0, s50
MAavv 116,120,48
Tv 88,137,0
MAavv 120,124,48
s_add_u32 s62, s62, 16384
s_and_b32 s62, s62, 0xffff
s_mov_b32 m0, s62
MAavv 124,128,48
Tv 90,137,2048
MAavv 128,100,52
Rv 24,142,10240
MAavv 132,104,52
buffer_load_dwordx4 v147, s[28:31], s50 offen lds
MAavv 136,108,52
Tv 92,138,0
MAavv 140,112,52
MAavv 144,116,52
Tv 94,138,2048
MAavv 148,120,52
MAavv 152,124,52
Rv 28,142,12288
MAavv 156,128,52
Tv 96,139,0
MAavv 160,100,56
buffer_load_dwordx4 v148, s[28:31], s50 offen offset:1024 lds
MAavv 164,104,56
Tv 98,139,2048
MAavv 168,108,56
MAavv 172,112,56
Rv 32,142,14336
MAavv 176,116,56
MAavv 180,120,56
MAavv 184,124,56
buffer_load_dwordx4 v149, s[28:31], s50 offen offset:2048 lds
MAavv 188,128,56
MAavv 192,100,60
MAavv 196,104,60
MAavv 200,108,60
MAavv 204,112,60
MAavv 208,116,60
MAavv 212,120,60
buffer_load_dwordx4 v150, s[28:31], s50 offen offset:3072 lds
MAavv 216,124,60
MAavv 220,128,60
MAavv 224,100,64
MAavv 228,104,64
MAavv 232,108,64
MAavv 236,112,64
MAavv 240,116,64
MAavv 244,120,64
MAavv 248,124,64
MAavv 252,128,64
s_waitcnt vmcnt(16) lgkmcnt(0)
s_barrier
MAavv 0,68,4
Tv 100,132,16384
MAavv 4,72,4
Tv 102,132,18432
MAavv 8,76,4
s_add_u32 s65, s48, s49
s_add_u32 m0, s61, 4096
MAavv 12,80,4
Rv 36,141,0
MAavv 16,84,4
buffer_load_dwordx4 v143, s[24:27], s65 offen lds
MAavv 20,88,4
Tv 104,133,16384
MAavv 24,92,4
MAavv 28,96,4
Tv 106,133,18432
MAavv 32,68,8
Rv 40,141,2048
MAavv 36,72,8
MAavv 40,76,8
Tv 108,134,16384
MAavv 44,80,8
buffer_load_dwordx4 v144, s[24:27], s65 offen offset:1024 lds
MAavv 48,84,8
Tv 110,134,18432
MAavv 52,88,8
MAavv 56,92,8
Rv 44,141,4096
MAavv 60,96,8
Tv 112,135,16384
MAavv 64,68,12
MAavv 68,72,12
Tv 114,135,18432
MAavv 72,76,12
buffer_load_dwordx4 v145, s[24:27], s65 offen offset:2048 lds
MAavv 76,80,12
Rv 48,141,6144
MAavv 80,84,12
MAavv 84,88,12
Tv 116,136,16384
MAavv 88,92,12
Tv 118,136,18432
MAavv 92,96,12
MAavv 96,68,16
Rv 52,141,8192
MAavv 100,72,16
buffer_load_dwordx4 v146, s[24:27], s65 offen offset:3072 lds
MAavv 104,76,16
Tv 120,137,16384
MAavv 108,80,16
s_add_u32 s50, s50, s51
s_cmp_eq_u32 s50, s53
s_cselect_b32 s50, 0, s50
MAavv 112,84,16
Tv 122,137,18432
MAavv 116,88,16
Rv 56,141,10240
MAavv 120,92,16
s_add_u32 s62, s62, 16384
s_and_b32 s62, s62, 0xffff
s_mov_b32 m0, s62
MAavv 124,96,16
Tv 124,138,16384
MAavv 128,68,20
buffer_load_dwordx4 v147, s[28:31], s50 offen lds
MAavv 132,72,20
Tv 126,138,18432
MAavv 136,76,20
MAavv 140,80,20
Rv 60,141,12288
MAavv 144,84,20
Tv 128,139,16384
MAavv 148,88,20
MAavv 152,92,20
Tv 130,139,18432
MAavv 156,96,20
buffer_load_dwordx4 v148, s[28:31], s50 offen offset:1024 lds
MAavv 160,68,24
Rv 64,141,14336
MAavv 164,72,24
MAavv 168,76,24
MAavv 172,80,24
MAavv 176,84,24
MAavv 180,88,24
MAavv 184,92,24
buffer_load_dwordx4 v149, s[28:31], s50 offen offset:2048 lds
MAavv 188,96,24
MAavv 192,68,28
MAavv 196,72,28
MAavv 200,76,28
MAavv 204,80,28
MAavv 208,84,28
MAavv 212,88,28
buffer_load_dwordx4 v150, s[28:31], s50 offen offset:3072 lds
MAavv 216,92,28
MAavv 220,96,28
MAavv 224,68,32
MAavv 228,72,32
MAavv 232,76,32
MAavv 236,80,32
MAavv 240,84,32
MAavv 244,88,32
MAavv 248,92,32
MAavv 252,96,32
s_waitcnt vmcnt(16) lgkmcnt(0)
s_barrier
MAavv 0,100,36
s_add_u32 s63, s63, 32768
s_cmp_eq_u32 s63, 163840
s_cselect_b32 s63, 65536, s63
MAavv 4,104,36
v_add_u32 v142, s63, v140
v_xor_b32 v141, 64, v142
MAavv 8,108,36
v_xor_b32 v132, 0x8000, v132
v_xor_b32 v133, 0x8000, v133
MAavv 12,112,36
v_xor_b32 v134, 0x8000, v134
v_xor_b32 v135, 0x8000, v135
MAavv 16,116,36
v_xor_b32 v136, 0x8000, v136
v_xor_b32 v137, 0x8000, v137
MAavv 20,120,36
v_xor_b32 v138, 0x8000, v138
v_xor_b32 v139, 0x8000, v139
MAavv 24,124,36
Tv 68,132,0
MAavv 28,128,36
Tv 70,132,2048
MAavv 32,100,40
Rv 4,142,0
MAavv 36,104,40
Tv 72,133,0
MAavv 40,108,40
Tv 74,133,2048
MAavv 44,112,40
Rv 8,142,2048
MAavv 48,116,40
s_add_u32 s48, s48, 128
s_cmp_eq_u32 s48, s52
s_cselect_b32 s48, 0, s48
MAavv 52,120,40
Tv 76,134,0
MAavv 56,124,40
s_add_u32 s61, s61, 32768
s_cmp_ge_u32 s61, 163840
s_cselect_b32 s64, 98304, 0
MAavv 60,128,40
Tv 78,134,2048
s_sub_u32 s61, s61, s64
s_mov_b32 m0, s61
MAavv 64,100,44
buffer_load_dwordx4 v143, s[24:27], s48 offen lds
MAavv 68,104,44
Rv 12,142,4096
MAavv 72,108,44
Tv 80,135,0
MAavv 76,112,44
buffer_load_dwordx4 v144, s[24:27], s48 offen offset:1024 lds
MAavv 80,116,44
Tv 82,135,2048
MAavv 84,120,44
buffer_load_dwordx4 v145, s[24:27], s48 offen offset:2048 lds
MAavv 88,124,44
Rv 16,142,6144
MAavv 92,128,44
MAavv 96,100,48
Tv 84,136,0
MAavv 100,104,48
Tv 86,136,2048
MAavv 104,108,48
buffer_load_dwordx4 v146, s[24:27], s48 offen offset:3072 lds
MAavv 108,112,48
Rv 20,142,8192
MAavv 112,116,48
s_add_u32 s50, s50, s51
s_cmp_eq_u32 s50, s53
s_cselect_b32 s50, 0, s50
MAavv 116,120,48
Tv 88,137,0
MAavv 120,124,48
s_add_u32 s62, s62, 16384
s_and_b32 s62, s62, 0xffff
s_mov_b32 m0, s62
MAavv 124,128,48
Tv 90,137,2048
MAavv 128,100,52
Rv 24,142,10240
MAavv 132,104,52
buffer_load_dwordx4 v147, s[28:31], s50 offen lds
MAavv 136,108,52
Tv 92,138,0
MAavv 140,112,52
MAavv 144,116,52
Tv 94,138,2048
MAavv 148,120,52
MAavv 152,124,52
Rv 28,142,12288
MAavv 156,128,52
Tv 96,139,0
MAavv 160,100,56
buffer_load_dwordx4 v148, s[28:31], s50 offen offset:1024 lds
MAavv 164,104,56
Tv 98,139,2048
MAavv 168,108,56
MAavv 172,112,56
Rv 32,142,14336
MAavv 176,116,56
MAavv 180,120,56
MAavv 184,124,56
buffer_load_dwordx4 v149, s[28:31], s50 offen offset:2048 lds
MAavv 188,128,56
MAavv 192,100,60
MAavv 196,104,60
MAavv 200,108,60
MAavv 204,112,60
MAavv 208,116,60
MAavv 212,120,60
buffer_load_dwordx4 v150, s[28:31], s50 offen offset:3072 lds
MAavv 216,124,60
MAavv 220,128,60
MAavv 224,100,64
MAavv 228,104,64
MAavv 232,108,64
MAavv 236,112,64
MAavv 240,116,64
MAavv 244,120,64
MAavv 248,124,64
MAavv 252,128,64
s_mov_b32 s67, 5
Lu2K_loop:
s_waitcnt vmcnt(16) lgkmcnt(0)
s_barrier
MAavv 0,68,4
Tv 100,132,16384
MAavv 4,72,4
Tv 102,132,18432
MAavv 8,76,4
s_add_u32 s65, s48, s49
s_add_u32 m0, s61, 4096
MAavv 12,80,4
Rv 36,141,0
MAavv 16,84,4
buffer_load_dwordx4 v143, s[24:27], s65 offen lds
MAavv 20,88,4
Tv 104,133,16384
MAavv 24,92,4
MAavv 28,96,4
Tv 106,133,18432
MAavv 32,68,8
Rv 40,141,2048
MAavv 36,72,8
MAavv 40,76,8
Tv 108,134,16384
MAavv 44,80,8
buffer_load_dwordx4 v144, s[24:27], s65 offen offset:1024 lds
MAavv 48,84,8
Tv 110,134,18432
MAavv 52,88,8
MAavv 56,92,8
Rv 44,141,4096
MAavv 60,96,8
Tv 112,135,16384
MAavv 64,68,12
MAavv 68,72,12
Tv 114,135,18432
MAavv 72,76,12
buffer_load_dwordx4 v145, s[24:27], s65 offen offset:2048 lds
MAavv 76,80,12
Rv 48,141,6144
MAavv 80,84,12
MAavv 84,88,12
Tv 116,136,16384
MAavv 88,92,12
Tv 118,136,18432
MAavv 92,96,12
MAavv 96,68,16
Rv 52,141,8192
MAavv 100,72,16
buffer_load_dwordx4 v146, s[24:27], s65 offen offset:3072 lds
MAavv 104,76,16
Tv 120,137,16384
MAavv 108,80,16
s_add_u32 s50, s50, s51
s_cmp_eq_u32 s50, s53
s_cselect_b32 s50, 0, s50
MAavv 112,84,16
Tv 122,137,18432
MAavv 116,88,16
Rv 56,141,10240
MAavv 120,92,16
s_add_u32 s62, s62, 16384
s_and_b32 s62, s62, 0xffff
s_mov_b32 m0, s62
MAavv 124,96,16
Tv 124,138,16384
MAavv 128,68,20
buffer_load_dwordx4 v147, s[28:31], s50 offen lds
MAavv 132,72,20
Tv 126,138,18432
MAavv 136,76,20
MAavv 140,80,20
Rv 60,141,12288
MAavv 144,84,20
Tv 128,139,16384
MAavv 148,88,20
MAavv 152,92,20
Tv 130,139,18432
MAavv 156,96,20
buffer_load_dwordx4 v148, s[28:31], s50 offen offset:1024 lds
MAavv 160,68,24
Rv 64,141,14336
MAavv 164,72,24
MAavv 168,76,24
MAavv 172,80,24
MAavv 176,84,24
MAavv 180,88,24
MAavv 184,92,24
buffer_load_dwordx4 v149, s[28:31], s50 offen offset:2048 lds
MAavv 188,96,24
MAavv 192,68,28
MAavv 196,72,28
MAavv 200,76,28
MAavv 204,80,28
MAavv 208,84,28
MAavv 212,88,28
buffer_load_dwordx4 v150, s[28:31], s50 offen offset:3072 lds
MAavv 216,92,28
MAavv 220,96,28
MAavv 224,68,32
MAavv 228,72,32
MAavv 232,76,32
MAavv 236,80,32
MAavv 240,84,32
MAavv 244,88,32
MAavv 248,92,32
MAavv 252,96,32
s_waitcnt vmcnt(16) lgkmcnt(0)
s_barrier
MAavv 0,100,36
s_add_u32 s63, s63, 32768
s_cmp_eq_u32 s63, 163840
s_cselect_b32 s63, 65536, s63
MAavv 4,104,36
v_add_u32 v142, s63, v140
v_xor_b32 v141, 64, v142
MAavv 8,108,36
v_xor_b32 v132, 0x8000, v132
v_xor_b32 v133, 0x8000, v133
MAavv 12,112,36
v_xor_b32 v134, 0x8000, v134
v_xor_b32 v135, 0x8000, v135
MAavv 16,116,36
v_xor_b32 v136, 0x8000, v136
v_xor_b32 v137, 0x8000, v137
MAavv 20,120,36
v_xor_b32 v138, 0x8000, v138
v_xor_b32 v139, 0x8000, v139
MAavv 24,124,36
Tv 68,132,0
MAavv 28,128,36
Tv 70,132,2048
MAavv 32,100,40
Rv 4,142,0
MAavv 36,104,40
Tv 72,133,0
MAavv 40,108,40
Tv 74,133,2048
MAavv 44,112,40
Rv 8,142,2048
MAavv 48,116,40
s_add_u32 s48, s48, 128
s_cmp_eq_u32 s48, s52
s_cselect_b32 s48, 0, s48
MAavv 52,120,40
Tv 76,134,0
MAavv 56,124,40
s_add_u32 s61, s61, 32768
s_cmp_ge_u32 s61, 163840
s_cselect_b32 s64, 98304, 0
MAavv 60,128,40
Tv 78,134,2048
s_sub_u32 s61, s61, s64
s_mov_b32 m0, s61
MAavv 64,100,44
buffer_load_dwordx4 v143, s[24:27], s48 offen lds
MAavv 68,104,44
Rv 12,142,4096
MAavv 72,108,44
Tv 80,135,0
MAavv 76,112,44
buffer_load_dwordx4 v144, s[24:27], s48 offen offset:1024 lds
MAavv 80,116,44
Tv 82,135,2048
MAavv 84,120,44
buffer_load_dwordx4 v145, s[24:27], s48 offen offset:2048 lds
MAavv 88,124,44
Rv 16,142,6144
MAavv 92,128,44
MAavv 96,100,48
Tv 84,136,0
MAavv 100,104,48
Tv 86,136,2048
MAavv 104,108,48
buffer_load_dwordx4 v146, s[24:27], s48 offen offset:3072 lds
MAavv 108,112,48
Rv 20,142,8192
MAavv 112,116,48
s_add_u32 s50, s50, s51
s_cmp_eq_u32 s50, s53
s_cselect_b32 s50, 0, s50
MAavv 116,120,48
Tv 88,137,0
MAavv 120,124,48
s_add_u32 s62, s62, 16384
s_and_b32 s62, s62, 0xffff
s_mov_b32 m0, s62
MAavv 124,128,48
Tv 90,137,2048
MAavv 128,100,52
Rv 24,142,10240
MAavv 132,104,52
buffer_load_dwordx4 v147, s[28:31], s50 offen lds
MAavv 136,108,52
Tv 92,138,0
MAavv 140,112,52
MAavv 144,116,52
Tv 94,138,2048
MAavv 148,120,52
MAavv 152,124,52
Rv 28,142,12288
MAavv 156,128,52
Tv 96,139,0
MAavv 160,100,56
buffer_load_dwordx4 v148, s[28:31], s50 offen offset:1024 lds
MAavv 164,104,56
Tv 98,139,2048
MAavv 168,108,56
MAavv 172,112,56
Rv 32,142,14336
MAavv 176,116,56
MAavv 180,120,56
MAavv 184,124,56
buffer_load_dwordx4 v149, s[28:31], s50 offen offset:2048 lds
MAavv 188,128,56
MAavv 192,100,60
MAavv 196,104,60
MAavv 200,108,60
MAavv 204,112,60
MAavv 208,116,60
MAavv 212,120,60
buffer_load_dwordx4 v150, s[28:31], s50 offen offset:3072 lds
MAavv 216,124,60
MAavv 220,128,60
MAavv 224,100,64
MAavv 228,104,64
MAavv 232,108,64
MAavv 236,112,64
MAavv 240,116,64
MAavv 244,120,64
MAavv 248,124,64
MAavv 252,128,64
s_waitcnt vmcnt(16) lgkmcnt(0)
s_barrier
MAavv 0,68,4
Tv 100,132,16384
MAavv 4,72,4
Tv 102,132,18432
MAavv 8,76,4
s_add_u32 s65, s48, s49
s_add_u32 m0, s61, 4096
MAavv 12,80,4
Rv 36,141,0
MAavv 16,84,4
buffer_load_dwordx4 v143, s[24:27], s65 offen lds
MAavv 20,88,4
Tv 104,133,16384
MAavv 24,92,4
MAavv 28,96,4
Tv 106,133,18432
MAavv 32,68,8
Rv 40,141,2048
MAavv 36,72,8
MAavv 40,76,8
Tv 108,134,16384
MAavv 44,80,8
buffer_load_dwordx4 v144, s[24:27], s65 offen offset:1024 lds
MAavv 48,84,8
Tv 110,134,18432
MAavv 52,88,8
MAavv 56,92,8
Rv 44,141,4096
MAavv 60,96,8
Tv 112,135,16384
MAavv 64,68,12
MAavv 68,72,12
Tv 114,135,18432
MAavv 72,76,12
buffer_load_dwordx4 v145, s[24:27], s65 offen offset:2048 lds
MAavv 76,80,12
Rv 48,141,6144
MAavv 80,84,12
MAavv 84,88,12
Tv 116,136,16384
MAavv 88,92,12
Tv 118,136,18432
MAavv 92,96,12
MAavv 96,68,16
Rv 52,141,8192
MAavv 100,72,16
buffer_load_dwordx4 v146, s[24:27], s65 offen offset:3072 lds
MAavv 104,76,16
Tv 120,137,16384
MAavv 108,80,16
s_add_u32 s50, s50, s51
s_cmp_eq_u32 s50, s53
s_cselect_b32 s50, 0, s50
MAavv 112,84,16
Tv 122,137,18432
MAavv 116,88,16
Rv 56,141,10240
MAavv 120,92,16
s_add_u32 s62, s62, 16384
s_and_b32 s62, s62, 0xffff
s_mov_b32 m0, s62
MAavv 124,96,16
Tv 124,138,16384
MAavv 128,68,20
buffer_load_dwordx4 v147, s[28:31], s50 offen lds
MAavv 132,72,20
Tv 126,138,18432
MAavv 136,76,20
MAavv 140,80,20
Rv 60,141,12288
MAavv 144,84,20
Tv 128,139,16384
MAavv 148,88,20
MAavv 152,92,20
Tv 130,139,18432
MAavv 156,96,20
buffer_load_dwordx4 v148, s[28:31], s50 offen offset:1024 lds
MAavv 160,68,24
Rv 64,141,14336
MAavv 164,72,24
MAavv 168,76,24
MAavv 172,80,24
MAavv 176,84,24
MAavv 180,88,24
MAavv 184,92,24
buffer_load_dwordx4 v149, s[28:31], s50 offen offset:2048 lds
MAavv 188,96,24
MAavv 192,68,28
MAavv 196,72,28
MAavv 200,76,28
MAavv 204,80,28
MAavv 208,84,28
MAavv 212,88,28
buffer_load_dwordx4 v150, s[28:31], s50 offen offset:3072 lds
MAavv 216,92,28
MAavv 220,96,28
MAavv 224,68,32
MAavv 228,72,32
MAavv 232,76,32
MAavv 236,80,32
MAavv 240,84,32
MAavv 244,88,32
MAavv 248,92,32
MAavv 252,96,32
s_waitcnt vmcnt(16) lgkmcnt(0)
s_barrier
MAavv 0,100,36
s_add_u32 s63, s63, 32768
s_cmp_eq_u32 s63, 163840
s_cselect_b32 s63, 65536, s63
MAavv 4,104,36
v_add_u32 v142, s63, v140
v_xor_b32 v141, 64, v142
MAavv 8,108,36
v_xor_b32 v132, 0x8000, v132
v_xor_b32 v133, 0x8000, v133
MAavv 12,112,36
v_xor_b32 v134, 0x8000, v134
v_xor_b32 v135, 0x8000, v135
MAavv 16,116,36
v_xor_b32 v136, 0x8000, v136
v_xor_b32 v137, 0x8000, v137
MAavv 20,120,36
v_xor_b32 v138, 0x8000, v138
v_xor_b32 v139, 0x8000, v139
MAavv 24,124,36
Tv 68,132,0
MAavv 28,128,36
Tv 70,132,2048
MAavv 32,100,40
Rv 4,142,0
MAavv 36,104,40
Tv 72,133,0
MAavv 40,108,40
Tv 74,133,2048
MAavv 44,112,40
Rv 8,142,2048
MAavv 48,116,40
s_add_u32 s48, s48, 128
s_cmp_eq_u32 s48, s52
s_cselect_b32 s48, 0, s48
MAavv 52,120,40
Tv 76,134,0
MAavv 56,124,40
s_add_u32 s61, s61, 32768
s_cmp_ge_u32 s61, 163840
s_cselect_b32 s64, 98304, 0
MAavv 60,128,40
Tv 78,134,2048
s_sub_u32 s61, s61, s64
s_mov_b32 m0, s61
MAavv 64,100,44
buffer_load_dwordx4 v143, s[24:27], s48 offen lds
MAavv 68,104,44
Rv 12,142,4096
MAavv 72,108,44
Tv 80,135,0
MAavv 76,112,44
buffer_load_dwordx4 v144, s[24:27], s48 offen offset:1024 lds
MAavv 80,116,44
Tv 82,135,2048
MAavv 84,120,44
buffer_load_dwordx4 v145, s[24:27], s48 offen offset:2048 lds
MAavv 88,124,44
Rv 16,142,6144
MAavv 92,128,44
MAavv 96,100,48
Tv 84,136,0
MAavv 100,104,48
Tv 86,136,2048
MAavv 104,108,48
buffer_load_dwordx4 v146, s[24:27], s48 offen offset:3072 lds
MAavv 108,112,48
Rv 20,142,8192
MAavv 112,116,48
s_add_u32 s50, s50, s51
s_cmp_eq_u32 s50, s53
s_cselect_b32 s50, 0, s50
MAavv 116,120,48
Tv 88,137,0
MAavv 120,124,48
s_add_u32 s62, s62, 16384
s_and_b32 s62, s62, 0xffff
s_mov_b32 m0, s62
MAavv 124,128,48
Tv 90,137,2048
MAavv 128,100,52
Rv 24,142,10240
MAavv 132,104,52
buffer_load_dwordx4 v147, s[28:31], s50 offen lds
MAavv 136,108,52
Tv 92,138,0
MAavv 140,112,52
MAavv 144,116,52
Tv 94,138,2048
MAavv 148,120,52
MAavv 152,124,52
Rv 28,142,12288
MAavv 156,128,52
Tv 96,139,0
MAavv 160,100,56
buffer_load_dwordx4 v148, s[28:31], s50 offen offset:1024 lds
MAavv 164,104,56
Tv 98,139,2048
MAavv 168,108,56
MAavv 172,112,56
Rv 32,142,14336
MAavv 176,116,56
MAavv 180,120,56
MAavv 184,124,56
buffer_load_dwordx4 v149, s[28:31], s50 offen offset:2048 lds
MAavv 188,128,56
MAavv 192,100,60
MAavv 196,104,60
MAavv 200,108,60
MAavv 204,112,60
MAavv 208,116,60
MAavv 212,120,60
buffer_load_dwordx4 v150, s[28:31], s50 offen offset:3072 lds
MAavv 216,124,60
MAavv 220,128,60
MAavv 224,100,64
MAavv 228,104,64
MAavv 232,108,64
MAavv 236,112,64
MAavv 240,116,64
MAavv 244,120,64
MAavv 248,124,64
MAavv 252,128,64
s_sub_u32 s67, s67, 1
s_cmp_lg_u32 s67, 0
s_cbranch_scc1 Lu2K_loop
s_waitcnt vmcnt(16) lgkmcnt(0)
s_barrier
MAavv 0,68,4
Tv 100,132,16384
MAavv 4,72,4
Tv 102,132,18432
MAavv 8,76,4
s_add_u32 s65, s48, s49
s_add_u32 m0, s61, 4096
MAavv 12,80,4
Rv 36,141,0
MAavv 16,84,4
buffer_load_dwordx4 v143, s[24:27], s65 offen lds
MAavv 20,88,4
Tv 104,133,16384
MAavv 24,92,4
MAavv 28,96,4
Tv 106,133,18432
MAavv 32,68,8
Rv 40,141,2048
MAavv 36,72,8
MAavv 40,76,8
Tv 108,134,16384
MAavv 44,80,8
buffer_load_dwordx4 v144, s[24:27], s65 offen offset:1024 lds
MAavv 48,84,8
Tv 110,134,18432
MAavv 52,88,8
MAavv 56,92,8
Rv 44,141,4096
MAavv 60,96,8
Tv 112,135,16384
MAavv 64,68,12
MAavv 68,72,12
Tv 114,135,18432
MAavv 72,76,12
buffer_load_dwordx4 v145, s[24:27], s65 offen offset:2048 lds
MAavv 76,80,12
Rv 48,141,6144
MAavv 80,84,12
MAavv 84,88,12
Tv 116,136,16384
MAavv 88,92,12
Tv 118,136,18432
MAavv 92,96,12
MAavv 96,68,16
Rv 52,141,8192
MAavv 100,72,16
buffer_load_dwordx4 v146, s[24:27], s65 offen offset:3072 lds
MAavv 104,76,16
Tv 120,137,16384
MAavv 108,80,16
s_add_u32 s50, s50, s51
s_cmp_eq_u32 s50, s53
s_cselect_b32 s50, 0, s50
MAavv 112,84,16
Tv 122,137,18432
MAavv 116,88,16
Rv 56,141,10240
MAavv 120,92,16
s_add_u32 s62, s62, 16384
s_and_b32 s62, s62, 0xffff
s_mov_b32 m0, s62
MAavv 124,96,16
Tv 124,138,16384
MAavv 128,68,20
buffer_load_dwordx4 v147, s[28:31], s50 offen lds
MAavv 132,72,20
Tv 126,138,18432
MAavv 136,76,20
MAavv 140,80,20
Rv 60,141,12288
MAavv 144,84,20
Tv 128,139,16384
MAavv 148,88,20
MAavv 152,92,20
Tv 130,139,18432
MAavv 156,96,20
buffer_load_dwordx4 v148, s[28:31], s50 offen offset:1024 lds
MAavv 160,68,24
Rv 64,141,14336
MAavv 164,72,24
MAavv 168,76,24
MAavv 172,80,24
MAavv 176,84,24
MAavv 180,88,24
MAavv 184,92,24
buffer_load_dwordx4 v149, s[28:31], s50 offen offset:2048 lds
MAavv 188,96,24
MAavv 192,68,28
MAavv 196,72,28
MAavv 200,76,28
MAavv 204,80,28
MAavv 208,84,28
MAavv 212,88,28
buffer_load_dwordx4 v150, s[28:31], s50 offen offset:3072 lds
MAavv 216,92,28
MAavv 220,96,28
MAavv 224,68,32
MAavv 228,72,32
MAavv 232,76,32
MAavv 236,80,32
MAavv 240,84,32
MAavv 244,88,32
MAavv 248,92,32
MAavv 252,96,32
s_waitcnt vmcnt(16) lgkmcnt(0)
s_barrier
MAavv 0,100,36
s_add_u32 s63, s63, 32768
s_cmp_eq_u32 s63, 163840
s_cselect_b32 s63, 65536, s63
MAavv 4,104,36
v_add_u32 v142, s63, v140
v_xor_b32 v141, 64, v142
MAavv 8,108,36
v_xor_b32 v132, 0x8000, v132
v_xor_b32 v133, 0x8000, v133
MAavv 12,112,36
v_xor_b32 v134, 0x8000, v134
v_xor_b32 v135, 0x8000, v135
MAavv 16,116,36
v_xor_b32 v136, 0x8000, v136
v_xor_b32 v137, 0x8000, v137
MAavv 20,120,36
v_xor_b32 v138, 0x8000, v138
v_xor_b32 v139, 0x8000, v139
MAavv 24,124,36
Tv 68,132,0
MAavv 28,128,36
Tv 70,132,2048
MAavv 32,100,40
Rv 4,142,0
MAavv 36,104,40
Tv 72,133,0
MAavv 40,108,40
Tv 74,133,2048
MAavv 44,112,40
Rv 8,142,2048
MAavv 48,116,40
s_mov_b64 s[24:25], s[36:37]
s_mov_b64 s[26:27], s[38:39]
s_mov_b32 s48, s58
MAavv 52,120,40
Tv 76,134,0
s_mov_b32 s49, s54
s_mov_b32 s52, s56
MAavv 56,124,40
v_mov_b32 v143, v151
v_mov_b32 v144, v152
MAavv 60,128,40
Tv 78,134,2048
v_mov_b32 v145, v153
MAavv 64,100,44
v_mov_b32 v146, v154
MAavv 68,104,44
Rv 12,142,4096
MAavv 72,108,44
Tv 80,135,0
MAavv 76,112,44
s_add_u32 s61, s61, 32768
s_cmp_ge_u32 s61, 163840
s_cselect_b32 s64, 98304, 0
MAavv 80,116,44
Tv 82,135,2048
s_sub_u32 s61, s61, s64
s_mov_b32 m0, s61
MAavv 84,120,44
buffer_load_dwordx4 v143, s[24:27], s48 offen lds
MAavv 88,124,44
Rv 16,142,6144
MAavv 92,128,44
buffer_load_dwordx4 v144, s[24:27], s48 offen offset:1024 lds
MAavv 96,100,48
Tv 84,136,0
MAavv 100,104,48
Tv 86,136,2048
MAavv 104,108,48
buffer_load_dwordx4 v145, s[24:27], s48 offen offset:2048 lds
MAavv 108,112,48
Rv 20,142,8192
MAavv 112,116,48
buffer_load_dwordx4 v146, s[24:27], s48 offen offset:3072 lds
MAavv 116,120,48
Tv 88,137,0
MAavv 120,124,48
s_add_u32 s50, s50, s51
s_cmp_eq_u32 s50, s53
s_cselect_b32 s50, 0, s50
MAavv 124,128,48
Tv 90,137,2048
MAavv 128,100,52
Rv 24,142,10240
MAavv 132,104,52
s_add_u32 s62, s62, 16384
s_and_b32 s62, s62, 0xffff
s_mov_b32 m0, s62
MAavv 136,108,52
Tv 92,138,0
MAavv 140,112,52
buffer_load_dwordx4 v147, s[28:31], s50 offen lds
MAavv 144,116,52
Tv 94,138,2048
MAavv 148,120,52
MAavv 152,124,52
Rv 28,142,12288
MAavv 156,128,52
Tv 96,139,0
MAavv 160,100,56
buffer_load_dwordx4 v148, s[28:31], s50 offen offset:1024 lds
MAavv 164,104,56
Tv 98,139,2048
MAavv 168,108,56
MAavv 172,112,56
Rv 32,142,14336
MAavv 176,116,56
MAavv 180,120,56
MAavv 184,124,56
buffer_load_dwordx4 v149, s[28:31], s50 offen offset:2048 lds
MAavv 188,128,56
MAavv 192,100,60
MAavv 196,104,60
MAavv 200,108,60
MAavv 204,112,60
MAavv 208,116,60
MAavv 212,120,60
buffer_load_dwordx4 v150, s[28:31], s50 offen offset:3072 lds
MAavv 216,124,60
MAavv 220,128,60
MAavv 224,100,64
MAavv 228,104,64
MAavv 232,108,64
MAavv 236,112,64
MAavv 240,116,64
MAavv 244,120,64
MAavv 248,124,64
MAavv 252,128,64
s_waitcnt vmcnt(16) lgkmcnt(0)
s_barrier
MAavv 0,68,4
Tv 100,132,16384
MAavv 4,72,4
Tv 102,132,18432
MAavv 8,76,4
s_add_u32 s65, s48, s49
s_add_u32 m0, s61, 4096
MAavv 12,80,4
Rv 36,141,0
MAavv 16,84,4
buffer_load_dwordx4 v143, s[24:27], s65 offen lds
MAavv 20,88,4
Tv 104,133,16384
MAavv 24,92,4
MAavv 28,96,4
Tv 106,133,18432
MAavv 32,68,8
Rv 40,141,2048
MAavv 36,72,8
MAavv 40,76,8
Tv 108,134,16384
MAavv 44,80,8
buffer_load_dwordx4 v144, s[24:27], s65 offen offset:1024 lds
MAavv 48,84,8
Tv 110,134,18432
MAavv 52,88,8
MAavv 56,92,8
Rv 44,141,4096
MAavv 60,96,8
Tv 112,135,16384
MAavv 64,68,12
MAavv 68,72,12
Tv 114,135,18432
MAavv 72,76,12
buffer_load_dwordx4 v145, s[24:27], s65 offen offset:2048 lds
MAavv 76,80,12
Rv 48,141,6144
MAavv 80,84,12
MAavv 84,88,12
Tv 116,136,16384
MAavv 88,92,12
Tv 118,136,18432
MAavv 92,96,12
MAavv 96,68,16
Rv 52,141,8192
MAavv 100,72,16
buffer_load_dwordx4 v146, s[24:27], s65 offen offset:3072 lds
MAavv 104,76,16
Tv 120,137,16384
MAavv 108,80,16
s_mov_b64 s[28:29], s[40:41]
s_mov_b64 s[30:31], s[42:43]
s_mov_b32 s50, s59
MAavv 112,84,16
Tv 122,137,18432
s_mov_b32 s51, s55
s_mov_b32 s53, s57
MAavv 116,88,16
Rv 56,141,10240
v_mov_b32 v147, v155
MAavv 120,92,16
v_mov_b32 v148, v156
v_mov_b32 v149, v157
MAavv 124,96,16
Tv 124,138,16384
v_mov_b32 v150, v158
MAavv 128,68,20
s_add_u32 s62, s62, 16384
s_and_b32 s62, s62, 0xffff
s_mov_b32 m0, s62
MAavv 132,72,20
Tv 126,138,18432
MAavv 136,76,20
buffer_load_dwordx4 v147, s[28:31], s50 offen lds
MAavv 140,80,20
Rv 60,141,12288
MAavv 144,84,20
Tv 128,139,16384
MAavv 148,88,20
MAavv 152,92,20
Tv 130,139,18432
MAavv 156,96,20
buffer_load_dwordx4 v148, s[28:31], s50 offen offset:1024 lds
MAavv 160,68,24
Rv 64,141,14336
MAavv 164,72,24
MAavv 168,76,24
MAavv 172,80,24
MAavv 176,84,24
MAavv 180,88,24
MAavv 184,92,24
buffer_load_dwordx4 v149, s[28:31], s50 offen offset:2048 lds
MAavv 188,96,24
MAavv 192,68,28
MAavv 196,72,28
MAavv 200,76,28
MAavv 204,80,28
MAavv 208,84,28
MAavv 212,88,28
buffer_load_dwordx4 v150, s[28:31], s50 offen offset:3072 lds
MAavv 216,92,28
MAavv 220,96,28
MAavv 224,68,32
MAavv 228,72,32
MAavv 232,76,32
MAavv 236,80,32
MAavv 240,84,32
MAavv 244,88,32
MAavv 248,92,32
MAavv 252,96,32
s_waitcnt vmcnt(16) lgkmcnt(0)
s_barrier
MAavv 0,100,36
s_add_u32 s63, s63, 32768
s_cmp_eq_u32 s63, 163840
s_cselect_b32 s63, 65536, s63
MAavv 4,104,36
v_add_u32 v142, s63, v140
v_xor_b32 v141, 64, v142
MAavv 8,108,36
v_xor_b32 v132, 0x8000, v132
v_xor_b32 v133, 0x8000, v133
MAavv 12,112,36
v_xor_b32 v134, 0x8000, v134
v_xor_b32 v135, 0x8000, v135
MAavv 16,116,36
v_xor_b32 v136, 0x8000, v136
v_xor_b32 v137, 0x8000, v137
MAavv 20,120,36
v_xor_b32 v138, 0x8000, v138
v_xor_b32 v139, 0x8000, v139
MAavv 24,124,36
Tv 68,132,0
MAavv 28,128,36
Tv 70,132,2048
MAavv 32,100,40
Rv 4,142,0
MAavv 36,104,40
Tv 72,133,0
MAavv 40,108,40
Tv 74,133,2048
MAavv 44,112,40
Rv 8,142,2048
MAavv 48,116,40
s_add_u32 s48, s48, 128
s_cmp_eq_u32 s48, s52
s_cselect_b32 s48, 0, s48
MAavv 52,120,40
Tv 76,134,0
MAavv 56,124,40
s_add_u32 s61, s61, 32768
s_cmp_ge_u32 s61, 163840
s_cselect_b32 s64, 98304, 0
MAavv 60,128,40
Tv 78,134,2048
s_sub_u32 s61, s61, s64
s_mov_b32 m0, s61
MAavv 64,100,44
buffer_load_dwordx4 v143, s[24:27], s48 offen lds
MAavv 68,104,44
Rv 12,142,4096
MAavv 72,108,44
Tv 80,135,0
MAavv 76,112,44
buffer_load_dwordx4 v144, s[24:27], s48 offen offset:1024 lds
MAavv 80,116,44
Tv 82,135,2048
MAavv 84,120,44
buffer_load_dwordx4 v145, s[24:27], s48 offen offset:2048 lds
MAavv 88,124,44
Rv 16,142,6144
MAavv 92,128,44
MAavv 96,100,48
Tv 84,136,0
MAavv 100,104,48
Tv 86,136,2048
MAavv 104,108,48
buffer_load_dwordx4 v146, s[24:27], s48 offen offset:3072 lds
MAavv 108,112,48
Rv 20,142,8192
MAavv 112,116,48
s_add_u32 s50, s50, s51
s_cmp_eq_u32 s50, s53
s_cselect_b32 s50, 0, s50
MAavv 116,120,48
Tv 88,137,0
MAavv 120,124,48
s_add_u32 s62, s62, 16384
s_and_b32 s62, s62, 0xffff
s_mov_b32 m0, s62
MAavv 124,128,48
Tv 90,137,2048
MAavv 128,100,52
Rv 24,142,10240
MAavv 132,104,52
buffer_load_dwordx4 v147, s[28:31], s50 offen lds
MAavv 136,108,52
Tv 92,138,0
MAavv 140,112,52
MAavv 144,116,52
Tv 94,138,2048
MAavv 148,120,52
MAavv 152,124,52
Rv 28,142,12288
MAavv 156,128,52
Tv 96,139,0
MAavv 160,100,56
buffer_load_dwordx4 v148, s[28:31], s50 offen offset:1024 lds
MAavv 164,104,56
Tv 98,139,2048
MAavv 168,108,56
MAavv 172,112,56
Rv 32,142,14336
MAavv 176,116,56
MAavv 180,120,56
MAavv 184,124,56
buffer_load_dwordx4 v149, s[28:31], s50 offen offset:2048 lds
MAavv 188,128,56
MAavv 192,100,60
MAavv 196,104,60
MAavv 200,108,60
MAavv 204,112,60
MAavv 208,116,60
MAavv 212,120,60
buffer_load_dwordx4 v150, s[28:31], s50 offen offset:3072 lds
MAavv 216,124,60
MAavv 220,128,60
MAavv 224,100,64
MAavv 228,104,64
MAavv 232,108,64
MAavv 236,112,64
MAavv 240,116,64
MAavv 244,120,64
MAavv 248,124,64
MAavv 252,128,64
s_waitcnt vmcnt(16) lgkmcnt(0)
s_barrier
MAavv 0,68,4
Tv 100,132,16384
MAavv 4,72,4
Tv 102,132,18432
MAavv 8,76,4
Rv 36,141,0
MAavv 12,80,4
Tv 104,133,16384
MAavv 16,84,4
Tv 106,133,18432
MAavv 20,88,4
Rv 40,141,2048
MAavv 24,92,4
Tv 108,134,16384
MAavv 28,96,4
Tv 110,134,18432
MAavv 32,68,8
Rv 44,141,4096
MAavv 36,72,8
Tv 112,135,16384
MAavv 40,76,8
Tv 114,135,18432
MAavv 44,80,8
Rv 48,141,6144
MAavv 48,84,8
MAavv 52,88,8
Tv 116,136,16384
MAavv 56,92,8
Tv 118,136,18432
MAavv 60,96,8
Rv 52,141,8192
MAavv 64,68,12
Tv 120,137,16384
MAavv 68,72,12
Tv 122,137,18432
MAavv 72,76,12
Rv 56,141,10240
MAavv 76,80,12
Tv 124,138,16384
MAavv 80,84,12
Tv 126,138,18432
MAavv 84,88,12
Rv 60,141,12288
MAavv 88,92,12
Tv 128,139,16384
MAavv 92,96,12
Tv 130,139,18432
MAavv 96,68,16
Rv 64,141,14336
MAavv 100,72,16
MAavv 104,76,16
MAavv 108,80,16
MAavv 112,84,16
MAavv 116,88,16
MAavv 120,92,16
MAavv 124,96,16
s_waitcnt vmcnt(63) lgkmcnt(0)
s_barrier
MAavv 0,100,36
MAavv 4,104,36
MAavv 8,108,36
MAavv 12,112,36
MAavv 16,116,36
MAavv 20,120,36
MAavv 24,124,36
MAavv 28,128,36
MAavv 32,100,40
MAavv 36,104,40
MAavv 40,108,40
MAavv 44,112,40
AR 172,0
AR 173,1
AR 174,2
AR 175,3
MAavv 48,116,40
AR 176,4
AR 177,5
AR 178,6
AR 179,7
MAavv 52,120,40
CV 204,172,173
CV 205,174,175
CV 206,176,177
CV 207,178,179
MAavv 56,124,40
s_mul_i32 s60, s73, 0
s_add_u32 s60, s60, s72
buffer_store_dwordx4 v[204:207], v159, s[44:47], s60 offen sc1
AR 180,8
AR 181,9
MAavv 60,128,40
AR 182,10
AR 183,11
AR 184,12
AR 185,13
MAavv 64,100,44
AR 186,14
AR 187,15
CV 208,180,181
CV 209,182,183
MAavv 68,104,44
CV 210,184,185
CV 211,186,187
buffer_store_dwordx4 v[208:211], v159, s[44:47], s60 offen offset:1024 sc1
MAavv 72,108,44
AR 188,16
AR 189,17
AR 190,18
AR 191,19
MAavv 76,112,44
AR 192,20
AR 193,21
AR 194,22
AR 195,23
MAavv 80,116,44
CV 212,188,189
CV 213,190,191
CV 214,192,193
CV 215,194,195
MAavv 84,120,44
buffer_store_dwordx4 v[212:215], v159, s[44:47], s60 offen offset:2048 sc1
AR 196,24
AR 197,25
MAavv 88,124,44
AR 198,26
AR 199,27
AR 200,28
AR 201,29
MAavv 92,128,44
AR 202,30
AR 203,31
CV 216,196,197
CV 217,198,199
MAavv 96,100,48
CV 218,200,201
CV 219,202,203
buffer_store_dwordx4 v[216:219], v159, s[44:47], s60 offen offset:3072 sc1
MAavv 100,104,48
AR 172,32
AR 173,33
AR 174,34
AR 175,35
MAavv 104,108,48
AR 176,36
AR 177,37
AR 178,38
AR 179,39
MAavv 108,112,48
CV 220,172,173
CV 221,174,175
CV 222,176,177
CV 223,178,179
MAavv 112,116,48
s_mul_i32 s60, s73, 1
s_add_u32 s60, s60, s72
buffer_store_dwordx4 v[220:223], v159, s[44:47], s60 offen sc1
AR 180,40
AR 181,41
MAavv 116,120,48
AR 182,42
AR 183,43
AR 184,44
AR 185,45
MAavv 120,124,48
AR 186,46
AR 187,47
CV 224,180,181
CV 225,182,183
MAavv 124,128,48
CV 226,184,185
CV 227,186,187
buffer_store_dwordx4 v[224:227], v159, s[44:47], s60 offen offset:1024 sc1
MAavv 128,68,20
AR 188,48
AR 189,49
AR 190,50
AR 191,51
MAavv 132,72,20
AR 192,52
AR 193,53
AR 194,54
AR 195,55
MAavv 136,76,20
CV 228,188,189
CV 229,190,191
CV 230,192,193
CV 231,194,195
MAavv 140,80,20
buffer_store_dwordx4 v[228:231], v159, s[44:47], s60 offen offset:2048 sc1
AR 196,56
AR 197,57
MAavv 144,84,20
AR 198,58
AR 199,59
AR 200,60
AR 201,61
MAavv 148,88,20
AR 202,62
AR 203,63
CV 232,196,197
CV 233,198,199
MAavv 152,92,20
CV 234,200,201
CV 235,202,203
buffer_store_dwordx4 v[232:235], v159, s[44:47], s60 offen offset:3072 sc1
MAavv 156,96,20
AR 172,64
AR 173,65
AR 174,66
AR 175,67
MAavv 160,68,24
AR 176,68
AR 177,69
AR 178,70
AR 179,71
MAavv 164,72,24
CV 204,172,173
CV 205,174,175
CV 206,176,177
CV 207,178,179
MAavv 168,76,24
s_mul_i32 s60, s73, 2
s_add_u32 s60, s60, s72
buffer_store_dwordx4 v[204:207], v159, s[44:47], s60 offen sc1
AR 180,72
AR 181,73
MAavv 172,80,24
AR 182,74
AR 183,75
AR 184,76
AR 185,77
MAavv 176,84,24
AR 186,78
AR 187,79
CV 208,180,181
CV 209,182,183
MAavv 180,88,24
CV 210,184,185
CV 211,186,187
buffer_store_dwordx4 v[208:211], v159, s[44:47], s60 offen offset:1024 sc1
MAavv 184,92,24
AR 188,80
AR 189,81
AR 190,82
AR 191,83
MAavv 188,96,24
AR 192,84
AR 193,85
AR 194,86
AR 195,87
MAavv 192,68,28
CV 212,188,189
CV 213,190,191
CV 214,192,193
CV 215,194,195
MAavv 196,72,28
buffer_store_dwordx4 v[212:215], v159, s[44:47], s60 offen offset:2048 sc1
AR 196,88
AR 197,89
MAavv 200,76,28
AR 198,90
AR 199,91
AR 200,92
AR 201,93
MAavv 204,80,28
AR 202,94
AR 203,95
CV 216,196,197
CV 217,198,199
MAavv 208,84,28
CV 218,200,201
CV 219,202,203
buffer_store_dwordx4 v[216:219], v159, s[44:47], s60 offen offset:3072 sc1
MAavv 212,88,28
AR 172,96
AR 173,97
AR 174,98
AR 175,99
MAavv 216,92,28
AR 176,100
AR 177,101
AR 178,102
AR 179,103
MAavv 220,96,28
CV 220,172,173
CV 221,174,175
CV 222,176,177
CV 223,178,179
MAavv 224,68,32
s_mul_i32 s60, s73, 3
s_add_u32 s60, s60, s72
buffer_store_dwordx4 v[220:223], v159, s[44:47], s60 offen sc1
AR 180,104
AR 181,105
MAavv 228,72,32
AR 182,106
AR 183,107
AR 184,108
AR 185,109
MAavv 232,76,32
AR 186,110
AR 187,111
)ASM"
R"ASM(CV 224,180,181
CV 225,182,183
MAavv 236,80,32
CV 226,184,185
CV 227,186,187
buffer_store_dwordx4 v[224:227], v159, s[44:47], s60 offen offset:1024 sc1
MAavv 240,84,32
AR 188,112
AR 189,113
AR 190,114
AR 191,115
MAavv 244,88,32
AR 192,116
AR 193,117
AR 194,118
AR 195,119
MAavv 248,92,32
CV 228,188,189
CV 229,190,191
CV 230,192,193
CV 231,194,195
MAavv 252,96,32
buffer_store_dwordx4 v[228:231], v159, s[44:47], s60 offen offset:2048 sc1
AR 196,120
AR 197,121
MAavv 128,100,52
AR 198,122
AR 199,123
AR 200,124
AR 201,125
MAavv 132,104,52
AR 202,126
AR 203,127
CV 232,196,197
CV 233,198,199
MAavv 136,108,52
CV 234,200,201
CV 235,202,203
buffer_store_dwordx4 v[232:235], v159, s[44:47], s60 offen offset:3072 sc1
MAavv 140,112,52
MAavv 144,116,52
MAavv 148,120,52
MAavv 152,124,52
MAavv 156,128,52
MAavv 160,100,56
MAavv 164,104,56
MAavv 168,108,56
MAavv 172,112,56
AR 172,128
AR 173,129
AR 174,130
AR 175,131
MAavv 176,116,56
AR 176,132
AR 177,133
AR 178,134
AR 179,135
MAavv 180,120,56
CV 204,172,173
CV 205,174,175
CV 206,176,177
CV 207,178,179
MAavv 184,124,56
s_mul_i32 s60, s73, 4
s_add_u32 s60, s60, s72
buffer_store_dwordx4 v[204:207], v159, s[44:47], s60 offen sc1
AR 180,136
AR 181,137
MAavv 188,128,56
AR 182,138
AR 183,139
AR 184,140
AR 185,141
MAavv 192,100,60
AR 186,142
AR 187,143
CV 208,180,181
CV 209,182,183
MAavv 196,104,60
CV 210,184,185
CV 211,186,187
buffer_store_dwordx4 v[208:211], v159, s[44:47], s60 offen offset:1024 sc1
MAavv 200,108,60
AR 188,144
AR 189,145
AR 190,146
AR 191,147
MAavv 204,112,60
AR 192,148
AR 193,149
AR 194,150
AR 195,151
MAavv 208,116,60
CV 212,188,189
CV 213,190,191
CV 214,192,193
CV 215,194,195
MAavv 212,120,60
buffer_store_dwordx4 v[212:215], v159, s[44:47], s60 offen offset:2048 sc1
AR 196,152
AR 197,153
MAavv 216,124,60
AR 198,154
AR 199,155
AR 200,156
AR 201,157
MAavv 220,128,60
AR 202,158
AR 203,159
CV 216,196,197
CV 217,198,199
MAavv 224,100,64
CV 218,200,201
CV 219,202,203
buffer_store_dwordx4 v[216:219], v159, s[44:47], s60 offen offset:3072 sc1
MAavv 228,104,64
AR 172,160
AR 173,161
AR 174,162
AR 175,163
MAavv 232,108,64
AR 176,164
AR 177,165
AR 178,166
AR 179,167
MAavv 236,112,64
CV 220,172,173
CV 221,174,175
CV 222,176,177
CV 223,178,179
MAavv 240,116,64
s_mul_i32 s60, s73, 5
s_add_u32 s60, s60, s72
buffer_store_dwordx4 v[220:223], v159, s[44:47], s60 offen sc1
AR 180,168
AR 181,169
MAavv 244,120,64
AR 182,170
AR 183,171
AR 184,172
AR 185,173
MAavv 248,124,64
AR 186,174
AR 187,175
CV 224,180,181
CV 225,182,183
CV 226,184,185
CV 227,186,187
buffer_store_dwordx4 v[224:227], v159, s[44:47], s60 offen offset:1024 sc1
AR 188,176
AR 189,177
AR 190,178
AR 191,179
AR 192,180
AR 193,181
AR 194,182
AR 195,183
CV 228,188,189
CV 229,190,191
CV 230,192,193
CV 231,194,195
buffer_store_dwordx4 v[228:231], v159, s[44:47], s60 offen offset:2048 sc1
AR 196,184
AR 197,185
AR 198,186
AR 199,187
AR 200,188
AR 201,189
AR 202,190
AR 203,191
CV 232,196,197
CV 233,198,199
CV 234,200,201
CV 235,202,203
buffer_store_dwordx4 v[232:235], v159, s[44:47], s60 offen offset:3072 sc1
MAavv 252,128,64
AR 172,192
AR 173,193
AR 174,194
AR 175,195
AR 176,196
AR 177,197
AR 178,198
AR 179,199
CV 204,172,173
CV 205,174,175
CV 206,176,177
CV 207,178,179
s_mul_i32 s60, s73, 6
s_add_u32 s60, s60, s72
buffer_store_dwordx4 v[204:207], v159, s[44:47], s60 offen sc1
AR 180,200
AR 181,201
AR 182,202
AR 183,203
AR 184,204
AR 185,205
AR 186,206
AR 187,207
CV 208,180,181
CV 209,182,183
CV 210,184,185
CV 211,186,187
buffer_store_dwordx4 v[208:211], v159, s[44:47], s60 offen offset:1024 sc1
AR 188,208
AR 189,209
AR 190,210
AR 191,211
AR 192,212
AR 193,213
AR 194,214
AR 195,215
CV 212,188,189
CV 213,190,191
CV 214,192,193
CV 215,194,195
buffer_store_dwordx4 v[212:215], v159, s[44:47], s60 offen offset:2048 sc1
AR 196,216
AR 197,217
AR 198,218
AR 199,219
AR 200,220
AR 201,221
AR 202,222
AR 203,223
CV 216,196,197
CV 217,198,199
CV 218,200,201
CV 219,202,203
buffer_store_dwordx4 v[216:219], v159, s[44:47], s60 offen offset:3072 sc1
AR 172,224
AR 173,225
AR 174,226
AR 175,227
AR 176,228
AR 177,229
AR 178,230
AR 179,231
CV 220,172,173
CV 221,174,175
CV 222,176,177
CV 223,178,179
s_mul_i32 s60, s73, 7
s_add_u32 s60, s60, s72
buffer_store_dwordx4 v[220:223], v159, s[44:47], s60 offen sc1
AR 180,232
AR 181,233
AR 182,234
AR 183,235
AR 184,236
AR 185,237
AR 186,238
AR 187,239
CV 224,180,181
CV 225,182,183
CV 226,184,185
CV 227,186,187
buffer_store_dwordx4 v[224:227], v159, s[44:47], s60 offen offset:1024 sc1
AR 188,240
AR 189,241
AR 190,242
AR 191,243
AR 192,244
AR 193,245
AR 194,246
AR 195,247
CV 228,188,189
CV 229,190,191
CV 230,192,193
CV 231,194,195
buffer_store_dwordx4 v[228:231], v159, s[44:47], s60 offen offset:2048 sc1
AR 196,248
AR 197,249
AR 198,250
AR 199,251
AR 200,252
AR 201,253
AR 202,254
AR 203,255
CV 232,196,197
CV 233,198,199
CV 234,200,201
CV 235,202,203
buffer_store_dwordx4 v[232:235], v159, s[44:47], s60 offen offset:3072 sc1
s_branch Lk1_end
Lk1_u2V:
s_cmp_lg_u32 s76, 0
s_cbranch_scc1 Lw4
s_waitcnt vmcnt(16)
Lw4:
s_waitcnt vmcnt(48) lgkmcnt(0)
s_barrier
MZavv 0,4,68
Tv 100,132,16384
MZavv 4,4,72
Tv 102,132,18432
MZavv 8,4,76
s_add_u32 s65, s48, s49
s_add_u32 m0, s61, 4096
MZavv 12,4,80
Rv 36,141,0
MZavv 16,4,84
buffer_load_dwordx4 v143, s[24:27], s65 offen lds
MZavv 20,4,88
Tv 104,133,16384
MZavv 24,4,92
MZavv 28,4,96
Tv 106,133,18432
MZavv 32,8,68
Rv 40,141,2048
MZavv 36,8,72
MZavv 40,8,76
Tv 108,134,16384
MZavv 44,8,80
buffer_load_dwordx4 v144, s[24:27], s65 offen offset:1024 lds
MZavv 48,8,84
Tv 110,134,18432
MZavv 52,8,88
MZavv 56,8,92
Rv 44,141,4096
MZavv 60,8,96
Tv 112,135,16384
MZavv 64,12,68
MZavv 68,12,72
Tv 114,135,18432
MZavv 72,12,76
buffer_load_dwordx4 v145, s[24:27], s65 offen offset:2048 lds
MZavv 76,12,80
Rv 48,141,6144
MZavv 80,12,84
MZavv 84,12,88
Tv 116,136,16384
MZavv 88,12,92
Tv 118,136,18432
MZavv 92,12,96
MZavv 96,16,68
Rv 52,141,8192
MZavv 100,16,72
buffer_load_dwordx4 v146, s[24:27], s65 offen offset:3072 lds
MZavv 104,16,76
Tv 120,137,16384
MZavv 108,16,80
s_add_u32 s50, s50, s51
s_cmp_eq_u32 s50, s53
s_cselect_b32 s50, 0, s50
MZavv 112,16,84
Tv 122,137,18432
MZavv 116,16,88
Rv 56,141,10240
MZavv 120,16,92
s_add_u32 s62, s62, 16384
s_and_b32 s62, s62, 0xffff
s_mov_b32 m0, s62
MZavv 124,16,96
Tv 124,138,16384
MZavv 128,20,68
buffer_load_dwordx4 v147, s[28:31], s50 offen lds
MZavv 132,20,72
Tv 126,138,18432
MZavv 136,20,76
MZavv 140,20,80
Rv 60,141,12288
MZavv 144,20,84
Tv 128,139,16384
MZavv 148,20,88
MZavv 152,20,92
Tv 130,139,18432
MZavv 156,20,96
buffer_load_dwordx4 v148, s[28:31], s50 offen offset:1024 lds
MZavv 160,24,68
Rv 64,141,14336
MZavv 164,24,72
MZavv 168,24,76
MZavv 172,24,80
MZavv 176,24,84
MZavv 180,24,88
MZavv 184,24,92
buffer_load_dwordx4 v149, s[28:31], s50 offen offset:2048 lds
MZavv 188,24,96
MZavv 192,28,68
MZavv 196,28,72
MZavv 200,28,76
MZavv 204,28,80
MZavv 208,28,84
MZavv 212,28,88
buffer_load_dwordx4 v150, s[28:31], s50 offen offset:3072 lds
MZavv 216,28,92
MZavv 220,28,96
MZavv 224,32,68
MZavv 228,32,72
MZavv 232,32,76
MZavv 236,32,80
MZavv 240,32,84
MZavv 244,32,88
MZavv 248,32,92
MZavv 252,32,96
s_cmp_lg_u32 s76, 0
s_cbranch_scc1 Lw5
s_waitcnt vmcnt(16)
Lw5:
s_waitcnt vmcnt(48) lgkmcnt(0)
s_barrier
MAavv 0,36,100
s_add_u32 s63, s63, 32768
s_cmp_eq_u32 s63, 163840
s_cselect_b32 s63, 65536, s63
MAavv 4,36,104
v_add_u32 v142, s63, v140
v_xor_b32 v141, 64, v142
MAavv 8,36,108
v_xor_b32 v132, 0x8000, v132
v_xor_b32 v133, 0x8000, v133
MAavv 12,36,112
v_xor_b32 v134, 0x8000, v134
v_xor_b32 v135, 0x8000, v135
MAavv 16,36,116
v_xor_b32 v136, 0x8000, v136
v_xor_b32 v137, 0x8000, v137
MAavv 20,36,120
v_xor_b32 v138, 0x8000, v138
v_xor_b32 v139, 0x8000, v139
MAavv 24,36,124
Tv 68,132,0
MAavv 28,36,128
Tv 70,132,2048
MAavv 32,40,100
Rv 4,142,0
MAavv 36,40,104
Tv 72,133,0
MAavv 40,40,108
Tv 74,133,2048
MAavv 44,40,112
Rv 8,142,2048
MAavv 48,40,116
s_add_u32 s48, s48, 128
s_cmp_eq_u32 s48, s52
s_cselect_b32 s48, 0, s48
MAavv 52,40,120
Tv 76,134,0
MAavv 56,40,124
s_add_u32 s61, s61, 32768
s_cmp_ge_u32 s61, 163840
s_cselect_b32 s64, 98304, 0
MAavv 60,40,128
Tv 78,134,2048
s_sub_u32 s61, s61, s64
s_mov_b32 m0, s61
MAavv 64,44,100
buffer_load_dwordx4 v143, s[24:27], s48 offen lds
MAavv 68,44,104
Rv 12,142,4096
MAavv 72,44,108
Tv 80,135,0
MAavv 76,44,112
buffer_load_dwordx4 v144, s[24:27], s48 offen offset:1024 lds
MAavv 80,44,116
Tv 82,135,2048
MAavv 84,44,120
buffer_load_dwordx4 v145, s[24:27], s48 offen offset:2048 lds
MAavv 88,44,124
Rv 16,142,6144
MAavv 92,44,128
MAavv 96,48,100
Tv 84,136,0
MAavv 100,48,104
Tv 86,136,2048
MAavv 104,48,108
buffer_load_dwordx4 v146, s[24:27], s48 offen offset:3072 lds
MAavv 108,48,112
Rv 20,142,8192
MAavv 112,48,116
s_add_u32 s50, s50, s51
s_cmp_eq_u32 s50, s53
s_cselect_b32 s50, 0, s50
MAavv 116,48,120
Tv 88,137,0
MAavv 120,48,124
s_add_u32 s62, s62, 16384
s_and_b32 s62, s62, 0xffff
s_mov_b32 m0, s62
MAavv 124,48,128
Tv 90,137,2048
MAavv 128,52,100
Rv 24,142,10240
MAavv 132,52,104
buffer_load_dwordx4 v147, s[28:31], s50 offen lds
MAavv 136,52,108
Tv 92,138,0
MAavv 140,52,112
MAavv 144,52,116
Tv 94,138,2048
MAavv 148,52,120
MAavv 152,52,124
Rv 28,142,12288
MAavv 156,52,128
Tv 96,139,0
MAavv 160,56,100
buffer_load_dwordx4 v148, s[28:31], s50 offen offset:1024 lds
MAavv 164,56,104
Tv 98,139,2048
MAavv 168,56,108
MAavv 172,56,112
Rv 32,142,14336
MAavv 176,56,116
MAavv 180,56,120
MAavv 184,56,124
buffer_load_dwordx4 v149, s[28:31], s50 offen offset:2048 lds
MAavv 188,56,128
MAavv 192,60,100
MAavv 196,60,104
MAavv 200,60,108
MAavv 204,60,112
MAavv 208,60,116
MAavv 212,60,120
buffer_load_dwordx4 v150, s[28:31], s50 offen offset:3072 lds
MAavv 216,60,124
MAavv 220,60,128
MAavv 224,64,100
MAavv 228,64,104
MAavv 232,64,108
MAavv 236,64,112
MAavv 240,64,116
MAavv 244,64,120
MAavv 248,64,124
MAavv 252,64,128
s_cmp_lg_u32 s76, 0
s_cbranch_scc1 Lw6
s_waitcnt vmcnt(16)
Lw6:
s_waitcnt vmcnt(48) lgkmcnt(0)
s_barrier
MAavv 0,4,68
Tv 100,132,16384
MAavv 4,4,72
Tv 102,132,18432
MAavv 8,4,76
s_add_u32 s65, s48, s49
s_add_u32 m0, s61, 4096
MAavv 12,4,80
Rv 36,141,0
MAavv 16,4,84
buffer_load_dwordx4 v143, s[24:27], s65 offen lds
MAavv 20,4,88
Tv 104,133,16384
MAavv 24,4,92
MAavv 28,4,96
Tv 106,133,18432
MAavv 32,8,68
Rv 40,141,2048
MAavv 36,8,72
MAavv 40,8,76
Tv 108,134,16384
MAavv 44,8,80
buffer_load_dwordx4 v144, s[24:27], s65 offen offset:1024 lds
MAavv 48,8,84
Tv 110,134,18432
MAavv 52,8,88
MAavv 56,8,92
Rv 44,141,4096
MAavv 60,8,96
Tv 112,135,16384
MAavv 64,12,68
MAavv 68,12,72
Tv 114,135,18432
MAavv 72,12,76
buffer_load_dwordx4 v145, s[24:27], s65 offen offset:2048 lds
MAavv 76,12,80
Rv 48,141,6144
MAavv 80,12,84
MAavv 84,12,88
Tv 116,136,16384
MAavv 88,12,92
Tv 118,136,18432
MAavv 92,12,96
MAavv 96,16,68
Rv 52,141,8192
MAavv 100,16,72
buffer_load_dwordx4 v146, s[24:27], s65 offen offset:3072 lds
MAavv 104,16,76
Tv 120,137,16384
MAavv 108,16,80
s_add_u32 s50, s50, s51
s_cmp_eq_u32 s50, s53
s_cselect_b32 s50, 0, s50
MAavv 112,16,84
Tv 122,137,18432
MAavv 116,16,88
Rv 56,141,10240
MAavv 120,16,92
s_add_u32 s62, s62, 16384
s_and_b32 s62, s62, 0xffff
s_mov_b32 m0, s62
MAavv 124,16,96
Tv 124,138,16384
MAavv 128,20,68
buffer_load_dwordx4 v147, s[28:31], s50 offen lds
MAavv 132,20,72
Tv 126,138,18432
MAavv 136,20,76
MAavv 140,20,80
Rv 60,141,12288
MAavv 144,20,84
Tv 128,139,16384
MAavv 148,20,88
MAavv 152,20,92
Tv 130,139,18432
MAavv 156,20,96
buffer_load_dwordx4 v148, s[28:31], s50 offen offset:1024 lds
MAavv 160,24,68
Rv 64,141,14336
MAavv 164,24,72
MAavv 168,24,76
MAavv 172,24,80
MAavv 176,24,84
MAavv 180,24,88
MAavv 184,24,92
buffer_load_dwordx4 v149, s[28:31], s50 offen offset:2048 lds
MAavv 188,24,96
MAavv 192,28,68
MAavv 196,28,72
MAavv 200,28,76
MAavv 204,28,80
MAavv 208,28,84
MAavv 212,28,88
buffer_load_dwordx4 v150, s[28:31], s50 offen offset:3072 lds
MAavv 216,28,92
MAavv 220,28,96
MAavv 224,32,68
MAavv 228,32,72
MAavv 232,32,76
MAavv 236,32,80
MAavv 240,32,84
MAavv 244,32,88
MAavv 248,32,92
MAavv 252,32,96
s_waitcnt vmcnt(16) lgkmcnt(0)
s_barrier
MAavv 0,36,100
s_add_u32 s63, s63, 32768
s_cmp_eq_u32 s63, 163840
s_cselect_b32 s63, 65536, s63
MAavv 4,36,104
v_add_u32 v142, s63, v140
v_xor_b32 v141, 64, v142
MAavv 8,36,108
v_xor_b32 v132, 0x8000, v132
v_xor_b32 v133, 0x8000, v133
MAavv 12,36,112
v_xor_b32 v134, 0x8000, v134
v_xor_b32 v135, 0x8000, v135
MAavv 16,36,116
v_xor_b32 v136, 0x8000, v136
v_xor_b32 v137, 0x8000, v137
MAavv 20,36,120
v_xor_b32 v138, 0x8000, v138
v_xor_b32 v139, 0x8000, v139
MAavv 24,36,124
Tv 68,132,0
MAavv 28,36,128
Tv 70,132,2048
MAavv 32,40,100
Rv 4,142,0
MAavv 36,40,104
Tv 72,133,0
MAavv 40,40,108
Tv 74,133,2048
MAavv 44,40,112
Rv 8,142,2048
MAavv 48,40,116
s_add_u32 s48, s48, 128
s_cmp_eq_u32 s48, s52
s_cselect_b32 s48, 0, s48
MAavv 52,40,120
Tv 76,134,0
MAavv 56,40,124
s_add_u32 s61, s61, 32768
s_cmp_ge_u32 s61, 163840
s_cselect_b32 s64, 98304, 0
MAavv 60,40,128
Tv 78,134,2048
s_sub_u32 s61, s61, s64
s_mov_b32 m0, s61
MAavv 64,44,100
buffer_load_dwordx4 v143, s[24:27], s48 offen lds
MAavv 68,44,104
Rv 12,142,4096
MAavv 72,44,108
Tv 80,135,0
MAavv 76,44,112
buffer_load_dwordx4 v144, s[24:27], s48 offen offset:1024 lds
MAavv 80,44,116
Tv 82,135,2048
MAavv 84,44,120
buffer_load_dwordx4 v145, s[24:27], s48 offen offset:2048 lds
MAavv 88,44,124
Rv 16,142,6144
MAavv 92,44,128
MAavv 96,48,100
Tv 84,136,0
MAavv 100,48,104
Tv 86,136,2048
MAavv 104,48,108
buffer_load_dwordx4 v146, s[24:27], s48 offen offset:3072 lds
MAavv 108,48,112
Rv 20,142,8192
MAavv 112,48,116
s_add_u32 s50, s50, s51
s_cmp_eq_u32 s50, s53
s_cselect_b32 s50, 0, s50
MAavv 116,48,120
Tv 88,137,0
MAavv 120,48,124
s_add_u32 s62, s62, 16384
s_and_b32 s62, s62, 0xffff
s_mov_b32 m0, s62
MAavv 124,48,128
Tv 90,137,2048
MAavv 128,52,100
Rv 24,142,10240
MAavv 132,52,104
buffer_load_dwordx4 v147, s[28:31], s50 offen lds
MAavv 136,52,108
Tv 92,138,0
MAavv 140,52,112
MAavv 144,52,116
Tv 94,138,2048
MAavv 148,52,120
MAavv 152,52,124
Rv 28,142,12288
MAavv 156,52,128
Tv 96,139,0
MAavv 160,56,100
buffer_load_dwordx4 v148, s[28:31], s50 offen offset:1024 lds
MAavv 164,56,104
Tv 98,139,2048
MAavv 168,56,108
MAavv 172,56,112
Rv 32,142,14336
MAavv 176,56,116
MAavv 180,56,120
MAavv 184,56,124
buffer_load_dwordx4 v149, s[28:31], s50 offen offset:2048 lds
MAavv 188,56,128
MAavv 192,60,100
MAavv 196,60,104
MAavv 200,60,108
MAavv 204,60,112
MAavv 208,60,116
MAavv 212,60,120
buffer_load_dwordx4 v150, s[28:31], s50 offen offset:3072 lds
MAavv 216,60,124
MAavv 220,60,128
MAavv 224,64,100
MAavv 228,64,104
MAavv 232,64,108
MAavv 236,64,112
MAavv 240,64,116
MAavv 244,64,120
MAavv 248,64,124
MAavv 252,64,128
s_waitcnt vmcnt(16) lgkmcnt(0)
s_barrier
MAavv 0,4,68
Tv 100,132,16384
MAavv 4,4,72
Tv 102,132,18432
MAavv 8,4,76
s_add_u32 s65, s48, s49
s_add_u32 m0, s61, 4096
MAavv 12,4,80
Rv 36,141,0
MAavv 16,4,84
buffer_load_dwordx4 v143, s[24:27], s65 offen lds
MAavv 20,4,88
Tv 104,133,16384
MAavv 24,4,92
MAavv 28,4,96
Tv 106,133,18432
MAavv 32,8,68
Rv 40,141,2048
MAavv 36,8,72
MAavv 40,8,76
Tv 108,134,16384
MAavv 44,8,80
buffer_load_dwordx4 v144, s[24:27], s65 offen offset:1024 lds
MAavv 48,8,84
Tv 110,134,18432
MAavv 52,8,88
MAavv 56,8,92
Rv 44,141,4096
MAavv 60,8,96
Tv 112,135,16384
MAavv 64,12,68
MAavv 68,12,72
Tv 114,135,18432
MAavv 72,12,76
buffer_load_dwordx4 v145, s[24:27], s65 offen offset:2048 lds
MAavv 76,12,80
Rv 48,141,6144
MAavv 80,12,84
MAavv 84,12,88
Tv 116,136,16384
MAavv 88,12,92
Tv 118,136,18432
MAavv 92,12,96
MAavv 96,16,68
Rv 52,141,8192
MAavv 100,16,72
buffer_load_dwordx4 v146, s[24:27], s65 offen offset:3072 lds
MAavv 104,16,76
Tv 120,137,16384
MAavv 108,16,80
s_add_u32 s50, s50, s51
s_cmp_eq_u32 s50, s53
s_cselect_b32 s50, 0, s50
MAavv 112,16,84
Tv 122,137,18432
MAavv 116,16,88
Rv 56,141,10240
MAavv 120,16,92
s_add_u32 s62, s62, 16384
s_and_b32 s62, s62, 0xffff
s_mov_b32 m0, s62
MAavv 124,16,96
Tv 124,138,16384
MAavv 128,20,68
buffer_load_dwordx4 v147, s[28:31], s50 offen lds
MAavv 132,20,72
Tv 126,138,18432
MAavv 136,20,76
MAavv 140,20,80
Rv 60,141,12288
MAavv 144,20,84
Tv 128,139,16384
MAavv 148,20,88
MAavv 152,20,92
Tv 130,139,18432
MAavv 156,20,96
buffer_load_dwordx4 v148, s[28:31], s50 offen offset:1024 lds
MAavv 160,24,68
Rv 64,141,14336
MAavv 164,24,72
MAavv 168,24,76
MAavv 172,24,80
MAavv 176,24,84
MAavv 180,24,88
MAavv 184,24,92
buffer_load_dwordx4 v149, s[28:31], s50 offen offset:2048 lds
MAavv 188,24,96
MAavv 192,28,68
MAavv 196,28,72
MAavv 200,28,76
MAavv 204,28,80
MAavv 208,28,84
MAavv 212,28,88
buffer_load_dwordx4 v150, s[28:31], s50 offen offset:3072 lds
MAavv 216,28,92
MAavv 220,28,96
MAavv 224,32,68
MAavv 228,32,72
MAavv 232,32,76
MAavv 236,32,80
MAavv 240,32,84
MAavv 244,32,88
MAavv 248,32,92
MAavv 252,32,96
s_waitcnt vmcnt(16) lgkmcnt(0)
s_barrier
MAavv 0,36,100
s_add_u32 s63, s63, 32768
s_cmp_eq_u32 s63, 163840
s_cselect_b32 s63, 65536, s63
MAavv 4,36,104
v_add_u32 v142, s63, v140
v_xor_b32 v141, 64, v142
MAavv 8,36,108
v_xor_b32 v132, 0x8000, v132
v_xor_b32 v133, 0x8000, v133
MAavv 12,36,112
v_xor_b32 v134, 0x8000, v134
v_xor_b32 v135, 0x8000, v135
MAavv 16,36,116
v_xor_b32 v136, 0x8000, v136
v_xor_b32 v137, 0x8000, v137
MAavv 20,36,120
v_xor_b32 v138, 0x8000, v138
v_xor_b32 v139, 0x8000, v139
MAavv 24,36,124
Tv 68,132,0
MAavv 28,36,128
Tv 70,132,2048
MAavv 32,40,100
Rv 4,142,0
MAavv 36,40,104
Tv 72,133,0
MAavv 40,40,108
Tv 74,133,2048
MAavv 44,40,112
Rv 8,142,2048
MAavv 48,40,116
s_add_u32 s48, s48, 128
s_cmp_eq_u32 s48, s52
s_cselect_b32 s48, 0, s48
MAavv 52,40,120
Tv 76,134,0
MAavv 56,40,124
s_add_u32 s61, s61, 32768
s_cmp_ge_u32 s61, 163840
s_cselect_b32 s64, 98304, 0
MAavv 60,40,128
Tv 78,134,2048
s_sub_u32 s61, s61, s64
s_mov_b32 m0, s61
MAavv 64,44,100
buffer_load_dwordx4 v143, s[24:27], s48 offen lds
MAavv 68,44,104
Rv 12,142,4096
MAavv 72,44,108
Tv 80,135,0
MAavv 76,44,112
buffer_load_dwordx4 v144, s[24:27], s48 offen offset:1024 lds
MAavv 80,44,116
Tv 82,135,2048
MAavv 84,44,120
buffer_load_dwordx4 v145, s[24:27], s48 offen offset:2048 lds
MAavv 88,44,124
Rv 16,142,6144
MAavv 92,44,128
MAavv 96,48,100
Tv 84,136,0
MAavv 100,48,104
Tv 86,136,2048
MAavv 104,48,108
buffer_load_dwordx4 v146, s[24:27], s48 offen offset:3072 lds
MAavv 108,48,112
Rv 20,142,8192
MAavv 112,48,116
s_add_u32 s50, s50, s51
s_cmp_eq_u32 s50, s53
s_cselect_b32 s50, 0, s50
MAavv 116,48,120
Tv 88,137,0
MAavv 120,48,124
s_add_u32 s62, s62, 16384
s_and_b32 s62, s62, 0xffff
s_mov_b32 m0, s62
MAavv 124,48,128
Tv 90,137,2048
MAavv 128,52,100
Rv 24,142,10240
MAavv 132,52,104
buffer_load_dwordx4 v147, s[28:31], s50 offen lds
MAavv 136,52,108
Tv 92,138,0
MAavv 140,52,112
MAavv 144,52,116
Tv 94,138,2048
MAavv 148,52,120
MAavv 152,52,124
Rv 28,142,12288
MAavv 156,52,128
Tv 96,139,0
MAavv 160,56,100
buffer_load_dwordx4 v148, s[28:31], s50 offen offset:1024 lds
MAavv 164,56,104
Tv 98,139,2048
MAavv 168,56,108
MAavv 172,56,112
Rv 32,142,14336
MAavv 176,56,116
MAavv 180,56,120
MAavv 184,56,124
buffer_load_dwordx4 v149, s[28:31], s50 offen offset:2048 lds
MAavv 188,56,128
MAavv 192,60,100
MAavv 196,60,104
MAavv 200,60,108
MAavv 204,60,112
MAavv 208,60,116
MAavv 212,60,120
buffer_load_dwordx4 v150, s[28:31], s50 offen offset:3072 lds
MAavv 216,60,124
MAavv 220,60,128
MAavv 224,64,100
MAavv 228,64,104
MAavv 232,64,108
MAavv 236,64,112
MAavv 240,64,116
MAavv 244,64,120
MAavv 248,64,124
MAavv 252,64,128
s_mov_b32 s67, 5
Lu2V_loop:
s_waitcnt vmcnt(16) lgkmcnt(0)
s_barrier
MAavv 0,4,68
Tv 100,132,16384
MAavv 4,4,72
Tv 102,132,18432
MAavv 8,4,76
s_add_u32 s65, s48, s49
s_add_u32 m0, s61, 4096
MAavv 12,4,80
Rv 36,141,0
MAavv 16,4,84
buffer_load_dwordx4 v143, s[24:27], s65 offen lds
MAavv 20,4,88
Tv 104,133,16384
MAavv 24,4,92
MAavv 28,4,96
Tv 106,133,18432
MAavv 32,8,68
Rv 40,141,2048
MAavv 36,8,72
MAavv 40,8,76
Tv 108,134,16384
MAavv 44,8,80
buffer_load_dwordx4 v144, s[24:27], s65 offen offset:1024 lds
MAavv 48,8,84
Tv 110,134,18432
MAavv 52,8,88
MAavv 56,8,92
Rv 44,141,4096
MAavv 60,8,96
Tv 112,135,16384
MAavv 64,12,68
MAavv 68,12,72
Tv 114,135,18432
MAavv 72,12,76
buffer_load_dwordx4 v145, s[24:27], s65 offen offset:2048 lds
MAavv 76,12,80
Rv 48,141,6144
MAavv 80,12,84
MAavv 84,12,88
Tv 116,136,16384
MAavv 88,12,92
Tv 118,136,18432
MAavv 92,12,96
MAavv 96,16,68
Rv 52,141,8192
MAavv 100,16,72
buffer_load_dwordx4 v146, s[24:27], s65 offen offset:3072 lds
MAavv 104,16,76
Tv 120,137,16384
MAavv 108,16,80
s_add_u32 s50, s50, s51
s_cmp_eq_u32 s50, s53
s_cselect_b32 s50, 0, s50
MAavv 112,16,84
Tv 122,137,18432
MAavv 116,16,88
Rv 56,141,10240
MAavv 120,16,92
s_add_u32 s62, s62, 16384
s_and_b32 s62, s62, 0xffff
s_mov_b32 m0, s62
MAavv 124,16,96
Tv 124,138,16384
MAavv 128,20,68
buffer_load_dwordx4 v147, s[28:31], s50 offen lds
MAavv 132,20,72
Tv 126,138,18432
MAavv 136,20,76
MAavv 140,20,80
Rv 60,141,12288
MAavv 144,20,84
Tv 128,139,16384
MAavv 148,20,88
MAavv 152,20,92
Tv 130,139,18432
MAavv 156,20,96
buffer_load_dwordx4 v148, s[28:31], s50 offen offset:1024 lds
MAavv 160,24,68
Rv 64,141,14336
MAavv 164,24,72
MAavv 168,24,76
MAavv 172,24,80
MAavv 176,24,84
MAavv 180,24,88
MAavv 184,24,92
buffer_load_dwordx4 v149, s[28:31], s50 offen offset:2048 lds
MAavv 188,24,96
MAavv 192,28,68
MAavv 196,28,72
MAavv 200,28,76
MAavv 204,28,80
MAavv 208,28,84
MAavv 212,28,88
buffer_load_dwordx4 v150, s[28:31], s50 offen offset:3072 lds
MAavv 216,28,92
MAavv 220,28,96
MAavv 224,32,68
MAavv 228,32,72
MAavv 232,32,76
MAavv 236,32,80
MAavv 240,32,84
MAavv 244,32,88
MAavv 248,32,92
MAavv 252,32,96
s_waitcnt vmcnt(16) lgkmcnt(0)
s_barrier
MAavv 0,36,100
s_add_u32 s63, s63, 32768
s_cmp_eq_u32 s63, 163840
s_cselect_b32 s63, 65536, s63
MAavv 4,36,104
v_add_u32 v142, s63, v140
v_xor_b32 v141, 64, v142
MAavv 8,36,108
v_xor_b32 v132, 0x8000, v132
v_xor_b32 v133, 0x8000, v133
MAavv 12,36,112
v_xor_b32 v134, 0x8000, v134
v_xor_b32 v135, 0x8000, v135
MAavv 16,36,116
v_xor_b32 v136, 0x8000, v136
v_xor_b32 v137, 0x8000, v137
MAavv 20,36,120
v_xor_b32 v138, 0x8000, v138
v_xor_b32 v139, 0x8000, v139
MAavv 24,36,124
Tv 68,132,0
MAavv 28,36,128
Tv 70,132,2048
MAavv 32,40,100
Rv 4,142,0
MAavv 36,40,104
Tv 72,133,0
MAavv 40,40,108
Tv 74,133,2048
MAavv 44,40,112
Rv 8,142,2048
MAavv 48,40,116
s_add_u32 s48, s48, 128
s_cmp_eq_u32 s48, s52
s_cselect_b32 s48, 0, s48
MAavv 52,40,120
Tv 76,134,0
MAavv 56,40,124
s_add_u32 s61, s61, 32768
s_cmp_ge_u32 s61, 163840
s_cselect_b32 s64, 98304, 0
MAavv 60,40,128
Tv 78,134,2048
s_sub_u32 s61, s61, s64
s_mov_b32 m0, s61
MAavv 64,44,100
buffer_load_dwordx4 v143, s[24:27], s48 offen lds
MAavv 68,44,104
Rv 12,142,4096
MAavv 72,44,108
Tv 80,135,0
MAavv 76,44,112
buffer_load_dwordx4 v144, s[24:27], s48 offen offset:1024 lds
MAavv 80,44,116
Tv 82,135,2048
MAavv 84,44,120
buffer_load_dwordx4 v145, s[24:27], s48 offen offset:2048 lds
MAavv 88,44,124
Rv 16,142,6144
MAavv 92,44,128
MAavv 96,48,100
Tv 84,136,0
MAavv 100,48,104
Tv 86,136,2048
MAavv 104,48,108
buffer_load_dwordx4 v146, s[24:27], s48 offen offset:3072 lds
MAavv 108,48,112
Rv 20,142,8192
MAavv 112,48,116
s_add_u32 s50, s50, s51
s_cmp_eq_u32 s50, s53
s_cselect_b32 s50, 0, s50
MAavv 116,48,120
Tv 88,137,0
MAavv 120,48,124
s_add_u32 s62, s62, 16384
s_and_b32 s62, s62, 0xffff
s_mov_b32 m0, s62
MAavv 124,48,128
Tv 90,137,2048
MAavv 128,52,100
Rv 24,142,10240
MAavv 132,52,104
buffer_load_dwordx4 v147, s[28:31], s50 offen lds
MAavv 136,52,108
Tv 92,138,0
MAavv 140,52,112
MAavv 144,52,116
Tv 94,138,2048
MAavv 148,52,120
MAavv 152,52,124
Rv 28,142,12288
MAavv 156,52,128
Tv 96,139,0
MAavv 160,56,100
buffer_load_dwordx4 v148, s[28:31], s50 offen offset:1024 lds
MAavv 164,56,104
Tv 98,139,2048
MAavv 168,56,108
MAavv 172,56,112
Rv 32,142,14336
MAavv 176,56,116
MAavv 180,56,120
MAavv 184,56,124
buffer_load_dwordx4 v149, s[28:31], s50 offen offset:2048 lds
MAavv 188,56,128
MAavv 192,60,100
MAavv 196,60,104
MAavv 200,60,108
MAavv 204,60,112
MAavv 208,60,116
MAavv 212,60,120
buffer_load_dwordx4 v150, s[28:31], s50 offen offset:3072 lds
MAavv 216,60,124
MAavv 220,60,128
MAavv 224,64,100
MAavv 228,64,104
MAavv 232,64,108
MAavv 236,64,112
MAavv 240,64,116
MAavv 244,64,120
MAavv 248,64,124
MAavv 252,64,128
s_waitcnt vmcnt(16) lgkmcnt(0)
s_barrier
MAavv 0,4,68
Tv 100,132,16384
MAavv 4,4,72
Tv 102,132,18432
MAavv 8,4,76
s_add_u32 s65, s48, s49
s_add_u32 m0, s61, 4096
MAavv 12,4,80
Rv 36,141,0
MAavv 16,4,84
buffer_load_dwordx4 v143, s[24:27], s65 offen lds
MAavv 20,4,88
Tv 104,133,16384
MAavv 24,4,92
MAavv 28,4,96
Tv 106,133,18432
MAavv 32,8,68
Rv 40,141,2048
MAavv 36,8,72
MAavv 40,8,76
Tv 108,134,16384
MAavv 44,8,80
buffer_load_dwordx4 v144, s[24:27], s65 offen offset:1024 lds
MAavv 48,8,84
Tv 110,134,18432
MAavv 52,8,88
MAavv 56,8,92
Rv 44,141,4096
MAavv 60,8,96
Tv 112,135,16384
MAavv 64,12,68
MAavv 68,12,72
Tv 114,135,18432
MAavv 72,12,76
buffer_load_dwordx4 v145, s[24:27], s65 offen offset:2048 lds
MAavv 76,12,80
Rv 48,141,6144
MAavv 80,12,84
MAavv 84,12,88
Tv 116,136,16384
MAavv 88,12,92
Tv 118,136,18432
MAavv 92,12,96
MAavv 96,16,68
Rv 52,141,8192
MAavv 100,16,72
buffer_load_dwordx4 v146, s[24:27], s65 offen offset:3072 lds
MAavv 104,16,76
Tv 120,137,16384
MAavv 108,16,80
s_add_u32 s50, s50, s51
s_cmp_eq_u32 s50, s53
s_cselect_b32 s50, 0, s50
MAavv 112,16,84
Tv 122,137,18432
MAavv 116,16,88
Rv 56,141,10240
MAavv 120,16,92
s_add_u32 s62, s62, 16384
s_and_b32 s62, s62, 0xffff
s_mov_b32 m0, s62
MAavv 124,16,96
Tv 124,138,16384
MAavv 128,20,68
buffer_load_dwordx4 v147, s[28:31], s50 offen lds
MAavv 132,20,72
Tv 126,138,18432
MAavv 136,20,76
MAavv 140,20,80
Rv 60,141,12288
MAavv 144,20,84
Tv 128,139,16384
MAavv 148,20,88
MAavv 152,20,92
Tv 130,139,18432
MAavv 156,20,96
buffer_load_dwordx4 v148, s[28:31], s50 offen offset:1024 lds
MAavv 160,24,68
Rv 64,141,14336
MAavv 164,24,72
MAavv 168,24,76
MAavv 172,24,80
MAavv 176,24,84
MAavv 180,24,88
MAavv 184,24,92
buffer_load_dwordx4 v149, s[28:31], s50 offen offset:2048 lds
MAavv 188,24,96
MAavv 192,28,68
MAavv 196,28,72
MAavv 200,28,76
MAavv 204,28,80
MAavv 208,28,84
MAavv 212,28,88
buffer_load_dwordx4 v150, s[28:31], s50 offen offset:3072 lds
MAavv 216,28,92
MAavv 220,28,96
MAavv 224,32,68
MAavv 228,32,72
MAavv 232,32,76
MAavv 236,32,80
MAavv 240,32,84
MAavv 244,32,88
MAavv 248,32,92
MAavv 252,32,96
s_waitcnt vmcnt(16) lgkmcnt(0)
s_barrier
MAavv 0,36,100
s_add_u32 s63, s63, 32768
s_cmp_eq_u32 s63, 163840
s_cselect_b32 s63, 65536, s63
MAavv 4,36,104
v_add_u32 v142, s63, v140
v_xor_b32 v141, 64, v142
MAavv 8,36,108
v_xor_b32 v132, 0x8000, v132
v_xor_b32 v133, 0x8000, v133
MAavv 12,36,112
v_xor_b32 v134, 0x8000, v134
v_xor_b32 v135, 0x8000, v135
MAavv 16,36,116
v_xor_b32 v136, 0x8000, v136
v_xor_b32 v137, 0x8000, v137
MAavv 20,36,120
v_xor_b32 v138, 0x8000, v138
v_xor_b32 v139, 0x8000, v139
MAavv 24,36,124
Tv 68,132,0
MAavv 28,36,128
Tv 70,132,2048
MAavv 32,40,100
Rv 4,142,0
MAavv 36,40,104
Tv 72,133,0
MAavv 40,40,108
Tv 74,133,2048
MAavv 44,40,112
Rv 8,142,2048
MAavv 48,40,116
s_add_u32 s48, s48, 128
s_cmp_eq_u32 s48, s52
s_cselect_b32 s48, 0, s48
MAavv 52,40,120
Tv 76,134,0
MAavv 56,40,124
s_add_u32 s61, s61, 32768
s_cmp_ge_u32 s61, 163840
s_cselect_b32 s64, 98304, 0
MAavv 60,40,128
Tv 78,134,2048
s_sub_u32 s61, s61, s64
s_mov_b32 m0, s61
MAavv 64,44,100
buffer_load_dwordx4 v143, s[24:27], s48 offen lds
MAavv 68,44,104
Rv 12,142,4096
MAavv 72,44,108
Tv 80,135,0
MAavv 76,44,112
buffer_load_dwordx4 v144, s[24:27], s48 offen offset:1024 lds
MAavv 80,44,116
Tv 82,135,2048
MAavv 84,44,120
buffer_load_dwordx4 v145, s[24:27], s48 offen offset:2048 lds
MAavv 88,44,124
Rv 16,142,6144
MAavv 92,44,128
MAavv 96,48,100
Tv 84,136,0
MAavv 100,48,104
Tv 86,136,2048
MAavv 104,48,108
buffer_load_dwordx4 v146, s[24:27], s48 offen offset:3072 lds
MAavv 108,48,112
Rv 20,142,8192
MAavv 112,48,116
s_add_u32 s50, s50, s51
s_cmp_eq_u32 s50, s53
s_cselect_b32 s50, 0, s50
MAavv 116,48,120
Tv 88,137,0
MAavv 120,48,124
s_add_u32 s62, s62, 16384
s_and_b32 s62, s62, 0xffff
s_mov_b32 m0, s62
MAavv 124,48,128
Tv 90,137,2048
MAavv 128,52,100
Rv 24,142,10240
MAavv 132,52,104
buffer_load_dwordx4 v147, s[28:31], s50 offen lds
MAavv 136,52,108
Tv 92,138,0
MAavv 140,52,112
MAavv 144,52,116
Tv 94,138,2048
MAavv 148,52,120
MAavv 152,52,124
Rv 28,142,12288
MAavv 156,52,128
Tv 96,139,0
MAavv 160,56,100
buffer_load_dwordx4 v148, s[28:31], s50 offen offset:1024 lds
MAavv 164,56,104
Tv 98,139,2048
MAavv 168,56,108
MAavv 172,56,112
Rv 32,142,14336
MAavv 176,56,116
MAavv 180,56,120
MAavv 184,56,124
buffer_load_dwordx4 v149, s[28:31], s50 offen offset:2048 lds
MAavv 188,56,128
MAavv 192,60,100
MAavv 196,60,104
MAavv 200,60,108
MAavv 204,60,112
MAavv 208,60,116
MAavv 212,60,120
buffer_load_dwordx4 v150, s[28:31], s50 offen offset:3072 lds
MAavv 216,60,124
MAavv 220,60,128
MAavv 224,64,100
MAavv 228,64,104
MAavv 232,64,108
MAavv 236,64,112
MAavv 240,64,116
MAavv 244,64,120
MAavv 248,64,124
MAavv 252,64,128
s_sub_u32 s67, s67, 1
s_cmp_lg_u32 s67, 0
s_cbranch_scc1 Lu2V_loop
s_waitcnt vmcnt(16) lgkmcnt(0)
s_barrier
MAavv 0,4,68
Tv 100,132,16384
MAavv 4,4,72
Tv 102,132,18432
MAavv 8,4,76
s_add_u32 s65, s48, s49
s_add_u32 m0, s61, 4096
MAavv 12,4,80
Rv 36,141,0
MAavv 16,4,84
buffer_load_dwordx4 v143, s[24:27], s65 offen lds
MAavv 20,4,88
Tv 104,133,16384
MAavv 24,4,92
MAavv 28,4,96
Tv 106,133,18432
MAavv 32,8,68
Rv 40,141,2048
MAavv 36,8,72
MAavv 40,8,76
Tv 108,134,16384
MAavv 44,8,80
buffer_load_dwordx4 v144, s[24:27], s65 offen offset:1024 lds
MAavv 48,8,84
Tv 110,134,18432
MAavv 52,8,88
MAavv 56,8,92
Rv 44,141,4096
MAavv 60,8,96
Tv 112,135,16384
MAavv 64,12,68
MAavv 68,12,72
Tv 114,135,18432
MAavv 72,12,76
buffer_load_dwordx4 v145, s[24:27], s65 offen offset:2048 lds
MAavv 76,12,80
Rv 48,141,6144
MAavv 80,12,84
MAavv 84,12,88
Tv 116,136,16384
MAavv 88,12,92
Tv 118,136,18432
MAavv 92,12,96
MAavv 96,16,68
Rv 52,141,8192
MAavv 100,16,72
buffer_load_dwordx4 v146, s[24:27], s65 offen offset:3072 lds
MAavv 104,16,76
Tv 120,137,16384
MAavv 108,16,80
s_add_u32 s50, s50, s51
s_cmp_eq_u32 s50, s53
s_cselect_b32 s50, 0, s50
MAavv 112,16,84
Tv 122,137,18432
MAavv 116,16,88
Rv 56,141,10240
MAavv 120,16,92
s_add_u32 s62, s62, 16384
s_and_b32 s62, s62, 0xffff
s_mov_b32 m0, s62
MAavv 124,16,96
Tv 124,138,16384
MAavv 128,20,68
buffer_load_dwordx4 v147, s[28:31], s50 offen lds
MAavv 132,20,72
Tv 126,138,18432
MAavv 136,20,76
MAavv 140,20,80
)ASM"
R"ASM(Rv 60,141,12288
MAavv 144,20,84
Tv 128,139,16384
MAavv 148,20,88
MAavv 152,20,92
Tv 130,139,18432
MAavv 156,20,96
buffer_load_dwordx4 v148, s[28:31], s50 offen offset:1024 lds
MAavv 160,24,68
Rv 64,141,14336
MAavv 164,24,72
MAavv 168,24,76
MAavv 172,24,80
MAavv 176,24,84
MAavv 180,24,88
MAavv 184,24,92
buffer_load_dwordx4 v149, s[28:31], s50 offen offset:2048 lds
MAavv 188,24,96
MAavv 192,28,68
MAavv 196,28,72
MAavv 200,28,76
MAavv 204,28,80
MAavv 208,28,84
MAavv 212,28,88
buffer_load_dwordx4 v150, s[28:31], s50 offen offset:3072 lds
MAavv 216,28,92
MAavv 220,28,96
MAavv 224,32,68
MAavv 228,32,72
MAavv 232,32,76
MAavv 236,32,80
MAavv 240,32,84
MAavv 244,32,88
MAavv 248,32,92
MAavv 252,32,96
s_waitcnt vmcnt(16) lgkmcnt(0)
s_barrier
MAavv 0,36,100
s_add_u32 s63, s63, 32768
s_cmp_eq_u32 s63, 163840
s_cselect_b32 s63, 65536, s63
MAavv 4,36,104
v_add_u32 v142, s63, v140
v_xor_b32 v141, 64, v142
MAavv 8,36,108
v_xor_b32 v132, 0x8000, v132
v_xor_b32 v133, 0x8000, v133
MAavv 12,36,112
v_xor_b32 v134, 0x8000, v134
v_xor_b32 v135, 0x8000, v135
MAavv 16,36,116
v_xor_b32 v136, 0x8000, v136
v_xor_b32 v137, 0x8000, v137
MAavv 20,36,120
v_xor_b32 v138, 0x8000, v138
v_xor_b32 v139, 0x8000, v139
MAavv 24,36,124
Tv 68,132,0
MAavv 28,36,128
Tv 70,132,2048
MAavv 32,40,100
Rv 4,142,0
MAavv 36,40,104
Tv 72,133,0
MAavv 40,40,108
Tv 74,133,2048
MAavv 44,40,112
Rv 8,142,2048
MAavv 48,40,116
s_mov_b64 s[24:25], s[36:37]
s_mov_b64 s[26:27], s[38:39]
s_mov_b32 s48, s58
MAavv 52,40,120
Tv 76,134,0
s_mov_b32 s49, s54
s_mov_b32 s52, s56
MAavv 56,40,124
v_mov_b32 v143, v151
v_mov_b32 v144, v152
MAavv 60,40,128
Tv 78,134,2048
v_mov_b32 v145, v153
MAavv 64,44,100
v_mov_b32 v146, v154
MAavv 68,44,104
Rv 12,142,4096
MAavv 72,44,108
Tv 80,135,0
MAavv 76,44,112
s_add_u32 s61, s61, 32768
s_cmp_ge_u32 s61, 163840
s_cselect_b32 s64, 98304, 0
MAavv 80,44,116
Tv 82,135,2048
s_sub_u32 s61, s61, s64
s_mov_b32 m0, s61
MAavv 84,44,120
buffer_load_dwordx4 v143, s[24:27], s48 offen lds
MAavv 88,44,124
Rv 16,142,6144
MAavv 92,44,128
buffer_load_dwordx4 v144, s[24:27], s48 offen offset:1024 lds
MAavv 96,48,100
Tv 84,136,0
MAavv 100,48,104
Tv 86,136,2048
MAavv 104,48,108
buffer_load_dwordx4 v145, s[24:27], s48 offen offset:2048 lds
MAavv 108,48,112
Rv 20,142,8192
MAavv 112,48,116
buffer_load_dwordx4 v146, s[24:27], s48 offen offset:3072 lds
MAavv 116,48,120
Tv 88,137,0
MAavv 120,48,124
s_add_u32 s50, s50, s51
s_cmp_eq_u32 s50, s53
s_cselect_b32 s50, 0, s50
MAavv 124,48,128
Tv 90,137,2048
MAavv 128,52,100
Rv 24,142,10240
MAavv 132,52,104
s_add_u32 s62, s62, 16384
s_and_b32 s62, s62, 0xffff
s_mov_b32 m0, s62
MAavv 136,52,108
Tv 92,138,0
MAavv 140,52,112
buffer_load_dwordx4 v147, s[28:31], s50 offen lds
MAavv 144,52,116
Tv 94,138,2048
MAavv 148,52,120
MAavv 152,52,124
Rv 28,142,12288
MAavv 156,52,128
Tv 96,139,0
MAavv 160,56,100
buffer_load_dwordx4 v148, s[28:31], s50 offen offset:1024 lds
MAavv 164,56,104
Tv 98,139,2048
MAavv 168,56,108
MAavv 172,56,112
Rv 32,142,14336
MAavv 176,56,116
MAavv 180,56,120
MAavv 184,56,124
buffer_load_dwordx4 v149, s[28:31], s50 offen offset:2048 lds
MAavv 188,56,128
MAavv 192,60,100
MAavv 196,60,104
MAavv 200,60,108
MAavv 204,60,112
MAavv 208,60,116
MAavv 212,60,120
buffer_load_dwordx4 v150, s[28:31], s50 offen offset:3072 lds
MAavv 216,60,124
MAavv 220,60,128
MAavv 224,64,100
MAavv 228,64,104
MAavv 232,64,108
MAavv 236,64,112
MAavv 240,64,116
MAavv 244,64,120
MAavv 248,64,124
MAavv 252,64,128
s_waitcnt vmcnt(16) lgkmcnt(0)
s_barrier
MAavv 0,4,68
Tv 100,132,16384
MAavv 4,4,72
Tv 102,132,18432
MAavv 8,4,76
s_add_u32 s65, s48, s49
s_add_u32 m0, s61, 4096
MAavv 12,4,80
Rv 36,141,0
MAavv 16,4,84
buffer_load_dwordx4 v143, s[24:27], s65 offen lds
MAavv 20,4,88
Tv 104,133,16384
MAavv 24,4,92
MAavv 28,4,96
Tv 106,133,18432
MAavv 32,8,68
Rv 40,141,2048
MAavv 36,8,72
MAavv 40,8,76
Tv 108,134,16384
MAavv 44,8,80
buffer_load_dwordx4 v144, s[24:27], s65 offen offset:1024 lds
MAavv 48,8,84
Tv 110,134,18432
MAavv 52,8,88
MAavv 56,8,92
Rv 44,141,4096
MAavv 60,8,96
Tv 112,135,16384
MAavv 64,12,68
MAavv 68,12,72
Tv 114,135,18432
MAavv 72,12,76
buffer_load_dwordx4 v145, s[24:27], s65 offen offset:2048 lds
MAavv 76,12,80
Rv 48,141,6144
MAavv 80,12,84
MAavv 84,12,88
Tv 116,136,16384
MAavv 88,12,92
Tv 118,136,18432
MAavv 92,12,96
MAavv 96,16,68
Rv 52,141,8192
MAavv 100,16,72
buffer_load_dwordx4 v146, s[24:27], s65 offen offset:3072 lds
MAavv 104,16,76
Tv 120,137,16384
MAavv 108,16,80
s_mov_b64 s[28:29], s[40:41]
s_mov_b64 s[30:31], s[42:43]
s_mov_b32 s50, s59
MAavv 112,16,84
Tv 122,137,18432
s_mov_b32 s51, s55
s_mov_b32 s53, s57
MAavv 116,16,88
Rv 56,141,10240
v_mov_b32 v147, v155
MAavv 120,16,92
v_mov_b32 v148, v156
v_mov_b32 v149, v157
MAavv 124,16,96
Tv 124,138,16384
v_mov_b32 v150, v158
MAavv 128,20,68
s_add_u32 s62, s62, 16384
s_and_b32 s62, s62, 0xffff
s_mov_b32 m0, s62
MAavv 132,20,72
Tv 126,138,18432
MAavv 136,20,76
buffer_load_dwordx4 v147, s[28:31], s50 offen lds
MAavv 140,20,80
Rv 60,141,12288
MAavv 144,20,84
Tv 128,139,16384
MAavv 148,20,88
MAavv 152,20,92
Tv 130,139,18432
MAavv 156,20,96
buffer_load_dwordx4 v148, s[28:31], s50 offen offset:1024 lds
MAavv 160,24,68
Rv 64,141,14336
MAavv 164,24,72
MAavv 168,24,76
MAavv 172,24,80
MAavv 176,24,84
MAavv 180,24,88
MAavv 184,24,92
buffer_load_dwordx4 v149, s[28:31], s50 offen offset:2048 lds
MAavv 188,24,96
MAavv 192,28,68
MAavv 196,28,72
MAavv 200,28,76
MAavv 204,28,80
MAavv 208,28,84
MAavv 212,28,88
buffer_load_dwordx4 v150, s[28:31], s50 offen offset:3072 lds
MAavv 216,28,92
MAavv 220,28,96
MAavv 224,32,68
MAavv 228,32,72
MAavv 232,32,76
MAavv 236,32,80
MAavv 240,32,84
MAavv 244,32,88
MAavv 248,32,92
MAavv 252,32,96
s_waitcnt vmcnt(16) lgkmcnt(0)
s_barrier
MAavv 0,36,100
s_add_u32 s63, s63, 32768
s_cmp_eq_u32 s63, 163840
s_cselect_b32 s63, 65536, s63
MAavv 4,36,104
v_add_u32 v142, s63, v140
v_xor_b32 v141, 64, v142
MAavv 8,36,108
v_xor_b32 v132, 0x8000, v132
v_xor_b32 v133, 0x8000, v133
MAavv 12,36,112
v_xor_b32 v134, 0x8000, v134
v_xor_b32 v135, 0x8000, v135
MAavv 16,36,116
v_xor_b32 v136, 0x8000, v136
v_xor_b32 v137, 0x8000, v137
MAavv 20,36,120
v_xor_b32 v138, 0x8000, v138
v_xor_b32 v139, 0x8000, v139
MAavv 24,36,124
Tv 68,132,0
MAavv 28,36,128
Tv 70,132,2048
MAavv 32,40,100
Rv 4,142,0
MAavv 36,40,104
Tv 72,133,0
MAavv 40,40,108
Tv 74,133,2048
MAavv 44,40,112
Rv 8,142,2048
MAavv 48,40,116
s_add_u32 s48, s48, 128
s_cmp_eq_u32 s48, s52
s_cselect_b32 s48, 0, s48
MAavv 52,40,120
Tv 76,134,0
MAavv 56,40,124
s_add_u32 s61, s61, 32768
s_cmp_ge_u32 s61, 163840
s_cselect_b32 s64, 98304, 0
MAavv 60,40,128
Tv 78,134,2048
s_sub_u32 s61, s61, s64
s_mov_b32 m0, s61
MAavv 64,44,100
buffer_load_dwordx4 v143, s[24:27], s48 offen lds
MAavv 68,44,104
Rv 12,142,4096
MAavv 72,44,108
Tv 80,135,0
MAavv 76,44,112
buffer_load_dwordx4 v144, s[24:27], s48 offen offset:1024 lds
MAavv 80,44,116
Tv 82,135,2048
MAavv 84,44,120
buffer_load_dwordx4 v145, s[24:27], s48 offen offset:2048 lds
MAavv 88,44,124
Rv 16,142,6144
MAavv 92,44,128
MAavv 96,48,100
Tv 84,136,0
MAavv 100,48,104
Tv 86,136,2048
MAavv 104,48,108
buffer_load_dwordx4 v146, s[24:27], s48 offen offset:3072 lds
MAavv 108,48,112
Rv 20,142,8192
MAavv 112,48,116
s_add_u32 s50, s50, s51
s_cmp_eq_u32 s50, s53
s_cselect_b32 s50, 0, s50
MAavv 116,48,120
Tv 88,137,0
MAavv 120,48,124
s_add_u32 s62, s62, 16384
s_and_b32 s62, s62, 0xffff
s_mov_b32 m0, s62
MAavv 124,48,128
Tv 90,137,2048
MAavv 128,52,100
Rv 24,142,10240
MAavv 132,52,104
buffer_load_dwordx4 v147, s[28:31], s50 offen lds
MAavv 136,52,108
Tv 92,138,0
MAavv 140,52,112
MAavv 144,52,116
Tv 94,138,2048
MAavv 148,52,120
MAavv 152,52,124
Rv 28,142,12288
MAavv 156,52,128
Tv 96,139,0
MAavv 160,56,100
buffer_load_dwordx4 v148, s[28:31], s50 offen offset:1024 lds
MAavv 164,56,104
Tv 98,139,2048
MAavv 168,56,108
MAavv 172,56,112
Rv 32,142,14336
MAavv 176,56,116
MAavv 180,56,120
MAavv 184,56,124
buffer_load_dwordx4 v149, s[28:31], s50 offen offset:2048 lds
MAavv 188,56,128
MAavv 192,60,100
MAavv 196,60,104
MAavv 200,60,108
MAavv 204,60,112
MAavv 208,60,116
MAavv 212,60,120
buffer_load_dwordx4 v150, s[28:31], s50 offen offset:3072 lds
MAavv 216,60,124
MAavv 220,60,128
MAavv 224,64,100
MAavv 228,64,104
MAavv 232,64,108
MAavv 236,64,112
MAavv 240,64,116
MAavv 244,64,120
MAavv 248,64,124
MAavv 252,64,128
s_waitcnt vmcnt(16) lgkmcnt(0)
s_barrier
MAavv 0,4,68
Tv 100,132,16384
MAavv 4,4,72
Tv 102,132,18432
MAavv 8,4,76
Rv 36,141,0
MAavv 12,4,80
Tv 104,133,16384
MAavv 16,4,84
Tv 106,133,18432
MAavv 20,4,88
Rv 40,141,2048
MAavv 24,4,92
Tv 108,134,16384
MAavv 28,4,96
Tv 110,134,18432
MAavv 32,8,68
Rv 44,141,4096
MAavv 36,8,72
Tv 112,135,16384
MAavv 40,8,76
Tv 114,135,18432
MAavv 44,8,80
Rv 48,141,6144
MAavv 48,8,84
MAavv 52,8,88
Tv 116,136,16384
MAavv 56,8,92
Tv 118,136,18432
MAavv 60,8,96
Rv 52,141,8192
MAavv 64,12,68
Tv 120,137,16384
MAavv 68,12,72
Tv 122,137,18432
MAavv 72,12,76
Rv 56,141,10240
MAavv 76,12,80
Tv 124,138,16384
MAavv 80,12,84
Tv 126,138,18432
MAavv 84,12,88
Rv 60,141,12288
MAavv 88,12,92
Tv 128,139,16384
MAavv 92,12,96
Tv 130,139,18432
MAavv 96,16,68
Rv 64,141,14336
MAavv 100,16,72
MAavv 104,16,76
MAavv 108,16,80
MAavv 112,16,84
MAavv 116,16,88
MAavv 120,16,92
MAavv 124,16,96
s_waitcnt vmcnt(63) lgkmcnt(0)
s_barrier
MAavv 0,36,100
MAavv 4,36,104
MAavv 8,36,108
MAavv 12,36,112
MAavv 16,36,116
MAavv 20,36,120
MAavv 24,36,124
MAavv 28,36,128
MAavv 32,40,100
MAavv 36,40,104
MAavv 40,40,108
MAavv 44,40,112
MAavv 48,40,116
MAavv 52,40,120
MAavv 56,40,124
MAavv 60,40,128
MAavv 64,44,100
MAavv 68,44,104
MAavv 72,44,108
MAavv 76,44,112
AR 172,0
AR 173,1
AR 174,2
AR 175,3
MAavv 80,44,116
AR 176,32
AR 177,33
AR 178,34
AR 179,35
MAavv 84,44,120
CV 204,172,173
CV 205,174,175
CV 206,176,177
CV 207,178,179
MAavv 88,44,124
s_mul_i32 s60, s73, 0
s_add_u32 s60, s60, s72
buffer_store_dwordx4 v[204:207], v159, s[44:47], s60 offen sc1
AR 180,4
AR 181,5
MAavv 92,44,128
AR 182,6
AR 183,7
AR 184,36
AR 185,37
MAavv 96,48,100
AR 186,38
AR 187,39
CV 208,180,181
CV 209,182,183
MAavv 100,48,104
CV 210,184,185
CV 211,186,187
buffer_store_dwordx4 v[208:211], v159, s[44:47], s60 offen offset:1024 sc1
MAavv 104,48,108
AR 188,8
AR 189,9
AR 190,10
AR 191,11
MAavv 108,48,112
AR 192,40
AR 193,41
AR 194,42
AR 195,43
MAavv 112,48,116
CV 212,188,189
CV 213,190,191
CV 214,192,193
CV 215,194,195
MAavv 116,48,120
buffer_store_dwordx4 v[212:215], v159, s[44:47], s60 offen offset:2048 sc1
AR 196,12
AR 197,13
MAavv 120,48,124
AR 198,14
AR 199,15
AR 200,44
AR 201,45
MAavv 124,48,128
AR 202,46
AR 203,47
CV 216,196,197
CV 217,198,199
MAavv 128,20,68
CV 218,200,201
CV 219,202,203
buffer_store_dwordx4 v[216:219], v159, s[44:47], s60 offen offset:3072 sc1
MAavv 132,20,72
AR 172,16
AR 173,17
AR 174,18
AR 175,19
MAavv 136,20,76
AR 176,48
AR 177,49
AR 178,50
AR 179,51
MAavv 140,20,80
CV 220,172,173
CV 221,174,175
CV 222,176,177
CV 223,178,179
MAavv 144,20,84
s_mul_i32 s60, s73, 0
s_add_u32 s60, s60, s72
s_add_u32 s60, s60, 4096
buffer_store_dwordx4 v[220:223], v159, s[44:47], s60 offen sc1
MAavv 148,20,88
AR 180,20
AR 181,21
AR 182,22
AR 183,23
MAavv 152,20,92
AR 184,52
AR 185,53
AR 186,54
AR 187,55
MAavv 156,20,96
CV 224,180,181
CV 225,182,183
CV 226,184,185
CV 227,186,187
MAavv 160,24,68
buffer_store_dwordx4 v[224:227], v159, s[44:47], s60 offen offset:1024 sc1
AR 188,24
AR 189,25
MAavv 164,24,72
AR 190,26
AR 191,27
AR 192,56
AR 193,57
MAavv 168,24,76
AR 194,58
AR 195,59
CV 228,188,189
CV 229,190,191
MAavv 172,24,80
CV 230,192,193
CV 231,194,195
buffer_store_dwordx4 v[228:231], v159, s[44:47], s60 offen offset:2048 sc1
MAavv 176,24,84
AR 196,28
AR 197,29
AR 198,30
AR 199,31
MAavv 180,24,88
AR 200,60
AR 201,61
AR 202,62
AR 203,63
MAavv 184,24,92
CV 232,196,197
CV 233,198,199
CV 234,200,201
CV 235,202,203
MAavv 188,24,96
buffer_store_dwordx4 v[232:235], v159, s[44:47], s60 offen offset:3072 sc1
AR 172,64
AR 173,65
MAavv 192,28,68
AR 174,66
AR 175,67
AR 176,96
AR 177,97
MAavv 196,28,72
AR 178,98
AR 179,99
CV 204,172,173
CV 205,174,175
MAavv 200,28,76
CV 206,176,177
CV 207,178,179
s_mul_i32 s60, s73, 1
s_add_u32 s60, s60, s72
buffer_store_dwordx4 v[204:207], v159, s[44:47], s60 offen sc1
MAavv 204,28,80
AR 180,68
AR 181,69
AR 182,70
AR 183,71
MAavv 208,28,84
AR 184,100
AR 185,101
AR 186,102
AR 187,103
MAavv 212,28,88
CV 208,180,181
CV 209,182,183
CV 210,184,185
CV 211,186,187
MAavv 216,28,92
buffer_store_dwordx4 v[208:211], v159, s[44:47], s60 offen offset:1024 sc1
AR 188,72
AR 189,73
MAavv 220,28,96
AR 190,74
AR 191,75
AR 192,104
AR 193,105
MAavv 224,32,68
AR 194,106
AR 195,107
CV 212,188,189
CV 213,190,191
MAavv 228,32,72
CV 214,192,193
CV 215,194,195
buffer_store_dwordx4 v[212:215], v159, s[44:47], s60 offen offset:2048 sc1
MAavv 232,32,76
AR 196,76
AR 197,77
AR 198,78
AR 199,79
MAavv 236,32,80
AR 200,108
AR 201,109
AR 202,110
AR 203,111
MAavv 240,32,84
CV 216,196,197
CV 217,198,199
CV 218,200,201
CV 219,202,203
MAavv 244,32,88
buffer_store_dwordx4 v[216:219], v159, s[44:47], s60 offen offset:3072 sc1
AR 172,80
AR 173,81
MAavv 248,32,92
AR 174,82
AR 175,83
AR 176,112
AR 177,113
MAavv 252,32,96
AR 178,114
AR 179,115
CV 220,172,173
CV 221,174,175
MAavv 128,52,100
CV 222,176,177
CV 223,178,179
AR 180,84
AR 181,85
MAavv 132,52,104
s_mul_i32 s60, s73, 1
s_add_u32 s60, s60, s72
s_add_u32 s60, s60, 4096
buffer_store_dwordx4 v[220:223], v159, s[44:47], s60 offen sc1
MAavv 136,52,108
AR 182,86
AR 183,87
AR 184,116
AR 185,117
MAavv 140,52,112
AR 186,118
AR 187,119
CV 224,180,181
CV 225,182,183
MAavv 144,52,116
CV 226,184,185
CV 227,186,187
buffer_store_dwordx4 v[224:227], v159, s[44:47], s60 offen offset:1024 sc1
MAavv 148,52,120
AR 188,88
AR 189,89
AR 190,90
AR 191,91
MAavv 152,52,124
AR 192,120
AR 193,121
AR 194,122
AR 195,123
MAavv 156,52,128
CV 228,188,189
CV 229,190,191
CV 230,192,193
CV 231,194,195
MAavv 160,56,100
buffer_store_dwordx4 v[228:231], v159, s[44:47], s60 offen offset:2048 sc1
AR 196,92
AR 197,93
MAavv 164,56,104
AR 198,94
AR 199,95
AR 200,124
AR 201,125
MAavv 168,56,108
AR 202,126
AR 203,127
CV 232,196,197
CV 233,198,199
MAavv 172,56,112
CV 234,200,201
CV 235,202,203
buffer_store_dwordx4 v[232:235], v159, s[44:47], s60 offen offset:3072 sc1
MAavv 176,56,116
MAavv 180,56,120
MAavv 184,56,124
MAavv 188,56,128
MAavv 192,60,100
MAavv 196,60,104
MAavv 200,60,108
MAavv 204,60,112
AR 172,128
AR 173,129
AR 174,130
AR 175,131
MAavv 208,60,116
AR 176,160
AR 177,161
AR 178,162
AR 179,163
MAavv 212,60,120
CV 204,172,173
CV 205,174,175
CV 206,176,177
CV 207,178,179
MAavv 216,60,124
s_mul_i32 s60, s73, 2
s_add_u32 s60, s60, s72
buffer_store_dwordx4 v[204:207], v159, s[44:47], s60 offen sc1
AR 180,132
AR 181,133
MAavv 220,60,128
AR 182,134
AR 183,135
AR 184,164
AR 185,165
MAavv 224,64,100
AR 186,166
AR 187,167
CV 208,180,181
CV 209,182,183
MAavv 228,64,104
CV 210,184,185
CV 211,186,187
buffer_store_dwordx4 v[208:211], v159, s[44:47], s60 offen offset:1024 sc1
MAavv 232,64,108
AR 188,136
AR 189,137
AR 190,138
AR 191,139
MAavv 236,64,112
AR 192,168
AR 193,169
AR 194,170
AR 195,171
MAavv 240,64,116
CV 212,188,189
CV 213,190,191
CV 214,192,193
CV 215,194,195
MAavv 244,64,120
buffer_store_dwordx4 v[212:215], v159, s[44:47], s60 offen offset:2048 sc1
AR 196,140
AR 197,141
MAavv 248,64,124
AR 198,142
AR 199,143
AR 200,172
AR 201,173
AR 202,174
AR 203,175
CV 216,196,197
CV 217,198,199
CV 218,200,201
CV 219,202,203
buffer_store_dwordx4 v[216:219], v159, s[44:47], s60 offen offset:3072 sc1
AR 172,144
AR 173,145
AR 174,146
AR 175,147
AR 176,176
AR 177,177
AR 178,178
AR 179,179
CV 220,172,173
CV 221,174,175
CV 222,176,177
CV 223,178,179
s_mul_i32 s60, s73, 2
s_add_u32 s60, s60, s72
s_add_u32 s60, s60, 4096
buffer_store_dwordx4 v[220:223], v159, s[44:47], s60 offen sc1
AR 180,148
AR 181,149
AR 182,150
AR 183,151
AR 184,180
AR 185,181
AR 186,182
AR 187,183
CV 224,180,181
CV 225,182,183
CV 226,184,185
CV 227,186,187
buffer_store_dwordx4 v[224:227], v159, s[44:47], s60 offen offset:1024 sc1
AR 188,152
AR 189,153
AR 190,154
AR 191,155
AR 192,184
AR 193,185
AR 194,186
AR 195,187
CV 228,188,189
CV 229,190,191
CV 230,192,193
CV 231,194,195
buffer_store_dwordx4 v[228:231], v159, s[44:47], s60 offen offset:2048 sc1
AR 196,156
AR 197,157
AR 198,158
AR 199,159
AR 200,188
AR 201,189
AR 202,190
AR 203,191
CV 232,196,197
CV 233,198,199
CV 234,200,201
CV 235,202,203
buffer_store_dwordx4 v[232:235], v159, s[44:47], s60 offen offset:3072 sc1
MAavv 252,64,128
AR 172,192
AR 173,193
AR 174,194
AR 175,195
AR 176,224
AR 177,225
AR 178,226
AR 179,227
CV 204,172,173
CV 205,174,175
CV 206,176,177
CV 207,178,179
s_mul_i32 s60, s73, 3
s_add_u32 s60, s60, s72
buffer_store_dwordx4 v[204:207], v159, s[44:47], s60 offen sc1
AR 180,196
AR 181,197
AR 182,198
AR 183,199
AR 184,228
AR 185,229
AR 186,230
AR 187,231
CV 208,180,181
CV 209,182,183
CV 210,184,185
CV 211,186,187
buffer_store_dwordx4 v[208:211], v159, s[44:47], s60 offen offset:1024 sc1
AR 188,200
AR 189,201
AR 190,202
AR 191,203
AR 192,232
AR 193,233
AR 194,234
AR 195,235
CV 212,188,189
CV 213,190,191
CV 214,192,193
CV 215,194,195
buffer_store_dwordx4 v[212:215], v159, s[44:47], s60 offen offset:2048 sc1
AR 196,204
AR 197,205
AR 198,206
AR 199,207
AR 200,236
AR 201,237
AR 202,238
AR 203,239
CV 216,196,197
CV 217,198,199
CV 218,200,201
CV 219,202,203
buffer_store_dwordx4 v[216:219], v159, s[44:47], s60 offen offset:3072 sc1
AR 172,208
AR 173,209
AR 174,210
AR 175,211
AR 176,240
AR 177,241
AR 178,242
AR 179,243
CV 220,172,173
CV 221,174,175
CV 222,176,177
CV 223,178,179
s_mul_i32 s60, s73, 3
s_add_u32 s60, s60, s72
s_add_u32 s60, s60, 4096
buffer_store_dwordx4 v[220:223], v159, s[44:47], s60 offen sc1
AR 180,212
AR 181,213
AR 182,214
AR 183,215
AR 184,244
AR 185,245
AR 186,246
AR 187,247
CV 224,180,181
CV 225,182,183
CV 226,184,185
CV 227,186,187
buffer_store_dwordx4 v[224:227], v159, s[44:47], s60 offen offset:1024 sc1
AR 188,216
AR 189,217
AR 190,218
AR 191,219
AR 192,248
AR 193,249
AR 194,250
AR 195,251
CV 228,188,189
CV 229,190,191
CV 230,192,193
CV 231,194,195
buffer_store_dwordx4 v[228:231], v159, s[44:47], s60 offen offset:2048 sc1
AR 196,220
AR 197,221
AR 198,222
AR 199,223
AR 200,252
AR 201,253
AR 202,254
AR 203,255
CV 232,196,197
CV 233,198,199
CV 234,200,201
CV 235,202,203
buffer_store_dwordx4 v[232:235], v159, s[44:47], s60 offen offset:3072 sc1
Lk1_end:
s_waitcnt vmcnt(0) lgkmcnt(0)
s_waitcnt vmcnt(0)
v_mov_b32 %0, v168
.purgem MZaaa
.purgem MAaaa
.purgem MZaav
.purgem MAaav
.purgem MZava
.purgem MAava
.purgem MZavv
.purgem MAavv
.purgem MZvaa
.purgem MAvaa
.purgem MZvav
.purgem MAvav
.purgem MZvva
.purgem MAvva
.purgem MZvvv
.purgem MAvvv
.purgem Ra
.purgem Ta
.purgem Rv
.purgem Tv
.purgem AR
.purgem CV
.purgem EX
)ASM"
  : "+v"(tid) : "s"(ka), "s"(bid), "s"((unsigned)(size_t)lds) :
"v1", "v2", "v3", "v4", "v5", "v6", "v7", "v8", "v9", "v10", "v11", "v12", "v13", "v14", "v15", "v16", "v17", "v18", "v19", "v20", "v21", "v22", "v23", "v24",
"v25", "v26", "v27", "v28", "v29", "v30", "v31", "v32", "v33", "v34", "v35", "v36", "v37", "v38", "v39", "v40", "v41", "v42", "v43", "v44", "v45", "v46", "v47", "v48",
"v49", "v50", "v51", "v52", "v53", "v54", "v55", "v56", "v57", "v58", "v59", "v60", "v61", "v62", "v63", "v64", "v65", "v66", "v67", "v68", "v69", "v70", "v71", "v72",
"v73", "v74", "v75", "v76", "v77", "v78", "v79", "v80", "v81", "v82", "v83", "v84", "v85", "v86", "v87", "v88", "v89", "v90", "v91", "v92", "v93", "v94", "v95", "v96",
"v97", "v98", "v99", "v100", "v101", "v102", "v103", "v104", "v105", "v106", "v107", "v108", "v109", "v110", "v111", "v112", "v113", "v114", "v115", "v116", "v117", "v118", "v119", "v120",
"v121", "v122", "v123", "v124", "v125", "v126", "v127", "v128", "v129", "v130", "v131", "v132", "v133", "v134", "v135", "v136", "v137", "v138", "v139", "v140", "v141", "v142", "v143", "v144",
"v145", "v146", "v147", "v148", "v149", "v150", "v151", "v152", "v153", "v154", "v155", "v156", "v157", "v158", "v159", "v160", "v161", "v162", "v163", "v164", "v165", "v166", "v167", "v168",
"v169", "v170", "v171", "v172", "v173", "v174", "v175", "v176", "v177", "v178", "v179", "v180", "v181", "v182", "v183", "v184", "v185", "v186", "v187", "v188", "v189", "v190", "v191", "v192",
"v193", "v194", "v195", "v196", "v197", "v198", "v199", "v200", "v201", "v202", "v203", "v204", "v205", "v206", "v207", "v208", "v209", "v210", "v211", "v212", "v213", "v214", "v215", "v216",
"v217", "v218", "v219", "v220", "v221", "v222", "v223", "v224", "v225", "v226", "v227", "v228", "v229", "v230", "v231", "v232", "v233", "v234", "v235", "v236", "v237", "v238", "v239", "v240",
"v241", "v242", "v243", "v244", "v245", "v246", "v247", "v248", "v249", "v250", "v251", "v252", "v253", "v254", "v255", "a0", "a1", "a2", "a3", "a4", "a5", "a6", "a7", "a8",
"a9", "a10", "a11", "a12", "a13", "a14", "a15", "a16", "a17", "a18", "a19", "a20", "a21", "a22", "a23", "a24", "a25", "a26", "a27", "a28", "a29", "a30", "a31", "a32",
"a33", "a34", "a35", "a36", "a37", "a38", "a39", "a40", "a41", "a42", "a43", "a44", "a45", "a46", "a47", "a48", "a49", "a50", "a51", "a52", "a53", "a54", "a55", "a56",
"a57", "a58", "a59", "a60", "a61", "a62", "a63", "a64", "a65", "a66", "a67", "a68", "a69", "a70", "a71", "a72", "a73", "a74", "a75", "a76", "a77", "a78", "a79", "a80",
"a81", "a82", "a83", "a84", "a85", "a86", "a87", "a88", "a89", "a90", "a91", "a92", "a93", "a94", "a95", "a96", "a97", "a98", "a99", "a100", "a101", "a102", "a103", "a104",
"a105", "a106", "a107", "a108", "a109", "a110", "a111", "a112", "a113", "a114", "a115", "a116", "a117", "a118", "a119", "a120", "a121", "a122", "a123", "a124", "a125", "a126", "a127", "a128",
"a129", "a130", "a131", "a132", "a133", "a134", "a135", "a136", "a137", "a138", "a139", "a140", "a141", "a142", "a143", "a144", "a145", "a146", "a147", "a148", "a149", "a150", "a151", "a152",
"a153", "a154", "a155", "a156", "a157", "a158", "a159", "a160", "a161", "a162", "a163", "a164", "a165", "a166", "a167", "a168", "a169", "a170", "a171", "a172", "a173", "a174", "a175", "a176",
"a177", "a178", "a179", "a180", "a181", "a182", "a183", "a184", "a185", "a186", "a187", "a188", "a189", "a190", "a191", "a192", "a193", "a194", "a195", "a196", "a197", "a198", "a199", "a200",
"a201", "a202", "a203", "a204", "a205", "a206", "a207", "a208", "a209", "a210", "a211", "a212", "a213", "a214", "a215", "a216", "a217", "a218", "a219", "a220", "a221", "a222", "a223", "a224",
"a225", "a226", "a227", "a228", "a229", "a230", "a231", "a232", "a233", "a234", "a235", "a236", "a237", "a238", "a239", "a240", "a241", "a242", "a243", "a244", "a245", "a246", "a247", "a248",
"a249", "a250", "a251", "a252", "a253", "a254", "a255", "s8", "s9", "s10", "s11", "s12", "s13", "s14", "s15", "s16", "s17", "s18", "s19", "s20", "s21", "s22", "s23", "s24",
"s25", "s26", "s27", "s28", "s29", "s30", "s31", "s34", "s35", "s36", "s37", "s38", "s39", "s40", "s41", "s42", "s43", "s44", "s45", "s46", "s47", "s48", "s49", "s50",
"s51", "s52", "s53", "s54", "s55", "s56", "s57", "s58", "s59", "s60", "s61", "s62", "s63", "s64", "s65", "s66", "s67", "s68", "s69", "s70", "s71", "s72", "s73", "s74",
"s75", "s76", "s77", "s78", "s79", "s80", "s81", "s82", "s83", "s84", "s85", "s86", "s87", "s88", "s89", "s90", "s91", "s92", "s93", "s94", "s95", "s96", "s97", "s98",
"s99", "vcc", "scc", "memory"
  );
  if ((bid >> 3) >= 24u) {
    const __attribute__((address_space(4))) K1Args* q = (const __attribute__((address_space(4))) K1Args*)ka;
    asm volatile("" : "+s"(q));
    K1Args a1; a1.x = q->x; a1.ckv = q->ckv; a1.wq = q->wq; a1.wuk = q->wuk; a1.wuv = q->wuv; a1.win = q->win; a1.gw = q->gw; a1.ws = q->ws; a1.wo = q->wo; a1.wout = q->wout; a1.wsp = q->wsp;
    __syncthreads(); k1_side(a1, lds, (bid & 7u) + 8u * ((bid >> 3) - 24u), tid);
  }
}
__global__ __launch_bounds__(256, 1) void k2_mix(K2Args p) {
  __shared__ __attribute__((aligned(16))) unsigned char lds[163840];
  if ((blockIdx.x >> 3) >= 24u) k2_side(p, lds, (blockIdx.x & 7u) * 8u + ((blockIdx.x >> 3) - 24u));
  unsigned tid = threadIdx.x;
  const __attribute__((address_space(4))) void* ka = (const __attribute__((address_space(4))) void*)__builtin_amdgcn_kernarg_segment_ptr();
  unsigned bid = blockIdx.x;
  asm volatile(
R"ASM(.macro MZaaa d,a,b
v_mfma_f32_16x16x32_bf16 a[\d:\d+3], a[\a:\a+3], a[\b:\b+3], 0
.endm
.macro MAaaa d,a,b
v_mfma_f32_16x16x32_bf16 a[\d:\d+3], a[\a:\a+3], a[\b:\b+3], a[\d:\d+3]
.endm
.macro MZaav d,a,b
v_mfma_f32_16x16x32_bf16 a[\d:\d+3], a[\a:\a+3], v[\b:\b+3], 0
.endm
.macro MAaav d,a,b
v_mfma_f32_16x16x32_bf16 a[\d:\d+3], a[\a:\a+3], v[\b:\b+3], a[\d:\d+3]
.endm
.macro MZava d,a,b
v_mfma_f32_16x16x32_bf16 a[\d:\d+3], v[\a:\a+3], a[\b:\b+3], 0
.endm
.macro MAava d,a,b
v_mfma_f32_16x16x32_bf16 a[\d:\d+3], v[\a:\a+3], a[\b:\b+3], a[\d:\d+3]
.endm
.macro MZavv d,a,b
v_mfma_f32_16x16x32_bf16 a[\d:\d+3], v[\a:\a+3], v[\b:\b+3], 0
.endm
.macro MAavv d,a,b
v_mfma_f32_16x16x32_bf16 a[\d:\d+3], v[\a:\a+3], v[\b:\b+3], a[\d:\d+3]
.endm
.macro MZvaa d,a,b
v_mfma_f32_16x16x32_bf16 v[\d:\d+3], a[\a:\a+3], a[\b:\b+3], 0
.endm
.macro MAvaa d,a,b
v_mfma_f32_16x16x32_bf16 v[\d:\d+3], a[\a:\a+3], a[\b:\b+3], v[\d:\d+3]
.endm
.macro MZvav d,a,b
v_mfma_f32_16x16x32_bf16 v[\d:\d+3], a[\a:\a+3], v[\b:\b+3], 0
.endm
.macro MAvav d,a,b
v_mfma_f32_16x16x32_bf16 v[\d:\d+3], a[\a:\a+3], v[\b:\b+3], v[\d:\d+3]
.endm
.macro MZvva d,a,b
v_mfma_f32_16x16x32_bf16 v[\d:\d+3], v[\a:\a+3], a[\b:\b+3], 0
.endm
.macro MAvva d,a,b
v_mfma_f32_16x16x32_bf16 v[\d:\d+3], v[\a:\a+3], a[\b:\b+3], v[\d:\d+3]
.endm
.macro MZvvv d,a,b
v_mfma_f32_16x16x32_bf16 v[\d:\d+3], v[\a:\a+3], v[\b:\b+3], 0
.endm
.macro MAvvv d,a,b
v_mfma_f32_16x16x32_bf16 v[\d:\d+3], v[\a:\a+3], v[\b:\b+3], v[\d:\d+3]
.endm
.macro Ra d,a,o
ds_read_b128 a[\d:\d+3], v\a offset:\o
.endm
.macro Ta d,a,o
ds_read_b64_tr_b16 a[\d:\d+1], v\a offset:\o
.endm
.macro Rv d,a,o
ds_read_b128 v[\d:\d+3], v\a offset:\o
.endm
.macro Tv d,a,o
ds_read_b64_tr_b16 v[\d:\d+1], v\a offset:\o
.endm
.macro AR d,a
v_accvgpr_read_b32 v\d, a\a
.endm
.macro CV d,a,b
v_cvt_pk_bf16_f32 v\d, v\a, v\b
.endm
.macro EX d,a
v_exp_f32 v\d, v\a
.endm
v_mov_b32 v1, %0
s_mov_b32 s17, %2
s_load_dwordx2 s[8:9], %1, 0x0
v_lshrrev_b32 v198, 6, v1
s_nop 0
v_readfirstlane_b32 s16, v198
s_and_b32 s18, s17, 7
s_lshr_b32 s19, s17, 3
v_and_b32 v198, 63, v1
v_lshlrev_b32 v182, 4, v198
v_add_u32 v183, 16384, v182
s_lshl_b32 s44, s16, 12
s_add_u32 s45, s44, 16384
v_add_u32 v184, s44, v182
v_mov_b32 v199, 0x3f803f80
v_accvgpr_write_b32 a200, v199
v_mov_b32 v199, 0x3f803f80
v_accvgpr_write_b32 a201, v199
v_mov_b32 v199, 0x3f803f80
v_accvgpr_write_b32 a202, v199
v_mov_b32 v199, 0x3f803f80
v_accvgpr_write_b32 a203, v199
v_mov_b32 v207, 0
v_mov_b32 v208, 1
s_lshl_b32 s62, s17, 8
s_add_u32 s62, s62, 125829312
s_mov_b32 s61, s62
s_waitcnt lgkmcnt(0)
s_mov_b32 s12, s8
s_mov_b32 s13, s9
s_mov_b32 s14, 130023424
s_mov_b32 s15, 0x20000
s_add_u32 s24, s8, 16777216
s_addc_u32 s25, s9, 0
s_mov_b32 s26, 16777216
s_mov_b32 s27, 0x20000
s_add_u32 s28, s8, 33554432
s_addc_u32 s29, s9, 0
s_mov_b32 s30, 16777216
s_mov_b32 s31, 0x20000
s_cmp_ge_u32 s19, 24
s_cbranch_scc1 Lp2_entry
s_cmp_ge_u32 s19, 24
s_cbranch_scc1 Lat_done
s_mul_i32 s20, s19, 36
s_getpc_b64 s[64:65]
s_add_u32 s64, s64, s20
s_addc_u32 s65, s65, 0
s_add_u32 s64, s64, 20
s_addc_u32 s65, s65, 0
s_setpc_b64 s[64:65]
s_mov_b32 s34, 0x80278031
s_mov_b32 s35, 0
s_nop 0
s_mov_b32 s36, 0
s_nop 0
s_mov_b32 s37, 0
s_nop 0
s_branch Lat_start
s_mov_b32 s34, 0x802f8031
s_mov_b32 s35, 0
s_nop 0
s_mov_b32 s36, 0
s_nop 0
s_mov_b32 s37, 0
s_nop 0
s_branch Lat_start
s_mov_b32 s34, 0x8017b10f
s_mov_b32 s35, 0x801fb10f
s_mov_b32 s36, 0x80000004
s_mov_b32 s37, 0x80080004
s_branch Lat_start
s_mov_b32 s34, 0x80270031
s_mov_b32 s35, 0
s_nop 0
s_mov_b32 s36, 0
s_nop 0
s_mov_b32 s37, 0
s_nop 0
s_branch Lat_start
s_mov_b32 s34, 0x802f0031
s_mov_b32 s35, 0
s_nop 0
s_mov_b32 s36, 0
s_nop 0
s_mov_b32 s37, 0
s_nop 0
s_branch Lat_start
s_mov_b32 s34, 0x8017310b
s_mov_b32 s35, 0x801f310b
s_mov_b32 s36, 0x80008008
s_mov_b32 s37, 0x80088008
s_branch Lat_start
s_mov_b32 s34, 0x80268031
s_mov_b32 s35, 0
s_nop 0
s_mov_b32 s36, 0
s_nop 0
s_mov_b32 s37, 0
s_nop 0
s_branch Lat_start
s_mov_b32 s34, 0x802e8031
s_mov_b32 s35, 0
s_nop 0
s_mov_b32 s36, 0
s_nop 0
s_mov_b32 s37, 0
s_nop 0
s_branch Lat_start
s_mov_b32 s34, 0x8016b107
s_mov_b32 s35, 0x801eb107
s_mov_b32 s36, 0x8001000c
s_mov_b32 s37, 0x8009000c
s_branch Lat_start
s_mov_b32 s34, 0x80260031
s_mov_b32 s35, 0
s_nop 0
s_mov_b32 s36, 0
s_nop 0
s_mov_b32 s37, 0
s_nop 0
s_branch Lat_start
s_mov_b32 s34, 0x802e0031
s_mov_b32 s35, 0
s_nop 0
s_mov_b32 s36, 0
s_nop 0
s_mov_b32 s37, 0
s_nop 0
s_branch Lat_start
s_mov_b32 s34, 0x80163103
s_mov_b32 s35, 0x801e3103
s_mov_b32 s36, 0x80018010
s_mov_b32 s37, 0x80098010
s_branch Lat_start
s_mov_b32 s34, 0x80058030
s_mov_b32 s35, 0
s_nop 0
s_mov_b32 s36, 0
s_nop 0
s_mov_b32 s37, 0
s_nop 0
s_branch Lat_start
s_mov_b32 s34, 0x800d8030
s_mov_b32 s35, 0
s_nop 0
s_mov_b32 s36, 0
s_nop 0
s_mov_b32 s37, 0
s_nop 0
s_branch Lat_start
s_mov_b32 s34, 0x80020014
s_mov_b32 s35, 0x800a0014
s_mov_b32 s36, 0
s_nop 0
s_mov_b32 s37, 0
s_nop 0
s_branch Lat_start
s_mov_b32 s34, 0x80128003
s_mov_b32 s35, 0x8005002c
s_mov_b32 s36, 0
s_nop 0
s_mov_b32 s37, 0
s_nop 0
s_branch Lat_start
s_mov_b32 s34, 0x801a8003
s_mov_b32 s35, 0x800d002c
s_mov_b32 s36, 0
s_nop 0
s_mov_b32 s37, 0
s_nop 0
s_branch Lat_start
s_mov_b32 s34, 0x80228315
s_mov_b32 s35, 0x802a8315
s_mov_b32 s36, 0
s_nop 0
s_mov_b32 s37, 0
s_nop 0
s_branch Lat_start
s_mov_b32 s34, 0x80130007
s_mov_b32 s35, 0x80048028
s_mov_b32 s36, 0
s_nop 0
s_mov_b32 s37, 0
s_nop 0
s_branch Lat_start
s_mov_b32 s34, 0x801b0007
s_mov_b32 s35, 0x800c8028
s_mov_b32 s36, 0
s_nop 0
s_mov_b32 s37, 0
s_nop 0
s_branch Lat_start
s_mov_b32 s34, 0x80230715
s_mov_b32 s35, 0x802b0715
s_mov_b32 s36, 0
s_nop 0
s_mov_b32 s37, 0
s_nop 0
s_branch Lat_start
s_mov_b32 s34, 0x8013800b
s_mov_b32 s35, 0x80040024
s_mov_b32 s36, 0
s_nop 0
s_mov_b32 s37, 0
s_nop 0
s_branch Lat_start
s_mov_b32 s34, 0x801b800b
s_mov_b32 s35, 0x800c0024
s_mov_b32 s36, 0
s_nop 0
s_mov_b32 s37, 0
s_nop 0
s_branch Lat_start
s_mov_b32 s34, 0x80238b15
s_mov_b32 s35, 0x802b8b15
s_mov_b32 s36, 0
s_nop 0
s_mov_b32 s37, 0
s_nop 0
s_branch Lat_start
Lat_start:
s_bfe_u32 s21, s34, 65555
s_lshl_b32 s22, s18, 1
s_add_u32 s21, s21, s22
s_lshl_b32 s41, s21, 20
s_bfe_u32 s21, s34, 458760
s_lshl_b32 s21, s21, 14
s_add_u32 s41, s41, s21
s_mov_b32 m0, s44
s_nop 0
buffer_load_dwordx4 v184, s[24:27], s41 offen lds
buffer_load_dwordx4 v184, s[24:27], s41 offen offset:1024 lds
buffer_load_dwordx4 v184, s[24:27], s41 offen offset:2048 lds
buffer_load_dwordx4 v184, s[24:27], s41 offen offset:3072 lds
s_bfe_u32 s21, s34, 65555
s_lshl_b32 s22, s18, 1
s_add_u32 s21, s21, s22
s_lshl_b32 s60, s21, 20
s_bfe_u32 s21, s34, 262159
s_lshl_b32 s21, s21, 16
s_add_u32 s60, s60, s21
s_lshl_b32 s21, s16, 14
s_add_u32 s60, s60, s21
s_cmp_eq_u32 s34, 0
s_cselect_b32 s60, 0x7ff00000, s60
buffer_load_dwordx4 a[136:139], v182, s[12:15], s60 offen
buffer_load_dwordx4 a[140:143], v182, s[12:15], s60 offen offset:1024
buffer_load_dwordx4 a[144:147], v182, s[12:15], s60 offen offset:2048
buffer_load_dwordx4 a[148:151], v182, s[12:15], s60 offen offset:3072
s_add_u32 s60, s60, 4096
buffer_load_dwordx4 a[152:155], v182, s[12:15], s60 offen
buffer_load_dwordx4 a[156:159], v182, s[12:15], s60 offen offset:1024
buffer_load_dwordx4 a[160:163], v182, s[12:15], s60 offen offset:2048
buffer_load_dwordx4 a[164:167], v182, s[12:15], s60 offen offset:3072
s_add_u32 s60, s60, 4096
buffer_load_dwordx4 a[168:171], v182, s[12:15], s60 offen
buffer_load_dwordx4 a[172:175], v182, s[12:15], s60 offen offset:1024
buffer_load_dwordx4 a[176:179], v182, s[12:15], s60 offen offset:2048
buffer_load_dwordx4 a[180:183], v182, s[12:15], s60 offen offset:3072
s_add_u32 s60, s60, 4096
buffer_load_dwordx4 a[184:187], v182, s[12:15], s60 offen
buffer_load_dwordx4 a[188:191], v182, s[12:15], s60 offen offset:1024
buffer_load_dwordx4 a[192:195], v182, s[12:15], s60 offen offset:2048
buffer_load_dwordx4 a[196:199], v182, s[12:15], s60 offen offset:3072
v_lshrrev_b32 v198, 4, v182
v_mov_b32 v206, 0x7fff0000
s_mov_b32 s54, 0
s_and_b32 s20, s34, 0x7f
s_bfe_u32 s21, s34, 65555
s_lshl_b32 s22, s18, 1
s_add_u32 s21, s21, s22
s_lshl_b32 s21, s21, 20
s_bfe_u32 s22, s34, 458760
s_lshl_b32 s22, s22, 14
s_add_u32 s21, s21, s22
v_subrev_u32 v199, s54, v198
v_cmp_gt_u32 vcc, s20, v199
v_lshl_add_u32 v200, v199, 14, s21
s_nop 0
v_cndmask_b32 v206, v206, v200, vcc
s_add_u32 s54, s54, s20
s_and_b32 s20, s35, 0x7f
s_bfe_u32 s21, s35, 65555
s_lshl_b32 s22, s18, 1
s_add_u32 s21, s21, s22
s_lshl_b32 s21, s21, 20
s_bfe_u32 s22, s35, 458760
s_lshl_b32 s22, s22, 14
s_add_u32 s21, s21, s22
v_subrev_u32 v199, s54, v198
v_cmp_gt_u32 vcc, s20, v199
v_lshl_add_u32 v200, v199, 14, s21
s_nop 0
v_cndmask_b32 v206, v206, v200, vcc
s_add_u32 s54, s54, s20
s_and_b32 s20, s36, 0x7f
s_bfe_u32 s21, s36, 65555
s_lshl_b32 s22, s18, 1
s_add_u32 s21, s21, s22
s_lshl_b32 s21, s21, 20
s_bfe_u32 s22, s36, 458760
s_lshl_b32 s22, s22, 14
s_add_u32 s21, s21, s22
v_subrev_u32 v199, s54, v198
v_cmp_gt_u32 vcc, s20, v199
v_lshl_add_u32 v200, v199, 14, s21
s_nop 0
v_cndmask_b32 v206, v206, v200, vcc
s_add_u32 s54, s54, s20
s_and_b32 s20, s37, 0x7f
s_bfe_u32 s21, s37, 65555
s_lshl_b32 s22, s18, 1
s_add_u32 s21, s21, s22
s_lshl_b32 s21, s21, 20
s_bfe_u32 s22, s37, 458760
s_lshl_b32 s22, s22, 14
s_add_u32 s21, s21, s22
v_subrev_u32 v199, s54, v198
v_cmp_gt_u32 vcc, s20, v199
v_lshl_add_u32 v200, v199, 14, s21
s_nop 0
v_cndmask_b32 v206, v206, v200, vcc
s_add_u32 s54, s54, s20
s_and_b32 s54, s34, 0x7f
s_bfe_u32 s55, s34, 458760
s_bfe_u32 s56, s34, 262159
s_bfe_u32 s57, s34, 65555
s_bfe_u32 s58, s34, 131092
s_lshl_b32 s20, s18, 1
s_add_u32 s57, s57, s20
s_lshl_b32 s59, s57, 4
s_add_u32 s59, s59, s56
s_lshl_b32 s20, s56, 2
s_sub_i32 s20, s20, s55
s_sub_i32 s20, s20, 2
s_max_i32 s20, s20, 0
s_sub_u32 s21, s54, 3
s_min_u32 s52, s20, s21
s_sub_u32 s53, s21, s52
s_lshl_b32 s20, s56, 7
s_lshl_b32 s21, s16, 5
s_add_u32 s20, s20, s21
v_lshrrev_b32 v198, 4, v182
v_and_b32 v199, 15, v198
v_lshrrev_b32 v198, 4, v198
v_lshlrev_b32 v198, 2, v198
v_add_u32 v199, s20, v199
v_sub_u32 v199, v199, v198
v_cvt_f32_i32 v199, v199
v_add_f32 v191, 0.5, v199
v_add_f32 v192, 0x41840000, v199
v_readlane_b32 s41, v206, 1
s_add_u32 m0, s44, 32768
s_nop 3
buffer_load_dwordx4 v184, s[24:27], s41 offen lds
buffer_load_dwordx4 v184, s[24:27], s41 offen offset:1024 lds
buffer_load_dwordx4 v184, s[24:27], s41 offen offset:2048 lds
buffer_load_dwordx4 v184, s[24:27], s41 offen offset:3072 lds
s_mov_b32 s38, 32768
s_mov_b32 s40, 98304
s_mov_b32 s46, 6
s_mov_b32 s51, s55
v_mov_b32 v180, v182
s_waitcnt vmcnt(20)
s_barrier
Rv 4,180,0
Rv 36,180,8192
Rv 8,180,1024
Rv 40,180,9216
Rv 12,180,2048
Rv 44,180,10240
Rv 16,180,3072
Rv 48,180,11264
Rv 20,180,4096
Rv 52,180,12288
Rv 24,180,5120
Rv 56,180,13312
Rv 28,180,6144
Rv 60,180,14336
Rv 32,180,7168
Rv 64,180,15360
s_waitcnt vmcnt(4) lgkmcnt(0)
MZvva 132,4,136
v_readlane_b32 s42, v206, 0
s_add_u32 m0, s45, 0
s_nop 3
MZvva 136,36,136
buffer_load_dwordx4 v184, s[28:31], s42 offen lds
MAvva 132,8,140
MAvva 136,40,140
buffer_load_dwordx4 v184, s[28:31], s42 offen offset:1024 lds
MAvva 132,12,144
buffer_load_dwordx4 v184, s[28:31], s42 offen offset:2048 lds
MAvva 136,44,144
MAvva 132,16,148
buffer_load_dwordx4 v184, s[28:31], s42 offen offset:3072 lds
MAvva 136,48,148
MAvva 132,20,152
v_readlane_b32 s41, v206, 2
s_add_u32 m0, s44, 65536
s_nop 3
MAvva 136,52,152
buffer_load_dwordx4 v184, s[24:27], s41 offen lds
MAvva 132,24,156
MAvva 136,56,156
buffer_load_dwordx4 v184, s[24:27], s41 offen offset:1024 lds
MAvva 132,28,160
buffer_load_dwordx4 v184, s[24:27], s41 offen offset:2048 lds
MAvva 136,60,160
MAvva 132,32,164
buffer_load_dwordx4 v184, s[24:27], s41 offen offset:3072 lds
MAvva 136,64,164
MZvva 140,4,168
v_readlane_b32 s41, v206, 3
s_add_u32 m0, s44, 98304
s_nop 3
MZvva 144,36,168
buffer_load_dwordx4 v184, s[24:27], s41 offen lds
MAvva 140,8,172
MAvva 144,40,172
buffer_load_dwordx4 v184, s[24:27], s41 offen offset:1024 lds
MAvva 140,12,176
buffer_load_dwordx4 v184, s[24:27], s41 offen offset:2048 lds
MAvva 144,44,176
MAvva 140,16,180
buffer_load_dwordx4 v184, s[24:27], s41 offen offset:3072 lds
MAvva 144,48,180
MAvva 140,20,184
v_readlane_b32 s42, v206, 1
s_add_u32 m0, s45, 32768
s_nop 3
MAvva 144,52,184
buffer_load_dwordx4 v184, s[28:31], s42 offen lds
MAvva 140,24,188
MAvva 144,56,188
buffer_load_dwordx4 v184, s[28:31], s42 offen offset:1024 lds
MAvva 140,28,192
buffer_load_dwordx4 v184, s[28:31], s42 offen offset:2048 lds
MAvva 144,60,192
MAvva 140,32,196
buffer_load_dwordx4 v184, s[28:31], s42 offen offset:3072 lds
MAvva 144,64,196
s_waitcnt vmcnt(12)
s_barrier
v_add_u32 v180, 32768, v182
v_mov_b32 v181, v183
Rv 4,180,0
Rv 36,180,8192
Rv 8,180,1024
Rv 40,180,9216
Rv 12,180,2048
Rv 44,180,10240
Rv 16,180,3072
Rv 48,180,11264
Rv 20,180,4096
Rv 52,180,12288
Rv 24,180,5120
Rv 56,180,13312
Rv 28,180,6144
Rv 60,180,14336
Rv 32,180,7168
Rv 64,180,15360
s_bfe_u32 s21, s35, 65555
s_lshl_b32 s22, s18, 1
s_add_u32 s21, s21, s22
s_lshl_b32 s60, s21, 20
s_bfe_u32 s21, s35, 262159
s_lshl_b32 s21, s21, 16
s_add_u32 s60, s60, s21
s_lshl_b32 s21, s16, 14
s_add_u32 s60, s60, s21
s_cmp_eq_u32 s35, 0
s_cselect_b32 s60, 0x7ff00000, s60
buffer_load_dwordx4 a[204:207], v182, s[12:15], s60 offen
buffer_load_dwordx4 a[208:211], v182, s[12:15], s60 offen offset:1024
buffer_load_dwordx4 a[212:215], v182, s[12:15], s60 offen offset:2048
buffer_load_dwordx4 a[216:219], v182, s[12:15], s60 offen offset:3072
s_add_u32 s60, s60, 4096
buffer_load_dwordx4 a[220:223], v182, s[12:15], s60 offen
buffer_load_dwordx4 a[224:227], v182, s[12:15], s60 offen offset:1024
buffer_load_dwordx4 a[228:231], v182, s[12:15], s60 offen offset:2048
buffer_load_dwordx4 a[232:235], v182, s[12:15], s60 offen offset:3072
s_add_u32 s60, s60, 4096
s_add_u32 s22, s51, 0
s_lshl_b32 s22, s22, 5
v_cvt_f32_u32 v194, s22
v_sub_f32 v193, v191, v194
v_sub_f32 v194, v192, v194
v_mul_f32 v193, 0x6c800000, v193
v_mul_f32 v194, 0x6c800000, v194
v_min_f32 v132, v132, v193
v_add_f32 v199, 0xec800000, v193
v_min_f32 v133, v133, v199
v_add_f32 v198, 0xed000000, v193
v_min_f32 v134, v134, v198
v_add_f32 v199, 0xed400000, v193
v_min_f32 v135, v135, v199
v_add_f32 v198, 0xee800000, v193
v_min_f32 v136, v136, v198
v_add_f32 v199, 0xee880000, v193
v_min_f32 v137, v137, v199
v_add_f32 v198, 0xee900000, v193
v_min_f32 v138, v138, v198
v_add_f32 v199, 0xee980000, v193
v_min_f32 v139, v139, v199
v_max3_f32 v204, v132, v133, v134
v_max3_f32 v204, v204, v135, v136
v_max3_f32 v204, v204, v137, v138
v_max_f32 v204, v204, v139
v_mov_b32 v202, v204
v_mov_b32 v203, v204
s_nop 1
v_permlane16_swap_b32 v202, v203
s_nop 1
v_max_f32 v204, v202, v203
v_mov_b32 v202, v204
v_mov_b32 v203, v204
s_nop 1
v_permlane32_swap_b32 v202, v203
s_nop 1
v_max_f32 v204, v202, v203
v_max_f32 v204, 0xc9742400, v204
v_add_f32 v185, 0x41c00000, v204
v_sub_f32 v148, 0, v185
v_sub_f32 v149, 0, v185
v_sub_f32 v150, 0, v185
v_sub_f32 v151, 0, v185
v_sub_f32 v132, v132, v185
v_sub_f32 v133, v133, v185
v_sub_f32 v134, v134, v185
v_sub_f32 v135, v135, v185
v_sub_f32 v136, v136, v185
v_sub_f32 v137, v137, v185
v_sub_f32 v138, v138, v185
v_sub_f32 v139, v139, v185
EX 164,132
EX 165,133
EX 166,134
EX 167,135
EX 168,136
EX 169,137
EX 170,138
EX 171,139
s_nop 0
CV 156,164,165
CV 157,166,167
CV 158,168,169
CV 159,170,171
v_min_f32 v140, v140, v194
v_add_f32 v199, 0xec800000, v194
v_min_f32 v141, v141, v199
v_add_f32 v198, 0xed000000, v194
v_min_f32 v142, v142, v198
v_add_f32 v199, 0xed400000, v194
v_min_f32 v143, v143, v199
v_add_f32 v198, 0xee800000, v194
v_min_f32 v144, v144, v198
v_add_f32 v199, 0xee880000, v194
v_min_f32 v145, v145, v199
v_add_f32 v198, 0xee900000, v194
v_min_f32 v146, v146, v198
v_add_f32 v199, 0xee980000, v194
v_min_f32 v147, v147, v199
v_max3_f32 v204, v140, v141, v142
v_max3_f32 v204, v204, v143, v144
v_max3_f32 v204, v204, v145, v146
v_max_f32 v204, v204, v147
v_mov_b32 v202, v204
v_mov_b32 v203, v204
s_nop 1
v_permlane16_swap_b32 v202, v203
s_nop 1
v_max_f32 v204, v202, v203
v_mov_b32 v202, v204
v_mov_b32 v203, v204
s_nop 1
v_permlane32_swap_b32 v202, v203
s_nop 1
v_max_f32 v204, v202, v203
v_max_f32 v204, 0xc9742400, v204
v_add_f32 v186, 0x41c00000, v204
v_sub_f32 v152, 0, v186
v_sub_f32 v153, 0, v186
v_sub_f32 v154, 0, v186
v_sub_f32 v155, 0, v186
v_sub_f32 v140, v140, v186
v_sub_f32 v141, v141, v186
v_sub_f32 v142, v142, v186
v_sub_f32 v143, v143, v186
v_sub_f32 v144, v144, v186
v_sub_f32 v145, v145, v186
v_sub_f32 v146, v146, v186
v_sub_f32 v147, v147, v186
EX 172,140
EX 173,141
EX 174,142
EX 175,143
EX 176,144
EX 177,145
EX 178,146
EX 179,147
s_nop 0
CV 160,172,173
CV 161,174,175
CV 162,176,177
CV 163,178,179
s_waitcnt lgkmcnt(0)
v_mfma_f32_16x16x32_bf16 v[132:135], v[4:7], a[136:139], v[148:151]
v_readlane_b32 s41, v206, 4
s_add_u32 m0, s44, 131072
s_nop 3
v_mfma_f32_16x16x32_bf16 v[136:139], v[36:39], a[136:139], v[148:151]
buffer_load_dwordx4 v184, s[24:27], s41 offen lds
MAvva 132,8,140
MAvva 136,40,140
buffer_load_dwordx4 v184, s[24:27], s41 offen offset:1024 lds
MAvva 132,12,144
buffer_load_dwordx4 v184, s[24:27], s41 offen offset:2048 lds
MAvva 136,44,144
MAvva 132,16,148
buffer_load_dwordx4 v184, s[24:27], s41 offen offset:3072 lds
MAvva 136,48,148
v_readlane_b32 s42, v206, 2
s_add_u32 m0, s45, 65536
s_nop 3
MAvva 132,20,152
MAvva 136,52,152
buffer_load_dwordx4 v184, s[28:31], s42 offen lds
MAvva 132,24,156
buffer_load_dwordx4 v184, s[28:31], s42 offen offset:1024 lds
MAvva 136,56,156
MAvva 132,28,160
buffer_load_dwordx4 v184, s[28:31], s42 offen offset:2048 lds
MAvva 136,60,160
buffer_load_dwordx4 v184, s[28:31], s42 offen offset:3072 lds
MAvva 132,32,164
MAvva 136,64,164
v_readlane_b32 s41, v206, 5
s_add_u32 m0, s44, 0
s_nop 3
v_mfma_f32_16x16x32_bf16 v[140:143], v[4:7], a[168:171], v[152:155]
buffer_load_dwordx4 v184, s[24:27], s41 offen lds
v_mfma_f32_16x16x32_bf16 v[144:147], v[36:39], a[168:171], v[152:155]
MAvva 140,8,172
buffer_load_dwordx4 v184, s[24:27], s41 offen offset:1024 lds
MAvva 144,40,172
buffer_load_dwordx4 v184, s[24:27], s41 offen offset:2048 lds
MAvva 140,12,176
MAvva 144,44,176
buffer_load_dwordx4 v184, s[24:27], s41 offen offset:3072 lds
MAvva 140,16,180
v_readlane_b32 s42, v206, 3
s_add_u32 m0, s45, 98304
s_nop 3
MAvva 144,48,180
MAvva 140,20,184
buffer_load_dwordx4 v184, s[28:31], s42 offen lds
MAvva 144,52,184
buffer_load_dwordx4 v184, s[28:31], s42 offen offset:1024 lds
MAvva 140,24,188
MAvva 144,56,188
buffer_load_dwordx4 v184, s[28:31], s42 offen offset:2048 lds
MAvva 140,28,192
buffer_load_dwordx4 v184, s[28:31], s42 offen offset:3072 lds
MAvva 144,60,192
MAvva 140,32,196
v_readlane_b32 s43, v206, 4
MAvva 144,64,196
Rv 68,181,0
Rv 72,181,1024
Rv 76,181,2048
Rv 80,181,3072
Rv 84,181,4096
Rv 88,181,5120
Rv 92,181,6144
Rv 96,181,7168
Rv 100,181,8192
Rv 104,181,9216
Rv 108,181,10240
Rv 112,181,11264
Rv 116,181,12288
Rv 120,181,13312
Rv 124,181,14336
Rv 128,181,15360
Lat_segloop:
s_waitcnt vmcnt(16)
s_barrier
s_waitcnt lgkmcnt(12)
MZavv 0,68,156
s_mov_b32 s39, s38
s_add_u32 s22, s51, 1
s_lshl_b32 s22, s22, 5
MZavv 8,72,156
s_add_u32 s20, s38, 32768
s_cmp_eq_u32 s20, 163840
s_cselect_b32 s38, 0, s20
v_add_u32 v180, s38, v182
MZavv 16,76,156
Rv 4,180,0
v_cvt_f32_u32 v194, s22
s_mov_b32 s42, s43
MZavv 24,80,156
Rv 36,180,8192
v_sub_f32 v193, v191, v194
s_waitcnt lgkmcnt(10)
MZavv 32,84,156
v_sub_f32 v194, v192, v194
v_mul_f32 v193, 0x6c800000, v193
s_mov_b32 s43, s41
MZavv 40,88,156
v_readlane_b32 s41, v206, s46
v_mul_f32 v194, 0x6c800000, v194
s_add_u32 s46, s46, 1
MZavv 48,92,156
Rv 8,180,1024
v_min_f32 v132, v132, v193
MZavv 56,96,156
Rv 40,180,9216
v_add_u32 v181, s39, v183
s_waitcnt lgkmcnt(8)
MZavv 64,100,156
buffer_load_dwordx4 v[212:215], v182, s[12:15], s60 offen
MZavv 72,104,156
Rv 12,180,2048
v_min_f32 v140, v140, v194
MZavv 80,108,156
buffer_load_dwordx4 v[216:219], v182, s[12:15], s60 offen offset:1024
MZavv 88,112,156
Rv 44,180,10240
s_waitcnt lgkmcnt(6)
MZavv 96,116,156
buffer_load_dwordx4 v[220:223], v182, s[12:15], s60 offen offset:2048
MZavv 104,120,156
Rv 16,180,3072
MZavv 112,124,156
buffer_load_dwordx4 v[224:227], v182, s[12:15], s60 offen offset:3072
s_add_u32 s60, s60, 4096
MZavv 120,128,156
Rv 48,180,11264
MZaav 128,200,156
buffer_load_dwordx4 v[228:231], v182, s[12:15], s60 offen
MZavv 4,68,160
buffer_load_dwordx4 v[232:235], v182, s[12:15], s60 offen offset:1024
MZavv 12,72,160
buffer_load_dwordx4 v[236:239], v182, s[12:15], s60 offen offset:2048
MZavv 20,76,160
buffer_load_dwordx4 v[240:243], v182, s[12:15], s60 offen offset:3072
s_add_u32 m0, s39, s44
MZavv 28,80,160
Rv 20,180,4096
MZavv 36,84,160
v_add_f32 v199, 0xec800000, v193
v_min_f32 v133, v133, v199
v_add_f32 v198, 0xed000000, v193
v_min_f32 v134, v134, v198
v_add_f32 v199, 0xed400000, v193
v_min_f32 v135, v135, v199
v_add_f32 v198, 0xee800000, v193
v_min_f32 v136, v136, v198
v_add_f32 v199, 0xee880000, v193
v_min_f32 v137, v137, v199
v_add_f32 v198, 0xee900000, v193
v_min_f32 v138, v138, v198
v_add_f32 v199, 0xee980000, v193
v_min_f32 v139, v139, v199
MZavv 44,88,160
EX 164,132
EX 165,133
EX 166,134
EX 167,135
EX 168,136
EX 169,137
EX 170,138
EX 171,139
Rv 52,180,12288
MZavv 52,92,160
Rv 24,180,5120
CV 156,164,165
MZavv 60,96,160
Rv 56,180,13312
CV 157,166,167
MZavv 68,100,160
Rv 28,180,6144
CV 158,168,169
MZavv 76,104,160
s_add_u32 s20, s40, 32768
s_cmp_eq_u32 s20, 163840
s_cselect_b32 s40, 0, s20
MZavv 84,108,160
Rv 60,180,14336
CV 159,170,171
MZavv 92,112,160
v_or3_b32 v195, v156, v157, v158
v_or_b32 v195, v195, v159
MZavv 100,116,160
Rv 32,180,7168
v_and_b32 v195, 0x40004000, v195
MZavv 108,120,160
v_cmp_ne_u32 vcc, 0, v195
MZavv 116,124,160
s_cbranch_vccnz Lat_stub1
Lat_ret1:
Rv 64,180,15360
MZavv 124,128,160
buffer_load_dwordx4 v184, s[24:27], s41 offen lds
MZaav 132,200,160
v_add_f32 v201, 0xec800000, v194
v_min_f32 v141, v141, v201
s_waitcnt lgkmcnt(12)
v_mfma_f32_16x16x32_bf16 v[132:135], v[4:7], a[136:139], v[148:151]
v_add_f32 v200, 0xed000000, v194
v_min_f32 v142, v142, v200
v_mfma_f32_16x16x32_bf16 v[136:139], v[36:39], a[136:139], v[148:151]
v_add_f32 v201, 0xed400000, v194
v_min_f32 v143, v143, v201
MAvva 132,8,140
v_add_f32 v200, 0xee800000, v194
v_min_f32 v144, v144, v200
MAvva 136,40,140
v_add_f32 v201, 0xee880000, v194
v_min_f32 v145, v145, v201
s_waitcnt lgkmcnt(8)
MAvva 132,12,144
v_add_f32 v200, 0xee900000, v194
v_min_f32 v146, v146, v200
MAvva 136,44,144
v_add_f32 v201, 0xee980000, v194
v_min_f32 v147, v147, v201
MAvva 132,16,148
buffer_load_dwordx4 v184, s[24:27], s41 offen offset:1024 lds
MAvva 136,48,148
EX 172,140
s_waitcnt lgkmcnt(4)
MAvva 132,20,152
EX 173,141
MAvva 136,52,152
EX 174,142
EX 175,143
EX 176,144
EX 177,145
EX 178,146
EX 179,147
buffer_load_dwordx4 v184, s[24:27], s41 offen offset:2048 lds
MAvva 132,24,156
buffer_load_dwordx4 v184, s[24:27], s41 offen offset:3072 lds
MAvva 136,56,156
CV 160,172,173
CV 161,174,175
s_waitcnt lgkmcnt(0)
MAvva 132,28,160
CV 162,176,177
CV 163,178,179
s_add_u32 m0, s40, s45
MAvva 136,60,160
v_or3_b32 v196, v160, v161, v162
v_or_b32 v196, v196, v163
MAvva 132,32,164
v_and_b32 v196, 0x40004000, v196
v_cmp_ne_u32 vcc, 0, v196
MAvva 136,64,164
s_cbranch_vccnz Lat_stub2
Lat_ret2:
buffer_load_dwordx4 v184, s[28:31], s42 offen lds
v_mfma_f32_16x16x32_bf16 v[140:143], v[4:7], a[168:171], v[152:155]
buffer_load_dwordx4 v184, s[28:31], s42 offen offset:1024 lds
v_mfma_f32_16x16x32_bf16 v[144:147], v[36:39], a[168:171], v[152:155]
Rv 68,181,0
MAvva 140,8,172
Rv 72,181,1024
MAvva 144,40,172
buffer_load_dwordx4 v184, s[28:31], s42 offen offset:2048 lds
MAvva 140,12,176
Rv 76,181,2048
MAvva 144,44,176
Rv 80,181,3072
MAvva 140,16,180
Rv 84,181,4096
MAvva 144,48,180
Rv 88,181,5120
MAvva 140,20,184
Rv 92,181,6144
MAvva 144,52,184
Rv 96,181,7168
MAvva 140,24,188
buffer_load_dwordx4 v184, s[28:31], s42 offen offset:3072 lds
MAvva 144,56,188
Rv 100,181,8192
MAvva 140,28,192
Rv 104,181,9216
MAvva 144,60,192
Rv 108,181,10240
MAvva 140,32,196
Rv 112,181,11264
s_add_u32 s51, s51, 1
MAvva 144,64,196
s_waitcnt vmcnt(16)
s_mov_b64 exec, 1
buffer_atomic_add v208, v207, s[12:15], s61 offen
s_mov_b64 exec, -1
s_mov_b32 s61, s62
Rv 116,181,12288
Rv 120,181,13312
Rv 124,181,14336
Rv 128,181,15360
s_cmp_eq_u32 s52, 0
s_cbranch_scc1 Lat_uend
s_and_b32 s20, s52, 3
s_add_u32 s52, s52, 3
s_lshr_b32 s52, s52, 2
s_cmp_eq_u32 s20, 1
s_cbranch_scc1 Lat_u3
s_cmp_eq_u32 s20, 2
s_cbranch_scc1 Lat_u2
s_cmp_eq_u32 s20, 3
s_cbranch_scc1 Lat_u1
Lat_u0:
s_waitcnt vmcnt(16)
s_barrier
s_waitcnt lgkmcnt(12)
MAavv 0,68,156
s_mov_b32 s39, s38
MAavv 8,72,156
s_add_u32 s20, s38, 32768
s_cmp_eq_u32 s20, 163840
s_cselect_b32 s38, 0, s20
v_add_u32 v180, s38, v182
MAavv 16,76,156
Rv 4,180,0
s_mov_b32 s42, s43
s_mov_b32 s43, s41
MAavv 24,80,156
v_readlane_b32 s41, v206, s46
Rv 36,180,8192
s_waitcnt lgkmcnt(10)
MAavv 32,84,156
s_add_u32 s46, s46, 1
EX 164,132
MAavv 40,88,156
Rv 8,180,1024
v_add_u32 v181, s39, v183
MAavv 48,92,156
s_add_u32 m0, s39, s44
EX 165,133
MAavv 56,96,156
Rv 40,180,9216
s_waitcnt lgkmcnt(8)
MAavv 64,100,156
EX 166,134
MAavv 72,104,156
Rv 12,180,2048
MAavv 80,108,156
EX 167,135
MAavv 88,112,156
Rv 44,180,10240
s_waitcnt lgkmcnt(6)
MAavv 96,116,156
EX 168,136
MAavv 104,120,156
Rv 16,180,3072
MAavv 112,124,156
EX 169,137
MAavv 120,128,156
Rv 48,180,11264
MAaav 128,200,156
EX 170,138
MAavv 4,68,160
Rv 20,180,4096
CV 156,164,165
MAavv 12,72,160
EX 171,139
MAavv 20,76,160
Rv 52,180,12288
CV 157,166,167
MAavv 28,80,160
s_add_u32 s20, s40, 32768
s_cmp_eq_u32 s20, 163840
s_cselect_b32 s40, 0, s20
MAavv 36,84,160
Rv 24,180,5120
CV 158,168,169
MAavv 44,88,160
CV 159,170,171
v_or3_b32 v195, v156, v157, v158
MAavv 52,92,160
Rv 56,180,13312
v_or_b32 v195, v195, v159
MAavv 60,96,160
v_and_b32 v195, 0x40004000, v195
v_cmp_ne_u32 vcc, 0, v195
MAavv 68,100,160
Rv 28,180,6144
s_cbranch_vccnz Lat_stub3
Lat_ret3:
MAavv 76,104,160
buffer_load_dwordx4 v184, s[24:27], s41 offen lds
MAavv 84,108,160
Rv 60,180,14336
MAavv 92,112,160
buffer_load_dwordx4 v184, s[24:27], s41 offen offset:1024 lds
MAavv 100,116,160
Rv 32,180,7168
MAavv 108,120,160
buffer_load_dwordx4 v184, s[24:27], s41 offen offset:2048 lds
MAavv 116,124,160
Rv 64,180,15360
MAavv 124,128,160
buffer_load_dwordx4 v184, s[24:27], s41 offen offset:3072 lds
MAaav 132,200,160
EX 172,140
s_waitcnt lgkmcnt(12)
v_mfma_f32_16x16x32_bf16 v[132:135], v[4:7], a[136:139], v[148:151]
EX 173,141
v_mfma_f32_16x16x32_bf16 v[136:139], v[36:39], a[136:139], v[148:151]
EX 174,142
MAvva 132,8,140
s_add_u32 m0, s40, s45
EX 175,143
MAvva 136,40,140
EX 176,144
s_waitcnt lgkmcnt(8)
MAvva 132,12,144
EX 177,145
MAvva 136,44,144
EX 178,146
MAvva 132,16,148
EX 179,147
MAvva 136,48,148
CV 160,172,173
CV 161,174,175
s_waitcnt lgkmcnt(4)
MAvva 132,20,152
CV 162,176,177
CV 163,178,179
MAvva 136,52,152
v_or3_b32 v196, v160, v161, v162
v_or_b32 v196, v196, v163
MAvva 132,24,156
v_and_b32 v196, 0x40004000, v196
v_cmp_ne_u32 vcc, 0, v196
MAvva 136,56,156
s_cbranch_vccnz Lat_stub4
Lat_ret4:
buffer_load_dwordx4 v184, s[28:31], s42 offen lds
s_waitcnt lgkmcnt(0)
MAvva 132,28,160
buffer_load_dwordx4 v184, s[28:31], s42 offen offset:1024 lds
MAvva 136,60,160
Rv 68,181,0
MAvva 132,32,164
Rv 72,181,1024
MAvva 136,64,164
Rv 76,181,2048
v_mfma_f32_16x16x32_bf16 v[140:143], v[4:7], a[168:171], v[152:155]
Rv 80,181,3072
v_mfma_f32_16x16x32_bf16 v[144:147], v[36:39], a[168:171], v[152:155]
Rv 84,181,4096
MAvva 140,8,172
Rv 88,181,5120
MAvva 144,40,172
buffer_load_dwordx4 v184, s[28:31], s42 offen offset:2048 lds
MAvva 140,12,176
Rv 92,181,6144
MAvva 144,44,176
Rv 96,181,7168
MAvva 140,16,180
Rv 100,181,8192
MAvva 144,48,180
Rv 104,181,9216
MAvva 140,20,184
Rv 108,181,10240
MAvva 144,52,184
Rv 112,181,11264
MAvva 140,24,188
buffer_load_dwordx4 v184, s[28:31], s42 offen offset:3072 lds
MAvva 144,56,188
Rv 116,181,12288
MAvva 140,28,192
Rv 120,181,13312
MAvva 144,60,192
Rv 124,181,14336
MAvva 140,32,196
Rv 128,181,15360
s_add_u32 s51, s51, 1
MAvva 144,64,196
Lat_u1:
s_waitcnt vmcnt(16)
s_barrier
s_waitcnt lgkmcnt(12)
MAavv 0,68,156
s_mov_b32 s39, s38
MAavv 8,72,156
s_add_u32 s20, s38, 32768
s_cmp_eq_u32 s20, 163840
s_cselect_b32 s38, 0, s20
v_add_u32 v180, s38, v182
MAavv 16,76,156
Rv 4,180,0
s_mov_b32 s42, s43
s_mov_b32 s43, s41
MAavv 24,80,156
v_readlane_b32 s41, v206, s46
Rv 36,180,8192
s_waitcnt lgkmcnt(10)
MAavv 32,84,156
s_add_u32 s46, s46, 1
EX 164,132
MAavv 40,88,156
Rv 8,180,1024
v_add_u32 v181, s39, v183
MAavv 48,92,156
s_add_u32 m0, s39, s44
EX 165,133
MAavv 56,96,156
Rv 40,180,9216
s_waitcnt lgkmcnt(8)
MAavv 64,100,156
EX 166,134
MAavv 72,104,156
Rv 12,180,2048
MAavv 80,108,156
EX 167,135
MAavv 88,112,156
Rv 44,180,10240
s_waitcnt lgkmcnt(6)
MAavv 96,116,156
EX 168,136
MAavv 104,120,156
Rv 16,180,3072
MAavv 112,124,156
EX 169,137
MAavv 120,128,156
Rv 48,180,11264
MAaav 128,200,156
EX 170,138
MAavv 4,68,160
Rv 20,180,4096
CV 156,164,165
MAavv 12,72,160
EX 171,139
MAavv 20,76,160
Rv 52,180,12288
CV 157,166,167
MAavv 28,80,160
s_add_u32 s20, s40, 32768
s_cmp_eq_u32 s20, 163840
s_cselect_b32 s40, 0, s20
MAavv 36,84,160
Rv 24,180,5120
CV 158,168,169
MAavv 44,88,160
CV 159,170,171
v_or3_b32 v195, v156, v157, v158
MAavv 52,92,160
Rv 56,180,13312
v_or_b32 v195, v195, v159
MAavv 60,96,160
v_and_b32 v195, 0x40004000, v195
v_cmp_ne_u32 vcc, 0, v195
MAavv 68,100,160
Rv 28,180,6144
s_cbranch_vccnz Lat_stub9
Lat_ret9:
MAavv 76,104,160
buffer_load_dwordx4 v184, s[24:27], s41 offen lds
MAavv 84,108,160
Rv 60,180,14336
MAavv 92,112,160
buffer_load_dwordx4 v184, s[24:27], s41 offen offset:1024 lds
MAavv 100,116,160
Rv 32,180,7168
MAavv 108,120,160
buffer_load_dwordx4 v184, s[24:27], s41 offen offset:2048 lds
MAavv 116,124,160
Rv 64,180,15360
MAavv 124,128,160
buffer_load_dwordx4 v184, s[24:27], s41 offen offset:3072 lds
MAaav 132,200,160
EX 172,140
s_waitcnt lgkmcnt(12)
v_mfma_f32_16x16x32_bf16 v[132:135], v[4:7], a[136:139], v[148:151]
EX 173,141
v_mfma_f32_16x16x32_bf16 v[136:139], v[36:39], a[136:139], v[148:151]
EX 174,142
MAvva 132,8,140
s_add_u32 m0, s40, s45
EX 175,143
MAvva 136,40,140
EX 176,144
s_waitcnt lgkmcnt(8)
MAvva 132,12,144
EX 177,145
MAvva 136,44,144
EX 178,146
MAvva 132,16,148
EX 179,147
MAvva 136,48,148
CV 160,172,173
CV 161,174,175
s_waitcnt lgkmcnt(4)
)ASM"
R"ASM(MAvva 132,20,152
CV 162,176,177
CV 163,178,179
MAvva 136,52,152
v_or3_b32 v196, v160, v161, v162
v_or_b32 v196, v196, v163
MAvva 132,24,156
v_and_b32 v196, 0x40004000, v196
v_cmp_ne_u32 vcc, 0, v196
MAvva 136,56,156
s_cbranch_vccnz Lat_stub10
Lat_ret10:
buffer_load_dwordx4 v184, s[28:31], s42 offen lds
s_waitcnt lgkmcnt(0)
MAvva 132,28,160
buffer_load_dwordx4 v184, s[28:31], s42 offen offset:1024 lds
MAvva 136,60,160
Rv 68,181,0
MAvva 132,32,164
Rv 72,181,1024
MAvva 136,64,164
Rv 76,181,2048
v_mfma_f32_16x16x32_bf16 v[140:143], v[4:7], a[168:171], v[152:155]
Rv 80,181,3072
v_mfma_f32_16x16x32_bf16 v[144:147], v[36:39], a[168:171], v[152:155]
Rv 84,181,4096
MAvva 140,8,172
Rv 88,181,5120
MAvva 144,40,172
buffer_load_dwordx4 v184, s[28:31], s42 offen offset:2048 lds
MAvva 140,12,176
Rv 92,181,6144
MAvva 144,44,176
Rv 96,181,7168
MAvva 140,16,180
Rv 100,181,8192
MAvva 144,48,180
Rv 104,181,9216
MAvva 140,20,184
Rv 108,181,10240
MAvva 144,52,184
Rv 112,181,11264
MAvva 140,24,188
buffer_load_dwordx4 v184, s[28:31], s42 offen offset:3072 lds
MAvva 144,56,188
Rv 116,181,12288
MAvva 140,28,192
Rv 120,181,13312
MAvva 144,60,192
Rv 124,181,14336
MAvva 140,32,196
Rv 128,181,15360
s_add_u32 s51, s51, 1
MAvva 144,64,196
Lat_u2:
s_waitcnt vmcnt(16)
s_barrier
s_waitcnt lgkmcnt(12)
MAavv 0,68,156
s_mov_b32 s39, s38
MAavv 8,72,156
s_add_u32 s20, s38, 32768
s_cmp_eq_u32 s20, 163840
s_cselect_b32 s38, 0, s20
v_add_u32 v180, s38, v182
MAavv 16,76,156
Rv 4,180,0
s_mov_b32 s42, s43
s_mov_b32 s43, s41
MAavv 24,80,156
v_readlane_b32 s41, v206, s46
Rv 36,180,8192
s_waitcnt lgkmcnt(10)
MAavv 32,84,156
s_add_u32 s46, s46, 1
EX 164,132
MAavv 40,88,156
Rv 8,180,1024
v_add_u32 v181, s39, v183
MAavv 48,92,156
s_add_u32 m0, s39, s44
EX 165,133
MAavv 56,96,156
Rv 40,180,9216
s_waitcnt lgkmcnt(8)
MAavv 64,100,156
EX 166,134
MAavv 72,104,156
Rv 12,180,2048
MAavv 80,108,156
EX 167,135
MAavv 88,112,156
Rv 44,180,10240
s_waitcnt lgkmcnt(6)
MAavv 96,116,156
EX 168,136
MAavv 104,120,156
Rv 16,180,3072
MAavv 112,124,156
EX 169,137
MAavv 120,128,156
Rv 48,180,11264
MAaav 128,200,156
EX 170,138
MAavv 4,68,160
Rv 20,180,4096
CV 156,164,165
MAavv 12,72,160
EX 171,139
MAavv 20,76,160
Rv 52,180,12288
CV 157,166,167
MAavv 28,80,160
s_add_u32 s20, s40, 32768
s_cmp_eq_u32 s20, 163840
s_cselect_b32 s40, 0, s20
MAavv 36,84,160
Rv 24,180,5120
CV 158,168,169
MAavv 44,88,160
CV 159,170,171
v_or3_b32 v195, v156, v157, v158
MAavv 52,92,160
Rv 56,180,13312
v_or_b32 v195, v195, v159
MAavv 60,96,160
v_and_b32 v195, 0x40004000, v195
v_cmp_ne_u32 vcc, 0, v195
MAavv 68,100,160
Rv 28,180,6144
s_cbranch_vccnz Lat_stub11
Lat_ret11:
MAavv 76,104,160
buffer_load_dwordx4 v184, s[24:27], s41 offen lds
MAavv 84,108,160
Rv 60,180,14336
MAavv 92,112,160
buffer_load_dwordx4 v184, s[24:27], s41 offen offset:1024 lds
MAavv 100,116,160
Rv 32,180,7168
MAavv 108,120,160
buffer_load_dwordx4 v184, s[24:27], s41 offen offset:2048 lds
MAavv 116,124,160
Rv 64,180,15360
MAavv 124,128,160
buffer_load_dwordx4 v184, s[24:27], s41 offen offset:3072 lds
MAaav 132,200,160
EX 172,140
s_waitcnt lgkmcnt(12)
v_mfma_f32_16x16x32_bf16 v[132:135], v[4:7], a[136:139], v[148:151]
EX 173,141
v_mfma_f32_16x16x32_bf16 v[136:139], v[36:39], a[136:139], v[148:151]
EX 174,142
MAvva 132,8,140
s_add_u32 m0, s40, s45
EX 175,143
MAvva 136,40,140
EX 176,144
s_waitcnt lgkmcnt(8)
MAvva 132,12,144
EX 177,145
MAvva 136,44,144
EX 178,146
MAvva 132,16,148
EX 179,147
MAvva 136,48,148
CV 160,172,173
CV 161,174,175
s_waitcnt lgkmcnt(4)
MAvva 132,20,152
CV 162,176,177
CV 163,178,179
MAvva 136,52,152
v_or3_b32 v196, v160, v161, v162
v_or_b32 v196, v196, v163
MAvva 132,24,156
v_and_b32 v196, 0x40004000, v196
v_cmp_ne_u32 vcc, 0, v196
MAvva 136,56,156
s_cbranch_vccnz Lat_stub12
Lat_ret12:
buffer_load_dwordx4 v184, s[28:31], s42 offen lds
s_waitcnt lgkmcnt(0)
MAvva 132,28,160
buffer_load_dwordx4 v184, s[28:31], s42 offen offset:1024 lds
MAvva 136,60,160
Rv 68,181,0
MAvva 132,32,164
Rv 72,181,1024
MAvva 136,64,164
Rv 76,181,2048
v_mfma_f32_16x16x32_bf16 v[140:143], v[4:7], a[168:171], v[152:155]
Rv 80,181,3072
v_mfma_f32_16x16x32_bf16 v[144:147], v[36:39], a[168:171], v[152:155]
Rv 84,181,4096
MAvva 140,8,172
Rv 88,181,5120
MAvva 144,40,172
buffer_load_dwordx4 v184, s[28:31], s42 offen offset:2048 lds
MAvva 140,12,176
Rv 92,181,6144
MAvva 144,44,176
Rv 96,181,7168
MAvva 140,16,180
Rv 100,181,8192
MAvva 144,48,180
Rv 104,181,9216
MAvva 140,20,184
Rv 108,181,10240
MAvva 144,52,184
Rv 112,181,11264
MAvva 140,24,188
buffer_load_dwordx4 v184, s[28:31], s42 offen offset:3072 lds
MAvva 144,56,188
Rv 116,181,12288
MAvva 140,28,192
Rv 120,181,13312
MAvva 144,60,192
Rv 124,181,14336
MAvva 140,32,196
Rv 128,181,15360
s_add_u32 s51, s51, 1
MAvva 144,64,196
Lat_u3:
s_waitcnt vmcnt(16)
s_barrier
s_waitcnt lgkmcnt(12)
MAavv 0,68,156
s_mov_b32 s39, s38
MAavv 8,72,156
s_add_u32 s20, s38, 32768
s_cmp_eq_u32 s20, 163840
s_cselect_b32 s38, 0, s20
v_add_u32 v180, s38, v182
MAavv 16,76,156
Rv 4,180,0
s_mov_b32 s42, s43
s_mov_b32 s43, s41
MAavv 24,80,156
v_readlane_b32 s41, v206, s46
Rv 36,180,8192
s_waitcnt lgkmcnt(10)
MAavv 32,84,156
s_add_u32 s46, s46, 1
EX 164,132
MAavv 40,88,156
Rv 8,180,1024
v_add_u32 v181, s39, v183
MAavv 48,92,156
s_add_u32 m0, s39, s44
EX 165,133
MAavv 56,96,156
Rv 40,180,9216
s_waitcnt lgkmcnt(8)
MAavv 64,100,156
EX 166,134
MAavv 72,104,156
Rv 12,180,2048
MAavv 80,108,156
EX 167,135
MAavv 88,112,156
Rv 44,180,10240
s_waitcnt lgkmcnt(6)
MAavv 96,116,156
EX 168,136
MAavv 104,120,156
Rv 16,180,3072
MAavv 112,124,156
EX 169,137
MAavv 120,128,156
Rv 48,180,11264
MAaav 128,200,156
EX 170,138
MAavv 4,68,160
Rv 20,180,4096
CV 156,164,165
MAavv 12,72,160
EX 171,139
MAavv 20,76,160
Rv 52,180,12288
CV 157,166,167
MAavv 28,80,160
s_add_u32 s20, s40, 32768
s_cmp_eq_u32 s20, 163840
s_cselect_b32 s40, 0, s20
MAavv 36,84,160
Rv 24,180,5120
CV 158,168,169
MAavv 44,88,160
CV 159,170,171
v_or3_b32 v195, v156, v157, v158
MAavv 52,92,160
Rv 56,180,13312
v_or_b32 v195, v195, v159
MAavv 60,96,160
v_and_b32 v195, 0x40004000, v195
v_cmp_ne_u32 vcc, 0, v195
MAavv 68,100,160
Rv 28,180,6144
s_cbranch_vccnz Lat_stub13
Lat_ret13:
MAavv 76,104,160
buffer_load_dwordx4 v184, s[24:27], s41 offen lds
MAavv 84,108,160
Rv 60,180,14336
MAavv 92,112,160
buffer_load_dwordx4 v184, s[24:27], s41 offen offset:1024 lds
MAavv 100,116,160
Rv 32,180,7168
MAavv 108,120,160
buffer_load_dwordx4 v184, s[24:27], s41 offen offset:2048 lds
MAavv 116,124,160
Rv 64,180,15360
MAavv 124,128,160
buffer_load_dwordx4 v184, s[24:27], s41 offen offset:3072 lds
MAaav 132,200,160
EX 172,140
s_waitcnt lgkmcnt(12)
v_mfma_f32_16x16x32_bf16 v[132:135], v[4:7], a[136:139], v[148:151]
EX 173,141
v_mfma_f32_16x16x32_bf16 v[136:139], v[36:39], a[136:139], v[148:151]
EX 174,142
MAvva 132,8,140
s_add_u32 m0, s40, s45
EX 175,143
MAvva 136,40,140
EX 176,144
s_waitcnt lgkmcnt(8)
MAvva 132,12,144
EX 177,145
MAvva 136,44,144
EX 178,146
MAvva 132,16,148
EX 179,147
MAvva 136,48,148
CV 160,172,173
CV 161,174,175
s_waitcnt lgkmcnt(4)
MAvva 132,20,152
CV 162,176,177
CV 163,178,179
MAvva 136,52,152
v_or3_b32 v196, v160, v161, v162
v_or_b32 v196, v196, v163
MAvva 132,24,156
v_and_b32 v196, 0x40004000, v196
v_cmp_ne_u32 vcc, 0, v196
MAvva 136,56,156
s_cbranch_vccnz Lat_stub14
Lat_ret14:
buffer_load_dwordx4 v184, s[28:31], s42 offen lds
s_waitcnt lgkmcnt(0)
MAvva 132,28,160
buffer_load_dwordx4 v184, s[28:31], s42 offen offset:1024 lds
MAvva 136,60,160
Rv 68,181,0
MAvva 132,32,164
Rv 72,181,1024
MAvva 136,64,164
Rv 76,181,2048
v_mfma_f32_16x16x32_bf16 v[140:143], v[4:7], a[168:171], v[152:155]
Rv 80,181,3072
v_mfma_f32_16x16x32_bf16 v[144:147], v[36:39], a[168:171], v[152:155]
Rv 84,181,4096
MAvva 140,8,172
Rv 88,181,5120
MAvva 144,40,172
buffer_load_dwordx4 v184, s[28:31], s42 offen offset:2048 lds
MAvva 140,12,176
Rv 92,181,6144
MAvva 144,44,176
Rv 96,181,7168
MAvva 140,16,180
Rv 100,181,8192
MAvva 144,48,180
Rv 104,181,9216
MAvva 140,20,184
Rv 108,181,10240
MAvva 144,52,184
Rv 112,181,11264
MAvva 140,24,188
buffer_load_dwordx4 v184, s[28:31], s42 offen offset:3072 lds
MAvva 144,56,188
Rv 116,181,12288
MAvva 140,28,192
Rv 120,181,13312
MAvva 144,60,192
Rv 124,181,14336
MAvva 140,32,196
Rv 128,181,15360
s_add_u32 s51, s51, 1
MAvva 144,64,196
s_sub_u32 s52, s52, 1
s_cmp_lg_u32 s52, 0
s_cbranch_scc1 Lat_u0
Lat_uend:
s_cmp_eq_u32 s53, 0
s_cbranch_scc1 Lat_mend
Lat_mloop:
s_waitcnt vmcnt(16)
s_barrier
s_waitcnt lgkmcnt(12)
MAavv 0,68,156
s_mov_b32 s39, s38
s_add_u32 s22, s51, 1
s_lshl_b32 s22, s22, 5
MAavv 8,72,156
s_add_u32 s20, s38, 32768
s_cmp_eq_u32 s20, 163840
s_cselect_b32 s38, 0, s20
v_add_u32 v180, s38, v182
MAavv 16,76,156
Rv 4,180,0
v_cvt_f32_u32 v194, s22
s_mov_b32 s42, s43
MAavv 24,80,156
Rv 36,180,8192
v_sub_f32 v193, v191, v194
s_waitcnt lgkmcnt(10)
MAavv 32,84,156
v_sub_f32 v194, v192, v194
v_mul_f32 v193, 0x6c800000, v193
s_mov_b32 s43, s41
MAavv 40,88,156
v_readlane_b32 s41, v206, s46
v_mul_f32 v194, 0x6c800000, v194
s_add_u32 s46, s46, 1
MAavv 48,92,156
Rv 8,180,1024
v_min_f32 v132, v132, v193
MAavv 56,96,156
Rv 40,180,9216
v_add_u32 v181, s39, v183
s_waitcnt lgkmcnt(8)
MAavv 64,100,156
s_add_u32 m0, s39, s44
v_add_f32 v199, 0xec800000, v193
v_min_f32 v133, v133, v199
MAavv 72,104,156
Rv 12,180,2048
v_min_f32 v140, v140, v194
MAavv 80,108,156
v_add_f32 v198, 0xed000000, v193
v_min_f32 v134, v134, v198
MAavv 88,112,156
Rv 44,180,10240
s_waitcnt lgkmcnt(6)
MAavv 96,116,156
v_add_f32 v199, 0xed400000, v193
v_min_f32 v135, v135, v199
MAavv 104,120,156
Rv 16,180,3072
MAavv 112,124,156
v_add_f32 v198, 0xee800000, v193
v_min_f32 v136, v136, v198
MAavv 120,128,156
Rv 48,180,11264
MAaav 128,200,156
v_add_f32 v199, 0xee880000, v193
v_min_f32 v137, v137, v199
MAavv 4,68,160
Rv 20,180,4096
MAavv 12,72,160
v_add_f32 v198, 0xee900000, v193
v_min_f32 v138, v138, v198
MAavv 20,76,160
v_add_f32 v199, 0xee980000, v193
v_min_f32 v139, v139, v199
MAavv 28,80,160
EX 164,132
MAavv 36,84,160
EX 165,133
MAavv 44,88,160
EX 166,134
EX 167,135
EX 168,136
EX 169,137
EX 170,138
EX 171,139
Rv 52,180,12288
MAavv 52,92,160
Rv 24,180,5120
CV 156,164,165
MAavv 60,96,160
Rv 56,180,13312
CV 157,166,167
MAavv 68,100,160
Rv 28,180,6144
CV 158,168,169
MAavv 76,104,160
s_add_u32 s20, s40, 32768
s_cmp_eq_u32 s20, 163840
s_cselect_b32 s40, 0, s20
MAavv 84,108,160
Rv 60,180,14336
CV 159,170,171
MAavv 92,112,160
v_or3_b32 v195, v156, v157, v158
v_or_b32 v195, v195, v159
MAavv 100,116,160
Rv 32,180,7168
v_and_b32 v195, 0x40004000, v195
MAavv 108,120,160
v_cmp_ne_u32 vcc, 0, v195
MAavv 116,124,160
buffer_load_dwordx4 v184, s[24:27], s41 offen lds
s_cbranch_vccnz Lat_stub5
Lat_ret5:
MAavv 124,128,160
buffer_load_dwordx4 v184, s[24:27], s41 offen offset:1024 lds
MAaav 132,200,160
Rv 64,180,15360
s_waitcnt lgkmcnt(12)
v_mfma_f32_16x16x32_bf16 v[132:135], v[4:7], a[136:139], v[148:151]
buffer_load_dwordx4 v184, s[24:27], s41 offen offset:2048 lds
v_mfma_f32_16x16x32_bf16 v[136:139], v[36:39], a[136:139], v[148:151]
buffer_load_dwordx4 v184, s[24:27], s41 offen offset:3072 lds
MAvva 132,8,140
s_add_u32 m0, s40, s45
v_add_f32 v201, 0xec800000, v194
v_min_f32 v141, v141, v201
MAvva 136,40,140
v_add_f32 v200, 0xed000000, v194
v_min_f32 v142, v142, v200
s_waitcnt lgkmcnt(8)
MAvva 132,12,144
v_add_f32 v201, 0xed400000, v194
v_min_f32 v143, v143, v201
MAvva 136,44,144
v_add_f32 v200, 0xee800000, v194
v_min_f32 v144, v144, v200
MAvva 132,16,148
v_add_f32 v201, 0xee880000, v194
v_min_f32 v145, v145, v201
MAvva 136,48,148
v_add_f32 v200, 0xee900000, v194
v_min_f32 v146, v146, v200
s_waitcnt lgkmcnt(4)
MAvva 132,20,152
v_add_f32 v201, 0xee980000, v194
v_min_f32 v147, v147, v201
MAvva 136,52,152
EX 172,140
EX 173,141
EX 174,142
EX 175,143
EX 176,144
EX 177,145
EX 178,146
EX 179,147
MAvva 132,24,156
CV 160,172,173
CV 161,174,175
MAvva 136,56,156
CV 162,176,177
CV 163,178,179
s_waitcnt lgkmcnt(0)
MAvva 132,28,160
v_or3_b32 v196, v160, v161, v162
v_or_b32 v196, v196, v163
MAvva 136,60,160
v_and_b32 v196, 0x40004000, v196
v_cmp_ne_u32 vcc, 0, v196
MAvva 132,32,164
s_cbranch_vccnz Lat_stub6
Lat_ret6:
buffer_load_dwordx4 v184, s[28:31], s42 offen lds
MAvva 136,64,164
buffer_load_dwordx4 v184, s[28:31], s42 offen offset:1024 lds
v_mfma_f32_16x16x32_bf16 v[140:143], v[4:7], a[168:171], v[152:155]
Rv 68,181,0
v_mfma_f32_16x16x32_bf16 v[144:147], v[36:39], a[168:171], v[152:155]
Rv 72,181,1024
MAvva 140,8,172
Rv 76,181,2048
MAvva 144,40,172
buffer_load_dwordx4 v184, s[28:31], s42 offen offset:2048 lds
MAvva 140,12,176
Rv 80,181,3072
MAvva 144,44,176
Rv 84,181,4096
MAvva 140,16,180
Rv 88,181,5120
MAvva 144,48,180
Rv 92,181,6144
MAvva 140,20,184
Rv 96,181,7168
MAvva 144,52,184
Rv 100,181,8192
MAvva 140,24,188
buffer_load_dwordx4 v184, s[28:31], s42 offen offset:3072 lds
MAvva 144,56,188
Rv 104,181,9216
MAvva 140,28,192
Rv 108,181,10240
MAvva 144,60,192
Rv 112,181,11264
MAvva 140,32,196
Rv 116,181,12288
s_add_u32 s51, s51, 1
MAvva 144,64,196
Rv 120,181,13312
Rv 124,181,14336
Rv 128,181,15360
s_sub_u32 s53, s53, 1
s_cmp_lg_u32 s53, 0
s_cbranch_scc1 Lat_mloop
Lat_mend:
s_cmp_eq_u32 s35, 0
s_cbranch_scc1 Lat_endpath
s_waitcnt vmcnt(8)
s_barrier
s_waitcnt lgkmcnt(12)
MAavv 0,68,156
s_mov_b32 s39, s38
s_add_u32 s22, s51, 1
s_lshl_b32 s22, s22, 5
MAavv 8,72,156
s_add_u32 s20, s38, 32768
s_cmp_eq_u32 s20, 163840
s_cselect_b32 s38, 0, s20
v_add_u32 v180, s38, v182
MAavv 16,76,156
Rv 4,180,0
v_cvt_f32_u32 v194, s22
s_mov_b32 s42, s43
MAavv 24,80,156
Rv 36,180,8192
v_sub_f32 v193, v191, v194
s_waitcnt lgkmcnt(10)
MAavv 32,84,156
v_sub_f32 v194, v192, v194
v_mul_f32 v193, 0x6c800000, v193
s_mov_b32 s43, s41
MAavv 40,88,156
v_readlane_b32 s41, v206, s46
v_mul_f32 v194, 0x6c800000, v194
s_add_u32 s46, s46, 1
MAavv 48,92,156
Rv 8,180,1024
v_min_f32 v132, v132, v193
MAavv 56,96,156
Rv 40,180,9216
v_add_u32 v181, s39, v183
s_waitcnt lgkmcnt(8)
MAavv 64,100,156
s_add_u32 m0, s39, s44
v_add_f32 v199, 0xec800000, v193
v_min_f32 v133, v133, v199
MAavv 72,104,156
Rv 12,180,2048
v_accvgpr_mov_b32 a136, a204
MAavv 80,108,156
v_add_f32 v198, 0xed000000, v193
v_min_f32 v134, v134, v198
MAavv 88,112,156
Rv 44,180,10240
v_accvgpr_mov_b32 a137, a205
s_waitcnt lgkmcnt(6)
MAavv 96,116,156
v_add_f32 v199, 0xed400000, v193
v_min_f32 v135, v135, v199
MAavv 104,120,156
Rv 16,180,3072
v_accvgpr_mov_b32 a138, a206
MAavv 112,124,156
v_add_f32 v198, 0xee800000, v193
v_min_f32 v136, v136, v198
MAavv 120,128,156
Rv 48,180,11264
v_accvgpr_mov_b32 a139, a207
MAaav 128,200,156
v_add_f32 v199, 0xee880000, v193
v_min_f32 v137, v137, v199
MAavv 4,68,160
Rv 20,180,4096
v_accvgpr_mov_b32 a140, a208
MAavv 12,72,160
v_add_f32 v198, 0xee900000, v193
v_min_f32 v138, v138, v198
MAavv 20,76,160
v_add_f32 v199, 0xee980000, v193
v_min_f32 v139, v139, v199
MAavv 28,80,160
EX 164,132
MAavv 36,84,160
EX 165,133
MAavv 44,88,160
EX 166,134
EX 167,135
EX 168,136
EX 169,137
EX 170,138
EX 171,139
Rv 52,180,12288
MAavv 52,92,160
Rv 24,180,5120
CV 156,164,165
MAavv 60,96,160
Rv 56,180,13312
CV 157,166,167
MAavv 68,100,160
Rv 28,180,6144
CV 158,168,169
MAavv 76,104,160
s_add_u32 s20, s40, 32768
s_cmp_eq_u32 s20, 163840
s_cselect_b32 s40, 0, s20
MAavv 84,108,160
Rv 60,180,14336
CV 159,170,171
MAavv 92,112,160
v_or3_b32 v195, v156, v157, v158
v_or_b32 v195, v195, v159
MAavv 100,116,160
Rv 32,180,7168
v_and_b32 v195, 0x40004000, v195
MAavv 108,120,160
v_cmp_ne_u32 vcc, 0, v195
v_accvgpr_mov_b32 a141, a209
MAavv 116,124,160
buffer_load_dwordx4 v184, s[24:27], s41 offen lds
s_cbranch_vccnz Lat_stub7
Lat_ret7:
MAavv 124,128,160
v_accvgpr_mov_b32 a142, a210
v_accvgpr_mov_b32 a143, a211
MAaav 132,200,160
v_accvgpr_mov_b32 a144, a212
v_accvgpr_mov_b32 a145, a213
v_accvgpr_mov_b32 a146, a214
v_accvgpr_mov_b32 a147, a215
v_accvgpr_mov_b32 a148, a216
v_accvgpr_mov_b32 a149, a217
v_accvgpr_mov_b32 a150, a218
v_accvgpr_mov_b32 a151, a219
v_accvgpr_mov_b32 a152, a220
v_accvgpr_mov_b32 a153, a221
v_accvgpr_mov_b32 a154, a222
v_accvgpr_mov_b32 a155, a223
v_accvgpr_mov_b32 a156, a224
v_accvgpr_mov_b32 a157, a225
v_accvgpr_mov_b32 a158, a226
v_accvgpr_mov_b32 a159, a227
v_accvgpr_mov_b32 a160, a228
v_accvgpr_mov_b32 a161, a229
v_accvgpr_mov_b32 a162, a230
v_accvgpr_mov_b32 a163, a231
v_accvgpr_mov_b32 a164, a232
v_accvgpr_mov_b32 a165, a233
v_accvgpr_mov_b32 a166, a234
v_accvgpr_mov_b32 a167, a235
s_waitcnt lgkmcnt(11)
MZvva 132,4,136
buffer_load_dwordx4 v184, s[24:27], s41 offen offset:1024 lds
Rv 64,180,15360
MZvva 136,36,136
buffer_load_dwordx4 v184, s[24:27], s41 offen offset:2048 lds
MAvva 132,8,140
buffer_load_dwordx4 v184, s[24:27], s41 offen offset:3072 lds
MAvva 136,40,140
s_add_u32 m0, s40, s45
v_min_f32 v140, v140, v194
s_waitcnt lgkmcnt(8)
MAvva 132,12,144
v_add_f32 v201, 0xec800000, v194
v_min_f32 v141, v141, v201
MAvva 136,44,144
v_add_f32 v200, 0xed000000, v194
v_min_f32 v142, v142, v200
MAvva 132,16,148
v_add_f32 v201, 0xed400000, v194
v_min_f32 v143, v143, v201
MAvva 136,48,148
v_add_f32 v200, 0xee800000, v194
v_min_f32 v144, v144, v200
s_waitcnt lgkmcnt(4)
MAvva 132,20,152
v_add_f32 v201, 0xee880000, v194
v_min_f32 v145, v145, v201
v_add_f32 v200, 0xee900000, v194
v_min_f32 v146, v146, v200
v_add_f32 v201, 0xee980000, v194
v_min_f32 v147, v147, v201
MAvva 136,52,152
EX 172,140
EX 173,141
EX 174,142
EX 175,143
EX 176,144
EX 177,145
EX 178,146
EX 179,147
MAvva 132,24,156
CV 160,172,173
CV 161,174,175
MAvva 136,56,156
CV 162,176,177
CV 163,178,179
s_waitcnt lgkmcnt(0)
MAvva 132,28,160
v_accvgpr_write_b32 a168, v212
v_accvgpr_write_b32 a169, v213
MAvva 136,60,160
v_accvgpr_write_b32 a170, v214
v_accvgpr_write_b32 a171, v215
v_accvgpr_write_b32 a172, v216
v_accvgpr_write_b32 a173, v217
v_accvgpr_write_b32 a174, v218
v_accvgpr_write_b32 a175, v219
v_accvgpr_write_b32 a176, v220
v_accvgpr_write_b32 a177, v221
v_accvgpr_write_b32 a178, v222
v_accvgpr_write_b32 a179, v223
v_accvgpr_write_b32 a180, v224
v_accvgpr_write_b32 a181, v225
v_accvgpr_write_b32 a182, v226
v_accvgpr_write_b32 a183, v227
v_accvgpr_write_b32 a184, v228
v_accvgpr_write_b32 a185, v229
v_accvgpr_write_b32 a186, v230
v_accvgpr_write_b32 a187, v231
v_accvgpr_write_b32 a188, v232
v_accvgpr_write_b32 a189, v233
v_accvgpr_write_b32 a190, v234
v_accvgpr_write_b32 a191, v235
v_accvgpr_write_b32 a192, v236
v_accvgpr_write_b32 a193, v237
v_accvgpr_write_b32 a194, v238
v_accvgpr_write_b32 a195, v239
v_accvgpr_write_b32 a196, v240
v_accvgpr_write_b32 a197, v241
v_accvgpr_write_b32 a198, v242
v_accvgpr_write_b32 a199, v243
MAvva 132,32,164
v_or3_b32 v196, v160, v161, v162
v_or_b32 v196, v196, v163
v_and_b32 v196, 0x40004000, v196
v_cmp_ne_u32 vcc, 0, v196
MAvva 136,64,164
s_cbranch_vccnz Lat_stub8
Lat_ret8:
buffer_load_dwordx4 v184, s[28:31], s42 offen lds
MZvva 140,4,168
buffer_load_dwordx4 v184, s[28:31], s42 offen offset:1024 lds
MZvva 144,36,168
Rv 68,181,0
MAvva 140,8,172
Rv 72,181,1024
MAvva 144,40,172
buffer_load_dwordx4 v184, s[28:31], s42 offen offset:2048 lds
MAvva 140,12,176
Rv 76,181,2048
MAvva 144,44,176
Rv 80,181,3072
MAvva 140,16,180
Rv 84,181,4096
MAvva 144,48,180
Rv 88,181,5120
MAvva 140,20,184
Rv 92,181,6144
MAvva 144,52,184
Rv 96,181,7168
MAvva 140,24,188
buffer_load_dwordx4 v184, s[28:31], s42 offen offset:3072 lds
MAvva 144,56,188
Rv 100,181,8192
MAvva 140,28,192
Rv 104,181,9216
MAvva 144,60,192
Rv 108,181,10240
MAvva 140,32,196
Rv 112,181,11264
s_add_u32 s51, s51, 1
MAvva 144,64,196
Rv 116,181,12288
Rv 120,181,13312
Rv 124,181,14336
Rv 128,181,15360
s_mov_b32 s47, s59
s_mov_b32 s48, s56
s_mov_b32 s49, s57
s_mov_b32 s50, s58
s_mov_b32 s34, s35
s_mov_b32 s35, s36
s_mov_b32 s36, s37
s_mov_b32 s37, 0
s_and_b32 s54, s34, 0x7f
s_bfe_u32 s55, s34, 458760
s_bfe_u32 s56, s34, 262159
s_bfe_u32 s57, s34, 65555
s_bfe_u32 s58, s34, 131092
s_lshl_b32 s20, s18, 1
s_add_u32 s57, s57, s20
s_lshl_b32 s59, s57, 4
s_add_u32 s59, s59, s56
s_lshl_b32 s20, s56, 2
s_sub_i32 s20, s20, s55
s_sub_i32 s20, s20, 2
s_max_i32 s20, s20, 0
s_sub_u32 s21, s54, 3
s_min_u32 s52, s20, s21
s_sub_u32 s53, s21, s52
s_lshl_b32 s20, s56, 7
s_lshl_b32 s21, s16, 5
s_add_u32 s20, s20, s21
v_lshrrev_b32 v198, 4, v182
v_and_b32 v199, 15, v198
v_lshrrev_b32 v198, 4, v198
v_lshlrev_b32 v198, 2, v198
v_add_u32 v199, s20, v199
v_sub_u32 v199, v199, v198
v_cvt_f32_i32 v199, v199
v_add_f32 v191, 0.5, v199
v_add_f32 v192, 0x41840000, v199
s_sub_u32 s51, s55, 1
s_waitcnt vmcnt(16)
s_barrier
s_waitcnt lgkmcnt(12)
MAavv 0,68,156
s_mov_b32 s39, s38
v_mov_b32 v187, v185
v_mov_b32 v188, v186
MAavv 8,72,156
s_add_u32 s20, s38, 32768
s_cmp_eq_u32 s20, 163840
s_cselect_b32 s38, 0, s20
v_add_u32 v180, s38, v182
MAavv 16,76,156
Rv 4,180,0
s_mov_b32 s42, s43
s_mov_b32 s43, s41
MAavv 24,80,156
v_readlane_b32 s41, v206, s46
Rv 36,180,8192
s_waitcnt lgkmcnt(10)
MAavv 32,84,156
s_add_u32 s46, s46, 1
s_add_u32 s22, s51, 1
s_lshl_b32 s22, s22, 5
MAavv 40,88,156
v_cvt_f32_u32 v194, s22
v_sub_f32 v193, v191, v194
s_bfe_u32 s21, s35, 65555
MAavv 48,92,156
v_sub_f32 v194, v192, v194
v_mul_f32 v193, 0x6c800000, v193
v_mul_f32 v194, 0x6c800000, v194
MAavv 56,96,156
Rv 8,180,1024
s_add_u32 m0, s39, s44
s_waitcnt lgkmcnt(7)
MAavv 64,100,156
Rv 40,180,9216
s_lshl_b32 s22, s18, 1
s_add_u32 s21, s21, s22
MAavv 72,104,156
s_lshl_b32 s60, s21, 20
s_bfe_u32 s21, s35, 262159
s_lshl_b32 s21, s21, 16
MAavv 80,108,156
s_add_u32 s60, s60, s21
s_lshl_b32 s21, s16, 14
s_add_u32 s60, s60, s21
s_cmp_eq_u32 s35, 0
s_cselect_b32 s60, 0x7ff00000, s60
MAavv 88,112,156
Rv 12,180,2048
v_min_f32 v132, v132, v193
s_waitcnt lgkmcnt(5)
MAavv 96,116,156
Rv 44,180,10240
v_add_u32 v181, s39, v183
MAavv 104,120,156
Rv 16,180,3072
v_min_f32 v140, v140, v194
MAavv 112,124,156
v_add_f32 v199, 0xec800000, v193
v_min_f32 v133, v133, v199
MAavv 120,128,156
Rv 48,180,11264
MAaav 128,200,156
v_add_f32 v198, 0xed000000, v193
v_min_f32 v134, v134, v198
MAavv 4,68,160
Rv 20,180,4096
MAavv 12,72,160
v_add_f32 v199, 0xed400000, v193
v_min_f32 v135, v135, v199
MAavv 20,76,160
Rv 52,180,12288
MAavv 28,80,160
v_add_f32 v198, 0xee800000, v193
v_min_f32 v136, v136, v198
MAavv 36,84,160
Rv 24,180,5120
MAavv 44,88,160
v_add_f32 v199, 0xee880000, v193
v_min_f32 v137, v137, v199
MAavv 52,92,160
Rv 56,180,13312
MAavv 60,96,160
v_add_f32 v198, 0xee900000, v193
v_min_f32 v138, v138, v198
MAavv 68,100,160
v_add_f32 v199, 0xee980000, v193
v_min_f32 v139, v139, v199
MAavv 76,104,160
v_max3_f32 v189, v132, v133, v134
v_max3_f32 v189, v189, v135, v136
MAavv 84,108,160
v_max3_f32 v189, v189, v137, v138
v_max_f32 v189, v189, v139
MAavv 92,112,160
v_mov_b32 v202, v189
v_mov_b32 v203, v189
Rv 28,180,6144
MAavv 100,116,160
s_nop 0
v_permlane16_swap_b32 v202, v203
Rv 60,180,14336
MAavv 108,120,160
v_max_f32 v189, v202, v203
v_mov_b32 v202, v189
v_mov_b32 v203, v189
s_add_u32 s20, s40, 32768
s_cmp_eq_u32 s20, 163840
s_cselect_b32 s40, 0, s20
MAavv 116,124,160
s_nop 0
v_permlane32_swap_b32 v202, v203
Rv 32,180,7168
MAavv 124,128,160
v_max_f32 v189, v202, v203
v_max_f32 v189, 0xc9742400, v189
v_add_f32 v185, 0x41c00000, v189
v_sub_f32 v148, 0, v185
v_sub_f32 v149, 0, v185
v_sub_f32 v150, 0, v185
v_sub_f32 v151, 0, v185
v_sub_f32 v132, v132, v185
v_sub_f32 v133, v133, v185
v_sub_f32 v134, v134, v185
v_sub_f32 v135, v135, v185
v_sub_f32 v136, v136, v185
v_sub_f32 v137, v137, v185
v_sub_f32 v138, v138, v185
v_sub_f32 v139, v139, v185
MAaav 132,200,160
EX 164,132
EX 165,133
EX 166,134
EX 167,135
EX 168,136
EX 169,137
EX 170,138
EX 171,139
buffer_load_dwordx4 v184, s[24:27], s41 offen lds
s_waitcnt lgkmcnt(11)
v_mfma_f32_16x16x32_bf16 v[132:135], v[4:7], a[136:139], v[148:151]
buffer_load_dwordx4 v184, s[24:27], s41 offen offset:1024 lds
Rv 64,180,15360
v_mfma_f32_16x16x32_bf16 v[136:139], v[36:39], a[136:139], v[148:151]
buffer_load_dwordx4 v184, s[24:27], s41 offen offset:2048 lds
MAvva 132,8,140
buffer_load_dwordx4 v184, s[24:27], s41 offen offset:3072 lds
MAvva 136,40,140
s_add_u32 m0, s40, s45
buffer_load_dwordx4 a[204:207], v182, s[12:15], s60 offen
s_waitcnt lgkmcnt(8)
MAvva 132,12,144
v_add_f32 v201, 0xec800000, v194
v_min_f32 v141, v141, v201
MAvva 136,44,144
v_add_f32 v200, 0xed000000, v194
v_min_f32 v142, v142, v200
MAvva 132,16,148
v_add_f32 v201, 0xed400000, v194
v_min_f32 v143, v143, v201
MAvva 136,48,148
v_add_f32 v200, 0xee800000, v194
v_min_f32 v144, v144, v200
s_waitcnt lgkmcnt(4)
MAvva 132,20,152
v_add_f32 v201, 0xee880000, v194
v_min_f32 v145, v145, v201
MAvva 136,52,152
v_add_f32 v200, 0xee900000, v194
v_min_f32 v146, v146, v200
v_add_f32 v201, 0xee980000, v194
v_min_f32 v147, v147, v201
MAvva 132,24,156
v_max3_f32 v190, v140, v141, v142
v_max3_f32 v190, v190, v143, v144
v_max3_f32 v190, v190, v145, v146
v_max_f32 v190, v190, v147
v_mov_b32 v204, v190
v_mov_b32 v205, v190
MAvva 136,56,156
s_nop 0
v_permlane16_swap_b32 v204, v205
s_waitcnt lgkmcnt(0)
MAvva 132,28,160
v_max_f32 v190, v204, v205
v_mov_b32 v204, v190
v_mov_b32 v205, v190
MAvva 136,60,160
s_nop 0
v_permlane32_swap_b32 v204, v205
Rv 68,181,0
MAvva 132,32,164
v_max_f32 v190, v204, v205
v_max_f32 v190, 0xc9742400, v190
v_add_f32 v186, 0x41c00000, v190
v_sub_f32 v152, 0, v186
v_sub_f32 v153, 0, v186
v_sub_f32 v154, 0, v186
v_sub_f32 v155, 0, v186
v_sub_f32 v140, v140, v186
v_sub_f32 v141, v141, v186
v_sub_f32 v142, v142, v186
v_sub_f32 v143, v143, v186
v_sub_f32 v144, v144, v186
v_sub_f32 v145, v145, v186
v_sub_f32 v146, v146, v186
v_sub_f32 v147, v147, v186
MAvva 136,64,164
EX 172,140
EX 173,141
EX 174,142
EX 175,143
EX 176,144
EX 177,145
EX 178,146
EX 179,147
v_mfma_f32_16x16x32_bf16 v[140:143], v[4:7], a[168:171], v[152:155]
buffer_load_dwordx4 a[208:211], v182, s[12:15], s60 offen offset:1024
v_mfma_f32_16x16x32_bf16 v[144:147], v[36:39], a[168:171], v[152:155]
buffer_load_dwordx4 a[212:215], v182, s[12:15], s60 offen offset:2048
MAvva 140,8,172
buffer_load_dwordx4 a[216:219], v182, s[12:15], s60 offen offset:3072
s_add_u32 s60, s60, 4096
MAvva 144,40,172
buffer_load_dwordx4 a[220:223], v182, s[12:15], s60 offen
MAvva 140,12,176
buffer_load_dwordx4 a[224:227], v182, s[12:15], s60 offen offset:1024
MAvva 144,44,176
buffer_load_dwordx4 a[228:231], v182, s[12:15], s60 offen offset:2048
MAvva 140,16,180
buffer_load_dwordx4 a[232:235], v182, s[12:15], s60 offen offset:3072
s_add_u32 s60, s60, 4096
MAvva 144,48,180
buffer_load_dwordx4 v184, s[28:31], s42 offen lds
MAvva 140,20,184
buffer_load_dwordx4 v184, s[28:31], s42 offen offset:1024 lds
MAvva 144,52,184
buffer_load_dwordx4 v184, s[28:31], s42 offen offset:2048 lds
MAvva 140,24,188
buffer_load_dwordx4 v184, s[28:31], s42 offen offset:3072 lds
MAvva 144,56,188
Rv 72,181,1024
CV 156,164,165
MAvva 140,28,192
Rv 76,181,2048
CV 157,166,167
MAvva 144,60,192
Rv 80,181,3072
CV 158,168,169
MAvva 140,32,196
Rv 84,181,4096
CV 159,170,171
s_add_u32 s51, s51, 1
MAvva 144,64,196
Rv 88,181,5120
Rv 92,181,6144
Rv 96,181,7168
Rv 100,181,8192
Rv 104,181,9216
Rv 108,181,10240
Rv 112,181,11264
Rv 116,181,12288
Rv 120,181,13312
Rv 124,181,14336
Rv 128,181,15360
CV 160,172,173
CV 161,174,175
CV 162,176,177
CV 163,178,179
s_cmp_eq_u32 s50, 1
s_cbranch_scc1 Lat_epP
s_cmp_eq_u32 s50, 2
s_cbranch_scc1 Lat_epM
Lat_epF:
s_nop 7
AR 198,128
AR 199,132
v_rcp_f32 v195, v198
v_rcp_f32 v196, v199
s_lshl_b32 s20, s48, 3
s_lshl_b32 s21, s16, 1
s_add_u32 s20, s20, s21
s_lshl_b32 s20, s20, 17
s_lshl_b32 s21, s49, 13
s_add_u32 s20, s20, s21
s_add_u32 s20, s20, 50331648
AR 4,0
AR 5,1
AR 6,2
AR 7,3
AR 8,8
AR 9,9
AR 10,10
AR 11,11
v_mul_f32 v4, v4, v195
v_mul_f32 v5, v5, v195
v_mul_f32 v6, v6, v195
v_mul_f32 v7, v7, v195
v_mul_f32 v8, v8, v195
v_mul_f32 v9, v9, v195
v_mul_f32 v10, v10, v195
v_mul_f32 v11, v11, v195
CV 36,4,5
CV 37,6,7
CV 38,8,9
CV 39,10,11
buffer_store_dwordx4 v[36:39], v182, s[12:15], s20 offen sc1
AR 12,16
AR 13,17
AR 14,18
AR 15,19
AR 16,24
AR 17,25
AR 18,26
AR 19,27
v_mul_f32 v12, v12, v195
v_mul_f32 v13, v13, v195
v_mul_f32 v14, v14, v195
v_mul_f32 v15, v15, v195
v_mul_f32 v16, v16, v195
v_mul_f32 v17, v17, v195
v_mul_f32 v18, v18, v195
v_mul_f32 v19, v19, v195
CV 40,12,13
CV 41,14,15
CV 42,16,17
CV 43,18,19
buffer_store_dwordx4 v[40:43], v182, s[12:15], s20 offen offset:1024 sc1
AR 20,32
AR 21,33
AR 22,34
AR 23,35
AR 24,40
AR 25,41
AR 26,42
AR 27,43
v_mul_f32 v20, v20, v195
v_mul_f32 v21, v21, v195
v_mul_f32 v22, v22, v195
v_mul_f32 v23, v23, v195
v_mul_f32 v24, v24, v195
v_mul_f32 v25, v25, v195
v_mul_f32 v26, v26, v195
v_mul_f32 v27, v27, v195
CV 44,20,21
CV 45,22,23
CV 46,24,25
CV 47,26,27
buffer_store_dwordx4 v[44:47], v182, s[12:15], s20 offen offset:2048 sc1
AR 28,48
AR 29,49
AR 30,50
AR 31,51
AR 32,56
AR 33,57
AR 34,58
AR 35,59
v_mul_f32 v28, v28, v195
v_mul_f32 v29, v29, v195
v_mul_f32 v30, v30, v195
v_mul_f32 v31, v31, v195
v_mul_f32 v32, v32, v195
v_mul_f32 v33, v33, v195
v_mul_f32 v34, v34, v195
v_mul_f32 v35, v35, v195
CV 48,28,29
CV 49,30,31
CV 50,32,33
CV 51,34,35
buffer_store_dwordx4 v[48:51], v182, s[12:15], s20 offen offset:3072 sc1
s_add_u32 s20, s20, 4096
AR 4,64
AR 5,65
AR 6,66
AR 7,67
AR 8,72
AR 9,73
AR 10,74
AR 11,75
v_mul_f32 v4, v4, v195
v_mul_f32 v5, v5, v195
v_mul_f32 v6, v6, v195
v_mul_f32 v7, v7, v195
v_mul_f32 v8, v8, v195
v_mul_f32 v9, v9, v195
v_mul_f32 v10, v10, v195
v_mul_f32 v11, v11, v195
CV 52,4,5
CV 53,6,7
CV 54,8,9
CV 55,10,11
buffer_store_dwordx4 v[52:55], v182, s[12:15], s20 offen sc1
AR 12,80
AR 13,81
AR 14,82
AR 15,83
AR 16,88
AR 17,89
AR 18,90
AR 19,91
v_mul_f32 v12, v12, v195
v_mul_f32 v13, v13, v195
v_mul_f32 v14, v14, v195
v_mul_f32 v15, v15, v195
v_mul_f32 v16, v16, v195
v_mul_f32 v17, v17, v195
v_mul_f32 v18, v18, v195
v_mul_f32 v19, v19, v195
CV 56,12,13
CV 57,14,15
CV 58,16,17
CV 59,18,19
buffer_store_dwordx4 v[56:59], v182, s[12:15], s20 offen offset:1024 sc1
AR 20,96
AR 21,97
AR 22,98
AR 23,99
AR 24,104
AR 25,105
AR 26,106
AR 27,107
v_mul_f32 v20, v20, v195
v_mul_f32 v21, v21, v195
v_mul_f32 v22, v22, v195
v_mul_f32 v23, v23, v195
v_mul_f32 v24, v24, v195
v_mul_f32 v25, v25, v195
v_mul_f32 v26, v26, v195
v_mul_f32 v27, v27, v195
CV 60,20,21
CV 61,22,23
CV 62,24,25
CV 63,26,27
buffer_store_dwordx4 v[60:63], v182, s[12:15], s20 offen offset:2048 sc1
AR 28,112
AR 29,113
AR 30,114
AR 31,115
AR 32,120
AR 33,121
AR 34,122
AR 35,123
v_mul_f32 v28, v28, v195
v_mul_f32 v29, v29, v195
v_mul_f32 v30, v30, v195
v_mul_f32 v31, v31, v195
v_mul_f32 v32, v32, v195
v_mul_f32 v33, v33, v195
)ASM"
R"ASM(v_mul_f32 v34, v34, v195
v_mul_f32 v35, v35, v195
CV 64,28,29
CV 65,30,31
CV 66,32,33
CV 67,34,35
buffer_store_dwordx4 v[64:67], v182, s[12:15], s20 offen offset:3072 sc1
s_add_u32 s20, s20, 126976
AR 4,4
AR 5,5
AR 6,6
AR 7,7
AR 8,12
AR 9,13
AR 10,14
AR 11,15
v_mul_f32 v4, v4, v196
v_mul_f32 v5, v5, v196
v_mul_f32 v6, v6, v196
v_mul_f32 v7, v7, v196
v_mul_f32 v8, v8, v196
v_mul_f32 v9, v9, v196
v_mul_f32 v10, v10, v196
v_mul_f32 v11, v11, v196
CV 36,4,5
CV 37,6,7
CV 38,8,9
CV 39,10,11
buffer_store_dwordx4 v[36:39], v182, s[12:15], s20 offen sc1
AR 12,20
AR 13,21
AR 14,22
AR 15,23
AR 16,28
AR 17,29
AR 18,30
AR 19,31
v_mul_f32 v12, v12, v196
v_mul_f32 v13, v13, v196
v_mul_f32 v14, v14, v196
v_mul_f32 v15, v15, v196
v_mul_f32 v16, v16, v196
v_mul_f32 v17, v17, v196
v_mul_f32 v18, v18, v196
v_mul_f32 v19, v19, v196
CV 40,12,13
CV 41,14,15
CV 42,16,17
CV 43,18,19
buffer_store_dwordx4 v[40:43], v182, s[12:15], s20 offen offset:1024 sc1
AR 20,36
AR 21,37
AR 22,38
AR 23,39
AR 24,44
AR 25,45
AR 26,46
AR 27,47
v_mul_f32 v20, v20, v196
v_mul_f32 v21, v21, v196
v_mul_f32 v22, v22, v196
v_mul_f32 v23, v23, v196
v_mul_f32 v24, v24, v196
v_mul_f32 v25, v25, v196
v_mul_f32 v26, v26, v196
v_mul_f32 v27, v27, v196
CV 44,20,21
CV 45,22,23
CV 46,24,25
CV 47,26,27
buffer_store_dwordx4 v[44:47], v182, s[12:15], s20 offen offset:2048 sc1
AR 28,52
AR 29,53
AR 30,54
AR 31,55
AR 32,60
AR 33,61
AR 34,62
AR 35,63
v_mul_f32 v28, v28, v196
v_mul_f32 v29, v29, v196
v_mul_f32 v30, v30, v196
v_mul_f32 v31, v31, v196
v_mul_f32 v32, v32, v196
v_mul_f32 v33, v33, v196
v_mul_f32 v34, v34, v196
v_mul_f32 v35, v35, v196
CV 48,28,29
CV 49,30,31
CV 50,32,33
CV 51,34,35
buffer_store_dwordx4 v[48:51], v182, s[12:15], s20 offen offset:3072 sc1
s_add_u32 s20, s20, 4096
AR 4,68
AR 5,69
AR 6,70
AR 7,71
AR 8,76
AR 9,77
AR 10,78
AR 11,79
v_mul_f32 v4, v4, v196
v_mul_f32 v5, v5, v196
v_mul_f32 v6, v6, v196
v_mul_f32 v7, v7, v196
v_mul_f32 v8, v8, v196
v_mul_f32 v9, v9, v196
v_mul_f32 v10, v10, v196
v_mul_f32 v11, v11, v196
CV 52,4,5
CV 53,6,7
CV 54,8,9
CV 55,10,11
buffer_store_dwordx4 v[52:55], v182, s[12:15], s20 offen sc1
AR 12,84
AR 13,85
AR 14,86
AR 15,87
AR 16,92
AR 17,93
AR 18,94
AR 19,95
v_mul_f32 v12, v12, v196
v_mul_f32 v13, v13, v196
v_mul_f32 v14, v14, v196
v_mul_f32 v15, v15, v196
v_mul_f32 v16, v16, v196
v_mul_f32 v17, v17, v196
v_mul_f32 v18, v18, v196
v_mul_f32 v19, v19, v196
CV 56,12,13
CV 57,14,15
CV 58,16,17
CV 59,18,19
buffer_store_dwordx4 v[56:59], v182, s[12:15], s20 offen offset:1024 sc1
AR 20,100
AR 21,101
AR 22,102
AR 23,103
AR 24,108
AR 25,109
AR 26,110
AR 27,111
v_mul_f32 v20, v20, v196
v_mul_f32 v21, v21, v196
v_mul_f32 v22, v22, v196
v_mul_f32 v23, v23, v196
v_mul_f32 v24, v24, v196
v_mul_f32 v25, v25, v196
v_mul_f32 v26, v26, v196
v_mul_f32 v27, v27, v196
CV 60,20,21
CV 61,22,23
CV 62,24,25
CV 63,26,27
buffer_store_dwordx4 v[60:63], v182, s[12:15], s20 offen offset:2048 sc1
AR 28,116
AR 29,117
AR 30,118
AR 31,119
AR 32,124
AR 33,125
AR 34,126
AR 35,127
v_mul_f32 v28, v28, v196
v_mul_f32 v29, v29, v196
v_mul_f32 v30, v30, v196
v_mul_f32 v31, v31, v196
v_mul_f32 v32, v32, v196
v_mul_f32 v33, v33, v196
v_mul_f32 v34, v34, v196
v_mul_f32 v35, v35, v196
CV 64,28,29
CV 65,30,31
CV 66,32,33
CV 67,34,35
buffer_store_dwordx4 v[64:67], v182, s[12:15], s20 offen offset:3072 sc1
s_branch Lat_segnext
Lat_epM:
s_nop 7
AR 198,128
AR 199,132
s_lshl_b32 s22, s47, 8
s_add_u32 s22, s22, 125829120
v_mov_b32 v200, 0
s_mov_b32 s21, 0
Lat_poll15:
buffer_load_dword v201, v200, s[12:15], s22 offen sc1
s_waitcnt vmcnt(0)
v_readfirstlane_b32 s20, v201
s_cmp_ge_u32 s20, 4
s_cbranch_scc1 Lat_polldone15
s_add_u32 s21, s21, 1
s_cmp_lt_u32 s21, 1048576
s_sleep 2
s_cbranch_scc1 Lat_poll15
Lat_polldone15:
s_barrier
s_lshl_b32 s20, s47, 16
s_lshl_b32 s21, s16, 14
s_add_u32 s20, s20, s21
s_add_u32 s20, s20, 84934656
buffer_load_dwordx4 v[4:7], v182, s[12:15], s20 offen sc1
buffer_load_dwordx4 v[8:11], v182, s[12:15], s20 offen offset:1024 sc1
buffer_load_dwordx4 v[12:15], v182, s[12:15], s20 offen offset:2048 sc1
buffer_load_dwordx4 v[16:19], v182, s[12:15], s20 offen offset:3072 sc1
s_add_u32 s20, s20, 4096
buffer_load_dwordx4 v[20:23], v182, s[12:15], s20 offen sc1
buffer_load_dwordx4 v[24:27], v182, s[12:15], s20 offen offset:1024 sc1
buffer_load_dwordx4 v[28:31], v182, s[12:15], s20 offen offset:2048 sc1
buffer_load_dwordx4 v[32:35], v182, s[12:15], s20 offen offset:3072 sc1
s_add_u32 s20, s20, 4096
buffer_load_dwordx4 v[36:39], v182, s[12:15], s20 offen sc1
buffer_load_dwordx4 v[40:43], v182, s[12:15], s20 offen offset:1024 sc1
buffer_load_dwordx4 v[44:47], v182, s[12:15], s20 offen offset:2048 sc1
buffer_load_dwordx4 v[48:51], v182, s[12:15], s20 offen offset:3072 sc1
s_add_u32 s20, s20, 4096
buffer_load_dwordx4 v[52:55], v182, s[12:15], s20 offen sc1
buffer_load_dwordx4 v[56:59], v182, s[12:15], s20 offen offset:1024 sc1
buffer_load_dwordx4 v[60:63], v182, s[12:15], s20 offen offset:2048 sc1
buffer_load_dwordx4 v[64:67], v182, s[12:15], s20 offen offset:3072 sc1
s_lshl_b32 s20, s47, 9
s_lshl_b32 s21, s16, 7
s_add_u32 s20, s20, s21
s_add_u32 s20, s20, 101711872
v_and_b32 v200, 0xf0, v182
v_lshrrev_b32 v200, 2, v200
buffer_load_dword v193, v200, s[12:15], s20 offen sc1
buffer_load_dword v194, v200, s[12:15], s20 offen offset:64 sc1
v_log_f32 v201, v198
s_nop 0
v_add_f32 v201, v201, v187
v_log_f32 v202, v199
s_nop 0
v_add_f32 v202, v202, v188
s_waitcnt vmcnt(0)
v_max_f32 v203, v201, v193
v_sub_f32 v201, v201, v203
v_sub_f32 v193, v193, v203
EX 201,201
EX 193,193
s_nop 0
v_add_f32 v203, v201, v193
v_rcp_f32 v203, v203
s_nop 0
v_mul_f32 v193, v193, v203
v_mul_f32 v201, v201, v203
v_max_f32 v198, 0x2081cea, v198
v_rcp_f32 v198, v198
s_nop 0
v_mul_f32 v195, v201, v198
v_max_f32 v203, v202, v194
v_sub_f32 v202, v202, v203
v_sub_f32 v194, v194, v203
EX 202,202
EX 194,194
s_nop 0
v_add_f32 v203, v202, v194
v_rcp_f32 v203, v203
s_nop 0
v_mul_f32 v194, v194, v203
v_mul_f32 v202, v202, v203
v_max_f32 v199, 0x2081cea, v199
v_rcp_f32 v199, v199
s_nop 0
v_mul_f32 v196, v202, v199
s_lshl_b32 s20, s48, 3
s_lshl_b32 s21, s16, 1
s_add_u32 s20, s20, s21
s_lshl_b32 s20, s20, 17
s_lshl_b32 s21, s49, 13
s_add_u32 s20, s20, s21
s_add_u32 s20, s20, 50331648
AR 212,0
AR 213,1
AR 214,2
AR 215,3
AR 216,8
AR 217,9
AR 218,10
AR 219,11
v_mul_f32 v212, v212, v195
v_mul_f32 v213, v213, v195
v_mul_f32 v214, v214, v195
v_mul_f32 v215, v215, v195
v_mul_f32 v216, v216, v195
v_mul_f32 v217, v217, v195
v_mul_f32 v218, v218, v195
v_mul_f32 v219, v219, v195
v_lshlrev_b32 v198, 16, v4
v_and_b32 v199, 0xffff0000, v4
v_fmac_f32 v212, v198, v193
v_fmac_f32 v213, v199, v193
v_lshlrev_b32 v198, 16, v5
v_and_b32 v199, 0xffff0000, v5
v_fmac_f32 v214, v198, v193
v_fmac_f32 v215, v199, v193
v_lshlrev_b32 v198, 16, v6
v_and_b32 v199, 0xffff0000, v6
v_fmac_f32 v216, v198, v193
v_fmac_f32 v217, v199, v193
v_lshlrev_b32 v198, 16, v7
v_and_b32 v199, 0xffff0000, v7
v_fmac_f32 v218, v198, v193
v_fmac_f32 v219, v199, v193
CV 228,212,213
CV 229,214,215
CV 230,216,217
CV 231,218,219
buffer_store_dwordx4 v[228:231], v182, s[12:15], s20 offen sc1
AR 220,16
AR 221,17
AR 222,18
AR 223,19
AR 224,24
AR 225,25
AR 226,26
AR 227,27
v_mul_f32 v220, v220, v195
v_mul_f32 v221, v221, v195
v_mul_f32 v222, v222, v195
v_mul_f32 v223, v223, v195
v_mul_f32 v224, v224, v195
v_mul_f32 v225, v225, v195
v_mul_f32 v226, v226, v195
v_mul_f32 v227, v227, v195
v_lshlrev_b32 v198, 16, v8
v_and_b32 v199, 0xffff0000, v8
v_fmac_f32 v220, v198, v193
v_fmac_f32 v221, v199, v193
v_lshlrev_b32 v198, 16, v9
v_and_b32 v199, 0xffff0000, v9
v_fmac_f32 v222, v198, v193
v_fmac_f32 v223, v199, v193
v_lshlrev_b32 v198, 16, v10
v_and_b32 v199, 0xffff0000, v10
v_fmac_f32 v224, v198, v193
v_fmac_f32 v225, v199, v193
v_lshlrev_b32 v198, 16, v11
v_and_b32 v199, 0xffff0000, v11
v_fmac_f32 v226, v198, v193
v_fmac_f32 v227, v199, v193
CV 232,220,221
CV 233,222,223
CV 234,224,225
CV 235,226,227
buffer_store_dwordx4 v[232:235], v182, s[12:15], s20 offen offset:1024 sc1
AR 212,32
AR 213,33
AR 214,34
AR 215,35
AR 216,40
AR 217,41
AR 218,42
AR 219,43
v_mul_f32 v212, v212, v195
v_mul_f32 v213, v213, v195
v_mul_f32 v214, v214, v195
v_mul_f32 v215, v215, v195
v_mul_f32 v216, v216, v195
v_mul_f32 v217, v217, v195
v_mul_f32 v218, v218, v195
v_mul_f32 v219, v219, v195
v_lshlrev_b32 v198, 16, v12
v_and_b32 v199, 0xffff0000, v12
v_fmac_f32 v212, v198, v193
v_fmac_f32 v213, v199, v193
v_lshlrev_b32 v198, 16, v13
v_and_b32 v199, 0xffff0000, v13
v_fmac_f32 v214, v198, v193
v_fmac_f32 v215, v199, v193
v_lshlrev_b32 v198, 16, v14
v_and_b32 v199, 0xffff0000, v14
v_fmac_f32 v216, v198, v193
v_fmac_f32 v217, v199, v193
v_lshlrev_b32 v198, 16, v15
v_and_b32 v199, 0xffff0000, v15
v_fmac_f32 v218, v198, v193
v_fmac_f32 v219, v199, v193
CV 236,212,213
CV 237,214,215
CV 238,216,217
CV 239,218,219
buffer_store_dwordx4 v[236:239], v182, s[12:15], s20 offen offset:2048 sc1
AR 220,48
AR 221,49
AR 222,50
AR 223,51
AR 224,56
AR 225,57
AR 226,58
AR 227,59
v_mul_f32 v220, v220, v195
v_mul_f32 v221, v221, v195
v_mul_f32 v222, v222, v195
v_mul_f32 v223, v223, v195
v_mul_f32 v224, v224, v195
v_mul_f32 v225, v225, v195
v_mul_f32 v226, v226, v195
v_mul_f32 v227, v227, v195
v_lshlrev_b32 v198, 16, v16
v_and_b32 v199, 0xffff0000, v16
v_fmac_f32 v220, v198, v193
v_fmac_f32 v221, v199, v193
v_lshlrev_b32 v198, 16, v17
v_and_b32 v199, 0xffff0000, v17
v_fmac_f32 v222, v198, v193
v_fmac_f32 v223, v199, v193
v_lshlrev_b32 v198, 16, v18
v_and_b32 v199, 0xffff0000, v18
v_fmac_f32 v224, v198, v193
v_fmac_f32 v225, v199, v193
v_lshlrev_b32 v198, 16, v19
v_and_b32 v199, 0xffff0000, v19
v_fmac_f32 v226, v198, v193
v_fmac_f32 v227, v199, v193
CV 240,220,221
CV 241,222,223
CV 242,224,225
CV 243,226,227
buffer_store_dwordx4 v[240:243], v182, s[12:15], s20 offen offset:3072 sc1
s_add_u32 s20, s20, 4096
AR 212,64
AR 213,65
AR 214,66
AR 215,67
AR 216,72
AR 217,73
AR 218,74
AR 219,75
v_mul_f32 v212, v212, v195
v_mul_f32 v213, v213, v195
v_mul_f32 v214, v214, v195
v_mul_f32 v215, v215, v195
v_mul_f32 v216, v216, v195
v_mul_f32 v217, v217, v195
v_mul_f32 v218, v218, v195
v_mul_f32 v219, v219, v195
v_lshlrev_b32 v198, 16, v20
v_and_b32 v199, 0xffff0000, v20
v_fmac_f32 v212, v198, v193
v_fmac_f32 v213, v199, v193
v_lshlrev_b32 v198, 16, v21
v_and_b32 v199, 0xffff0000, v21
v_fmac_f32 v214, v198, v193
v_fmac_f32 v215, v199, v193
v_lshlrev_b32 v198, 16, v22
v_and_b32 v199, 0xffff0000, v22
v_fmac_f32 v216, v198, v193
v_fmac_f32 v217, v199, v193
v_lshlrev_b32 v198, 16, v23
v_and_b32 v199, 0xffff0000, v23
v_fmac_f32 v218, v198, v193
v_fmac_f32 v219, v199, v193
CV 228,212,213
CV 229,214,215
CV 230,216,217
CV 231,218,219
buffer_store_dwordx4 v[228:231], v182, s[12:15], s20 offen sc1
AR 220,80
AR 221,81
AR 222,82
AR 223,83
AR 224,88
AR 225,89
AR 226,90
AR 227,91
v_mul_f32 v220, v220, v195
v_mul_f32 v221, v221, v195
v_mul_f32 v222, v222, v195
v_mul_f32 v223, v223, v195
v_mul_f32 v224, v224, v195
v_mul_f32 v225, v225, v195
v_mul_f32 v226, v226, v195
v_mul_f32 v227, v227, v195
v_lshlrev_b32 v198, 16, v24
v_and_b32 v199, 0xffff0000, v24
v_fmac_f32 v220, v198, v193
v_fmac_f32 v221, v199, v193
v_lshlrev_b32 v198, 16, v25
v_and_b32 v199, 0xffff0000, v25
v_fmac_f32 v222, v198, v193
v_fmac_f32 v223, v199, v193
v_lshlrev_b32 v198, 16, v26
v_and_b32 v199, 0xffff0000, v26
v_fmac_f32 v224, v198, v193
v_fmac_f32 v225, v199, v193
v_lshlrev_b32 v198, 16, v27
v_and_b32 v199, 0xffff0000, v27
v_fmac_f32 v226, v198, v193
v_fmac_f32 v227, v199, v193
CV 232,220,221
CV 233,222,223
CV 234,224,225
CV 235,226,227
buffer_store_dwordx4 v[232:235], v182, s[12:15], s20 offen offset:1024 sc1
AR 212,96
AR 213,97
AR 214,98
AR 215,99
AR 216,104
AR 217,105
AR 218,106
AR 219,107
v_mul_f32 v212, v212, v195
v_mul_f32 v213, v213, v195
v_mul_f32 v214, v214, v195
v_mul_f32 v215, v215, v195
v_mul_f32 v216, v216, v195
v_mul_f32 v217, v217, v195
v_mul_f32 v218, v218, v195
v_mul_f32 v219, v219, v195
v_lshlrev_b32 v198, 16, v28
v_and_b32 v199, 0xffff0000, v28
v_fmac_f32 v212, v198, v193
v_fmac_f32 v213, v199, v193
v_lshlrev_b32 v198, 16, v29
v_and_b32 v199, 0xffff0000, v29
v_fmac_f32 v214, v198, v193
v_fmac_f32 v215, v199, v193
v_lshlrev_b32 v198, 16, v30
v_and_b32 v199, 0xffff0000, v30
v_fmac_f32 v216, v198, v193
v_fmac_f32 v217, v199, v193
v_lshlrev_b32 v198, 16, v31
v_and_b32 v199, 0xffff0000, v31
v_fmac_f32 v218, v198, v193
v_fmac_f32 v219, v199, v193
CV 236,212,213
CV 237,214,215
CV 238,216,217
CV 239,218,219
buffer_store_dwordx4 v[236:239], v182, s[12:15], s20 offen offset:2048 sc1
AR 220,112
AR 221,113
AR 222,114
AR 223,115
AR 224,120
AR 225,121
AR 226,122
AR 227,123
v_mul_f32 v220, v220, v195
v_mul_f32 v221, v221, v195
v_mul_f32 v222, v222, v195
v_mul_f32 v223, v223, v195
v_mul_f32 v224, v224, v195
v_mul_f32 v225, v225, v195
v_mul_f32 v226, v226, v195
v_mul_f32 v227, v227, v195
v_lshlrev_b32 v198, 16, v32
v_and_b32 v199, 0xffff0000, v32
v_fmac_f32 v220, v198, v193
v_fmac_f32 v221, v199, v193
v_lshlrev_b32 v198, 16, v33
v_and_b32 v199, 0xffff0000, v33
v_fmac_f32 v222, v198, v193
v_fmac_f32 v223, v199, v193
v_lshlrev_b32 v198, 16, v34
v_and_b32 v199, 0xffff0000, v34
v_fmac_f32 v224, v198, v193
v_fmac_f32 v225, v199, v193
v_lshlrev_b32 v198, 16, v35
v_and_b32 v199, 0xffff0000, v35
v_fmac_f32 v226, v198, v193
v_fmac_f32 v227, v199, v193
CV 240,220,221
CV 241,222,223
CV 242,224,225
CV 243,226,227
buffer_store_dwordx4 v[240:243], v182, s[12:15], s20 offen offset:3072 sc1
s_add_u32 s20, s20, 126976
AR 212,4
AR 213,5
AR 214,6
AR 215,7
AR 216,12
AR 217,13
AR 218,14
AR 219,15
v_mul_f32 v212, v212, v196
v_mul_f32 v213, v213, v196
v_mul_f32 v214, v214, v196
v_mul_f32 v215, v215, v196
v_mul_f32 v216, v216, v196
v_mul_f32 v217, v217, v196
v_mul_f32 v218, v218, v196
v_mul_f32 v219, v219, v196
v_lshlrev_b32 v198, 16, v36
v_and_b32 v199, 0xffff0000, v36
v_fmac_f32 v212, v198, v194
v_fmac_f32 v213, v199, v194
v_lshlrev_b32 v198, 16, v37
v_and_b32 v199, 0xffff0000, v37
v_fmac_f32 v214, v198, v194
v_fmac_f32 v215, v199, v194
v_lshlrev_b32 v198, 16, v38
v_and_b32 v199, 0xffff0000, v38
v_fmac_f32 v216, v198, v194
v_fmac_f32 v217, v199, v194
v_lshlrev_b32 v198, 16, v39
v_and_b32 v199, 0xffff0000, v39
v_fmac_f32 v218, v198, v194
v_fmac_f32 v219, v199, v194
CV 228,212,213
CV 229,214,215
CV 230,216,217
CV 231,218,219
buffer_store_dwordx4 v[228:231], v182, s[12:15], s20 offen sc1
AR 220,20
AR 221,21
AR 222,22
AR 223,23
AR 224,28
AR 225,29
AR 226,30
AR 227,31
v_mul_f32 v220, v220, v196
v_mul_f32 v221, v221, v196
v_mul_f32 v222, v222, v196
v_mul_f32 v223, v223, v196
v_mul_f32 v224, v224, v196
v_mul_f32 v225, v225, v196
v_mul_f32 v226, v226, v196
v_mul_f32 v227, v227, v196
v_lshlrev_b32 v198, 16, v40
v_and_b32 v199, 0xffff0000, v40
v_fmac_f32 v220, v198, v194
v_fmac_f32 v221, v199, v194
v_lshlrev_b32 v198, 16, v41
v_and_b32 v199, 0xffff0000, v41
v_fmac_f32 v222, v198, v194
v_fmac_f32 v223, v199, v194
v_lshlrev_b32 v198, 16, v42
v_and_b32 v199, 0xffff0000, v42
v_fmac_f32 v224, v198, v194
v_fmac_f32 v225, v199, v194
v_lshlrev_b32 v198, 16, v43
v_and_b32 v199, 0xffff0000, v43
v_fmac_f32 v226, v198, v194
v_fmac_f32 v227, v199, v194
CV 232,220,221
CV 233,222,223
CV 234,224,225
CV 235,226,227
buffer_store_dwordx4 v[232:235], v182, s[12:15], s20 offen offset:1024 sc1
AR 212,36
AR 213,37
AR 214,38
AR 215,39
AR 216,44
AR 217,45
AR 218,46
AR 219,47
v_mul_f32 v212, v212, v196
v_mul_f32 v213, v213, v196
v_mul_f32 v214, v214, v196
v_mul_f32 v215, v215, v196
v_mul_f32 v216, v216, v196
v_mul_f32 v217, v217, v196
v_mul_f32 v218, v218, v196
v_mul_f32 v219, v219, v196
v_lshlrev_b32 v198, 16, v44
v_and_b32 v199, 0xffff0000, v44
v_fmac_f32 v212, v198, v194
v_fmac_f32 v213, v199, v194
v_lshlrev_b32 v198, 16, v45
v_and_b32 v199, 0xffff0000, v45
v_fmac_f32 v214, v198, v194
v_fmac_f32 v215, v199, v194
v_lshlrev_b32 v198, 16, v46
v_and_b32 v199, 0xffff0000, v46
v_fmac_f32 v216, v198, v194
v_fmac_f32 v217, v199, v194
v_lshlrev_b32 v198, 16, v47
v_and_b32 v199, 0xffff0000, v47
v_fmac_f32 v218, v198, v194
v_fmac_f32 v219, v199, v194
CV 236,212,213
CV 237,214,215
CV 238,216,217
CV 239,218,219
buffer_store_dwordx4 v[236:239], v182, s[12:15], s20 offen offset:2048 sc1
AR 220,52
AR 221,53
AR 222,54
AR 223,55
AR 224,60
AR 225,61
AR 226,62
AR 227,63
v_mul_f32 v220, v220, v196
v_mul_f32 v221, v221, v196
v_mul_f32 v222, v222, v196
v_mul_f32 v223, v223, v196
v_mul_f32 v224, v224, v196
v_mul_f32 v225, v225, v196
v_mul_f32 v226, v226, v196
v_mul_f32 v227, v227, v196
v_lshlrev_b32 v198, 16, v48
v_and_b32 v199, 0xffff0000, v48
v_fmac_f32 v220, v198, v194
v_fmac_f32 v221, v199, v194
v_lshlrev_b32 v198, 16, v49
v_and_b32 v199, 0xffff0000, v49
v_fmac_f32 v222, v198, v194
v_fmac_f32 v223, v199, v194
v_lshlrev_b32 v198, 16, v50
v_and_b32 v199, 0xffff0000, v50
v_fmac_f32 v224, v198, v194
v_fmac_f32 v225, v199, v194
v_lshlrev_b32 v198, 16, v51
v_and_b32 v199, 0xffff0000, v51
v_fmac_f32 v226, v198, v194
v_fmac_f32 v227, v199, v194
CV 240,220,221
CV 241,222,223
CV 242,224,225
CV 243,226,227
buffer_store_dwordx4 v[240:243], v182, s[12:15], s20 offen offset:3072 sc1
s_add_u32 s20, s20, 4096
AR 212,68
AR 213,69
AR 214,70
AR 215,71
AR 216,76
AR 217,77
AR 218,78
AR 219,79
v_mul_f32 v212, v212, v196
v_mul_f32 v213, v213, v196
v_mul_f32 v214, v214, v196
v_mul_f32 v215, v215, v196
v_mul_f32 v216, v216, v196
v_mul_f32 v217, v217, v196
v_mul_f32 v218, v218, v196
v_mul_f32 v219, v219, v196
v_lshlrev_b32 v198, 16, v52
v_and_b32 v199, 0xffff0000, v52
v_fmac_f32 v212, v198, v194
v_fmac_f32 v213, v199, v194
v_lshlrev_b32 v198, 16, v53
v_and_b32 v199, 0xffff0000, v53
v_fmac_f32 v214, v198, v194
v_fmac_f32 v215, v199, v194
v_lshlrev_b32 v198, 16, v54
v_and_b32 v199, 0xffff0000, v54
v_fmac_f32 v216, v198, v194
v_fmac_f32 v217, v199, v194
v_lshlrev_b32 v198, 16, v55
v_and_b32 v199, 0xffff0000, v55
v_fmac_f32 v218, v198, v194
v_fmac_f32 v219, v199, v194
CV 228,212,213
CV 229,214,215
CV 230,216,217
CV 231,218,219
buffer_store_dwordx4 v[228:231], v182, s[12:15], s20 offen sc1
AR 220,84
AR 221,85
AR 222,86
AR 223,87
AR 224,92
AR 225,93
AR 226,94
AR 227,95
v_mul_f32 v220, v220, v196
v_mul_f32 v221, v221, v196
v_mul_f32 v222, v222, v196
v_mul_f32 v223, v223, v196
v_mul_f32 v224, v224, v196
v_mul_f32 v225, v225, v196
v_mul_f32 v226, v226, v196
v_mul_f32 v227, v227, v196
v_lshlrev_b32 v198, 16, v56
v_and_b32 v199, 0xffff0000, v56
v_fmac_f32 v220, v198, v194
v_fmac_f32 v221, v199, v194
v_lshlrev_b32 v198, 16, v57
v_and_b32 v199, 0xffff0000, v57
v_fmac_f32 v222, v198, v194
v_fmac_f32 v223, v199, v194
v_lshlrev_b32 v198, 16, v58
v_and_b32 v199, 0xffff0000, v58
v_fmac_f32 v224, v198, v194
v_fmac_f32 v225, v199, v194
v_lshlrev_b32 v198, 16, v59
v_and_b32 v199, 0xffff0000, v59
v_fmac_f32 v226, v198, v194
v_fmac_f32 v227, v199, v194
CV 232,220,221
CV 233,222,223
CV 234,224,225
CV 235,226,227
buffer_store_dwordx4 v[232:235], v182, s[12:15], s20 offen offset:1024 sc1
AR 212,100
AR 213,101
AR 214,102
AR 215,103
AR 216,108
AR 217,109
AR 218,110
AR 219,111
v_mul_f32 v212, v212, v196
v_mul_f32 v213, v213, v196
v_mul_f32 v214, v214, v196
v_mul_f32 v215, v215, v196
v_mul_f32 v216, v216, v196
v_mul_f32 v217, v217, v196
v_mul_f32 v218, v218, v196
v_mul_f32 v219, v219, v196
v_lshlrev_b32 v198, 16, v60
v_and_b32 v199, 0xffff0000, v60
v_fmac_f32 v212, v198, v194
v_fmac_f32 v213, v199, v194
v_lshlrev_b32 v198, 16, v61
v_and_b32 v199, 0xffff0000, v61
v_fmac_f32 v214, v198, v194
v_fmac_f32 v215, v199, v194
v_lshlrev_b32 v198, 16, v62
v_and_b32 v199, 0xffff0000, v62
v_fmac_f32 v216, v198, v194
v_fmac_f32 v217, v199, v194
v_lshlrev_b32 v198, 16, v63
v_and_b32 v199, 0xffff0000, v63
v_fmac_f32 v218, v198, v194
v_fmac_f32 v219, v199, v194
CV 236,212,213
CV 237,214,215
CV 238,216,217
CV 239,218,219
buffer_store_dwordx4 v[236:239], v182, s[12:15], s20 offen offset:2048 sc1
AR 220,116
AR 221,117
AR 222,118
AR 223,119
AR 224,124
AR 225,125
AR 226,126
AR 227,127
v_mul_f32 v220, v220, v196
v_mul_f32 v221, v221, v196
v_mul_f32 v222, v222, v196
v_mul_f32 v223, v223, v196
v_mul_f32 v224, v224, v196
v_mul_f32 v225, v225, v196
v_mul_f32 v226, v226, v196
v_mul_f32 v227, v227, v196
v_lshlrev_b32 v198, 16, v64
v_and_b32 v199, 0xffff0000, v64
v_fmac_f32 v220, v198, v194
v_fmac_f32 v221, v199, v194
v_lshlrev_b32 v198, 16, v65
v_and_b32 v199, 0xffff0000, v65
v_fmac_f32 v222, v198, v194
v_fmac_f32 v223, v199, v194
v_lshlrev_b32 v198, 16, v66
v_and_b32 v199, 0xffff0000, v66
v_fmac_f32 v224, v198, v194
v_fmac_f32 v225, v199, v194
v_lshlrev_b32 v198, 16, v67
v_and_b32 v199, 0xffff0000, v67
v_fmac_f32 v226, v198, v194
v_fmac_f32 v227, v199, v194
CV 240,220,221
CV 241,222,223
CV 242,224,225
CV 243,226,227
buffer_store_dwordx4 v[240:243], v182, s[12:15], s20 offen offset:3072 sc1
s_branch Lat_segnext
Lat_epP:
s_nop 7
AR 198,128
AR 199,132
v_log_f32 v201, v198
v_max_f32 v198, 0x2081cea, v198
v_add_f32 v201, v201, v187
v_rcp_f32 v195, v198
v_log_f32 v202, v199
v_max_f32 v199, 0x2081cea, v199
v_add_f32 v202, v202, v188
v_rcp_f32 v196, v199
s_lshl_b32 s20, s47, 9
s_lshl_b32 s21, s16, 7
s_add_u32 s20, s20, s21
s_add_u32 s20, s20, 101711872
v_and_b32 v200, 0x1f0, v182
v_lshrrev_b32 v200, 2, v200
v_cmp_gt_u32 vcc, 64, v200
s_nop 1
v_cndmask_b32 v201, v202, v201, vcc
buffer_store_dword v201, v200, s[12:15], s20 offen sc1
s_lshl_b32 s20, s47, 16
s_lshl_b32 s21, s16, 14
s_add_u32 s20, s20, s21
s_add_u32 s20, s20, 84934656
AR 4,0
AR 5,1
AR 6,2
AR 7,3
AR 8,8
AR 9,9
AR 10,10
AR 11,11
v_mul_f32 v4, v4, v195
v_mul_f32 v5, v5, v195
v_mul_f32 v6, v6, v195
v_mul_f32 v7, v7, v195
v_mul_f32 v8, v8, v195
v_mul_f32 v9, v9, v195
v_mul_f32 v10, v10, v195
v_mul_f32 v11, v11, v195
CV 36,4,5
CV 37,6,7
CV 38,8,9
CV 39,10,11
buffer_store_dwordx4 v[36:39], v182, s[12:15], s20 offen sc1
AR 12,16
AR 13,17
AR 14,18
AR 15,19
AR 16,24
AR 17,25
AR 18,26
AR 19,27
v_mul_f32 v12, v12, v195
v_mul_f32 v13, v13, v195
v_mul_f32 v14, v14, v195
v_mul_f32 v15, v15, v195
v_mul_f32 v16, v16, v195
v_mul_f32 v17, v17, v195
v_mul_f32 v18, v18, v195
v_mul_f32 v19, v19, v195
CV 40,12,13
CV 41,14,15
CV 42,16,17
CV 43,18,19
buffer_store_dwordx4 v[40:43], v182, s[12:15], s20 offen offset:1024 sc1
AR 20,32
AR 21,33
AR 22,34
AR 23,35
AR 24,40
AR 25,41
AR 26,42
AR 27,43
v_mul_f32 v20, v20, v195
v_mul_f32 v21, v21, v195
v_mul_f32 v22, v22, v195
v_mul_f32 v23, v23, v195
v_mul_f32 v24, v24, v195
v_mul_f32 v25, v25, v195
v_mul_f32 v26, v26, v195
v_mul_f32 v27, v27, v195
CV 44,20,21
CV 45,22,23
CV 46,24,25
CV 47,26,27
buffer_store_dwordx4 v[44:47], v182, s[12:15], s20 offen offset:2048 sc1
AR 28,48
AR 29,49
AR 30,50
AR 31,51
AR 32,56
AR 33,57
AR 34,58
AR 35,59
v_mul_f32 v28, v28, v195
v_mul_f32 v29, v29, v195
v_mul_f32 v30, v30, v195
v_mul_f32 v31, v31, v195
v_mul_f32 v32, v32, v195
v_mul_f32 v33, v33, v195
v_mul_f32 v34, v34, v195
v_mul_f32 v35, v35, v195
CV 48,28,29
CV 49,30,31
CV 50,32,33
CV 51,34,35
buffer_store_dwordx4 v[48:51], v182, s[12:15], s20 offen offset:3072 sc1
s_add_u32 s20, s20, 4096
AR 4,64
AR 5,65
AR 6,66
AR 7,67
AR 8,72
AR 9,73
AR 10,74
AR 11,75
v_mul_f32 v4, v4, v195
v_mul_f32 v5, v5, v195
v_mul_f32 v6, v6, v195
v_mul_f32 v7, v7, v195
v_mul_f32 v8, v8, v195
v_mul_f32 v9, v9, v195
v_mul_f32 v10, v10, v195
v_mul_f32 v11, v11, v195
CV 52,4,5
CV 53,6,7
CV 54,8,9
CV 55,10,11
buffer_store_dwordx4 v[52:55], v182, s[12:15], s20 offen sc1
AR 12,80
AR 13,81
AR 14,82
AR 15,83
AR 16,88
AR 17,89
AR 18,90
AR 19,91
v_mul_f32 v12, v12, v195
v_mul_f32 v13, v13, v195
v_mul_f32 v14, v14, v195
v_mul_f32 v15, v15, v195
v_mul_f32 v16, v16, v195
v_mul_f32 v17, v17, v195
v_mul_f32 v18, v18, v195
v_mul_f32 v19, v19, v195
CV 56,12,13
CV 57,14,15
CV 58,16,17
CV 59,18,19
buffer_store_dwordx4 v[56:59], v182, s[12:15], s20 offen offset:1024 sc1
AR 20,96
AR 21,97
AR 22,98
AR 23,99
AR 24,104
AR 25,105
AR 26,106
AR 27,107
v_mul_f32 v20, v20, v195
v_mul_f32 v21, v21, v195
v_mul_f32 v22, v22, v195
v_mul_f32 v23, v23, v195
v_mul_f32 v24, v24, v195
v_mul_f32 v25, v25, v195
v_mul_f32 v26, v26, v195
v_mul_f32 v27, v27, v195
CV 60,20,21
CV 61,22,23
CV 62,24,25
CV 63,26,27
buffer_store_dwordx4 v[60:63], v182, s[12:15], s20 offen offset:2048 sc1
AR 28,112
AR 29,113
AR 30,114
AR 31,115
AR 32,120
AR 33,121
AR 34,122
AR 35,123
v_mul_f32 v28, v28, v195
v_mul_f32 v29, v29, v195
v_mul_f32 v30, v30, v195
v_mul_f32 v31, v31, v195
v_mul_f32 v32, v32, v195
v_mul_f32 v33, v33, v195
v_mul_f32 v34, v34, v195
v_mul_f32 v35, v35, v195
CV 64,28,29
CV 65,30,31
CV 66,32,33
CV 67,34,35
buffer_store_dwordx4 v[64:67], v182, s[12:15], s20 offen offset:3072 sc1
s_add_u32 s20, s20, 4096
AR 4,4
AR 5,5
AR 6,6
AR 7,7
AR 8,12
AR 9,13
AR 10,14
AR 11,15
v_mul_f32 v4, v4, v196
v_mul_f32 v5, v5, v196
v_mul_f32 v6, v6, v196
v_mul_f32 v7, v7, v196
v_mul_f32 v8, v8, v196
v_mul_f32 v9, v9, v196
v_mul_f32 v10, v10, v196
v_mul_f32 v11, v11, v196
CV 36,4,5
CV 37,6,7
CV 38,8,9
CV 39,10,11
buffer_store_dwordx4 v[36:39], v182, s[12:15], s20 offen sc1
AR 12,20
AR 13,21
AR 14,22
AR 15,23
AR 16,28
AR 17,29
AR 18,30
AR 19,31
v_mul_f32 v12, v12, v196
v_mul_f32 v13, v13, v196
v_mul_f32 v14, v14, v196
v_mul_f32 v15, v15, v196
v_mul_f32 v16, v16, v196
v_mul_f32 v17, v17, v196
v_mul_f32 v18, v18, v196
v_mul_f32 v19, v19, v196
CV 40,12,13
CV 41,14,15
CV 42,16,17
CV 43,18,19
buffer_store_dwordx4 v[40:43], v182, s[12:15], s20 offen offset:1024 sc1
AR 20,36
AR 21,37
AR 22,38
AR 23,39
AR 24,44
AR 25,45
AR 26,46
AR 27,47
v_mul_f32 v20, v20, v196
v_mul_f32 v21, v21, v196
v_mul_f32 v22, v22, v196
v_mul_f32 v23, v23, v196
v_mul_f32 v24, v24, v196
v_mul_f32 v25, v25, v196
v_mul_f32 v26, v26, v196
v_mul_f32 v27, v27, v196
CV 44,20,21
CV 45,22,23
CV 46,24,25
CV 47,26,27
buffer_store_dwordx4 v[44:47], v182, s[12:15], s20 offen offset:2048 sc1
AR 28,52
AR 29,53
AR 30,54
AR 31,55
AR 32,60
AR 33,61
AR 34,62
AR 35,63
v_mul_f32 v28, v28, v196
v_mul_f32 v29, v29, v196
v_mul_f32 v30, v30, v196
v_mul_f32 v31, v31, v196
v_mul_f32 v32, v32, v196
v_mul_f32 v33, v33, v196
v_mul_f32 v34, v34, v196
v_mul_f32 v35, v35, v196
CV 48,28,29
CV 49,30,31
CV 50,32,33
CV 51,34,35
buffer_store_dwordx4 v[48:51], v182, s[12:15], s20 offen offset:3072 sc1
s_add_u32 s20, s20, 4096
AR 4,68
AR 5,69
AR 6,70
AR 7,71
AR 8,76
AR 9,77
AR 10,78
AR 11,79
v_mul_f32 v4, v4, v196
v_mul_f32 v5, v5, v196
v_mul_f32 v6, v6, v196
v_mul_f32 v7, v7, v196
v_mul_f32 v8, v8, v196
v_mul_f32 v9, v9, v196
v_mul_f32 v10, v10, v196
v_mul_f32 v11, v11, v196
CV 52,4,5
CV 53,6,7
CV 54,8,9
CV 55,10,11
buffer_store_dwordx4 v[52:55], v182, s[12:15], s20 offen sc1
AR 12,84
AR 13,85
AR 14,86
AR 15,87
AR 16,92
AR 17,93
AR 18,94
AR 19,95
v_mul_f32 v12, v12, v196
v_mul_f32 v13, v13, v196
v_mul_f32 v14, v14, v196
v_mul_f32 v15, v15, v196
v_mul_f32 v16, v16, v196
v_mul_f32 v17, v17, v196
v_mul_f32 v18, v18, v196
v_mul_f32 v19, v19, v196
CV 56,12,13
CV 57,14,15
CV 58,16,17
CV 59,18,19
buffer_store_dwordx4 v[56:59], v182, s[12:15], s20 offen offset:1024 sc1
AR 20,100
AR 21,101
AR 22,102
AR 23,103
AR 24,108
AR 25,109
AR 26,110
AR 27,111
v_mul_f32 v20, v20, v196
v_mul_f32 v21, v21, v196
v_mul_f32 v22, v22, v196
v_mul_f32 v23, v23, v196
v_mul_f32 v24, v24, v196
v_mul_f32 v25, v25, v196
v_mul_f32 v26, v26, v196
v_mul_f32 v27, v27, v196
CV 60,20,21
CV 61,22,23
CV 62,24,25
CV 63,26,27
buffer_store_dwordx4 v[60:63], v182, s[12:15], s20 offen offset:2048 sc1
AR 28,116
AR 29,117
AR 30,118
AR 31,119
AR 32,124
AR 33,125
AR 34,126
AR 35,127
v_mul_f32 v28, v28, v196
v_mul_f32 v29, v29, v196
v_mul_f32 v30, v30, v196
v_mul_f32 v31, v31, v196
v_mul_f32 v32, v32, v196
v_mul_f32 v33, v33, v196
v_mul_f32 v34, v34, v196
v_mul_f32 v35, v35, v196
CV 64,28,29
CV 65,30,31
CV 66,32,33
CV 67,34,35
buffer_store_dwordx4 v[64:67], v182, s[12:15], s20 offen offset:3072 sc1
s_lshl_b32 s22, s47, 8
s_add_u32 s22, s22, 125829120
s_mov_b32 s61, s22
Lat_segnext:
s_cmp_lg_u32 s34, 0
s_cbranch_scc1 Lat_segloop
s_waitcnt vmcnt(0)
s_mov_b64 exec, 1
buffer_atomic_add v208, v207, s[12:15], s61 offen
s_mov_b64 exec, -1
s_mov_b32 s61, s62
s_branch Lat_done
Lat_endpath:
s_mov_b32 s47, s59
s_mov_b32 s48, s56
s_mov_b32 s49, s57
s_mov_b32 s50, s58
s_mov_b32 s34, 0
s_lshl_b32 s63, s59, 8
s_add_u32 s63, s63, 125829120
s_waitcnt vmcnt(16)
s_barrier
s_waitcnt lgkmcnt(12)
MAavv 0,68,156
v_add_u32 v181, s38, v183
s_add_u32 s22, s51, 1
s_lshl_b32 s22, s22, 5
MAavv 8,72,156
buffer_load_dword v209, v207, s[12:15], s63 offen sc1
MAavv 16,76,156
v_cvt_f32_u32 v194, s22
v_sub_f32 v193, v191, v194
MAavv 24,80,156
v_sub_f32 v194, v192, v194
v_mul_f32 v193, 0x6c800000, v193
s_waitcnt lgkmcnt(8)
MAavv 32,84,156
v_mul_f32 v194, 0x6c800000, v194
MAavv 40,88,156
v_min_f32 v132, v132, v193
v_min_f32 v140, v140, v194
MAavv 48,92,156
v_add_f32 v199, 0xec800000, v193
v_min_f32 v133, v133, v199
MAavv 56,96,156
v_add_f32 v198, 0xed000000, v193
v_min_f32 v134, v134, v198
s_waitcnt lgkmcnt(4)
MAavv 64,100,156
v_add_f32 v199, 0xed400000, v193
v_min_f32 v135, v135, v199
MAavv 72,104,156
v_add_f32 v198, 0xee800000, v193
v_min_f32 v136, v136, v198
MAavv 80,108,156
v_add_f32 v199, 0xee880000, v193
v_min_f32 v137, v137, v199
MAavv 88,112,156
v_add_f32 v198, 0xee900000, v193
v_min_f32 v138, v138, v198
s_waitcnt lgkmcnt(0)
MAavv 96,116,156
v_add_f32 v199, 0xee980000, v193
v_min_f32 v139, v139, v199
MAavv 104,120,156
v_add_f32 v201, 0xec800000, v194
v_min_f32 v141, v141, v201
MAavv 112,124,156
v_add_f32 v200, 0xed000000, v194
v_min_f32 v142, v142, v200
MAavv 120,128,156
v_add_f32 v201, 0xed400000, v194
v_min_f32 v143, v143, v201
MAaav 128,200,156
v_add_f32 v200, 0xee800000, v194
v_min_f32 v144, v144, v200
MAavv 4,68,160
v_add_f32 v201, 0xee880000, v194
v_min_f32 v145, v145, v201
MAavv 12,72,160
v_add_f32 v200, 0xee900000, v194
v_min_f32 v146, v146, v200
MAavv 20,76,160
v_add_f32 v201, 0xee980000, v194
v_min_f32 v147, v147, v201
MAavv 28,80,160
EX 164,132
MAavv 36,84,160
EX 165,133
MAavv 44,88,160
EX 166,134
MAavv 52,92,160
EX 167,135
MAavv 60,96,160
EX 168,136
MAavv 68,100,160
EX 169,137
MAavv 76,104,160
EX 170,138
MAavv 84,108,160
EX 171,139
MAavv 92,112,160
EX 172,140
MAavv 100,116,160
EX 173,141
MAavv 108,120,160
EX 174,142
EX 175,143
EX 176,144
EX 177,145
EX 178,146
EX 179,147
MAavv 116,124,160
CV 156,164,165
CV 157,166,167
CV 158,168,169
CV 159,170,171
MAavv 124,128,160
v_or3_b32 v195, v156, v157, v158
v_or_b32 v195, v195, v159
v_and_b32 v195, 0x40004000, v195
)ASM"
R"ASM(v_cmp_ne_u32 vcc, 0, v195
MAaav 132,200,160
CV 160,172,173
CV 161,174,175
CV 162,176,177
CV 163,178,179
Rv 68,181,0
Rv 72,181,1024
Rv 76,181,2048
Rv 80,181,3072
Rv 84,181,4096
Rv 88,181,5120
Rv 92,181,6144
Rv 96,181,7168
Rv 100,181,8192
Rv 104,181,9216
Rv 108,181,10240
Rv 112,181,11264
Rv 116,181,12288
Rv 120,181,13312
Rv 124,181,14336
Rv 128,181,15360
s_cbranch_vccnz Lat_stub16
Lat_ret16:
v_or3_b32 v196, v160, v161, v162
v_or_b32 v196, v196, v163
v_and_b32 v196, 0x40004000, v196
v_cmp_ne_u32 vcc, 0, v196
s_nop 0
s_cbranch_vccnz Lat_stub17
Lat_ret17:
v_mov_b32 v187, v185
v_mov_b32 v188, v186
s_cmp_eq_u32 s50, 2
s_cbranch_scc0 Lat_nomerge
s_waitcnt vmcnt(0)
v_readfirstlane_b32 s20, v209
s_cmp_ge_u32 s20, 4
s_cbranch_scc1 Lat_mgot
s_lshl_b32 s22, s47, 8
s_add_u32 s22, s22, 125829120
v_mov_b32 v200, 0
s_mov_b32 s21, 0
Lat_poll19:
buffer_load_dword v201, v200, s[12:15], s22 offen sc1
s_waitcnt vmcnt(0)
v_readfirstlane_b32 s20, v201
s_cmp_ge_u32 s20, 4
s_cbranch_scc1 Lat_polldone19
s_add_u32 s21, s21, 1
s_cmp_lt_u32 s21, 1048576
s_sleep 2
s_cbranch_scc1 Lat_poll19
Lat_polldone19:
Lat_mgot:
s_barrier
s_lshl_b32 s20, s47, 16
s_lshl_b32 s21, s16, 14
s_add_u32 s20, s20, s21
s_add_u32 s20, s20, 84934656
buffer_load_dwordx4 v[4:7], v182, s[12:15], s20 offen sc1
buffer_load_dwordx4 v[8:11], v182, s[12:15], s20 offen offset:1024 sc1
buffer_load_dwordx4 v[12:15], v182, s[12:15], s20 offen offset:2048 sc1
buffer_load_dwordx4 v[16:19], v182, s[12:15], s20 offen offset:3072 sc1
s_add_u32 s20, s20, 4096
buffer_load_dwordx4 v[20:23], v182, s[12:15], s20 offen sc1
buffer_load_dwordx4 v[24:27], v182, s[12:15], s20 offen offset:1024 sc1
buffer_load_dwordx4 v[28:31], v182, s[12:15], s20 offen offset:2048 sc1
buffer_load_dwordx4 v[32:35], v182, s[12:15], s20 offen offset:3072 sc1
s_add_u32 s20, s20, 4096
buffer_load_dwordx4 v[36:39], v182, s[12:15], s20 offen sc1
buffer_load_dwordx4 v[40:43], v182, s[12:15], s20 offen offset:1024 sc1
buffer_load_dwordx4 v[44:47], v182, s[12:15], s20 offen offset:2048 sc1
buffer_load_dwordx4 v[48:51], v182, s[12:15], s20 offen offset:3072 sc1
s_add_u32 s20, s20, 4096
buffer_load_dwordx4 v[52:55], v182, s[12:15], s20 offen sc1
buffer_load_dwordx4 v[56:59], v182, s[12:15], s20 offen offset:1024 sc1
buffer_load_dwordx4 v[60:63], v182, s[12:15], s20 offen offset:2048 sc1
buffer_load_dwordx4 v[64:67], v182, s[12:15], s20 offen offset:3072 sc1
s_lshl_b32 s20, s47, 9
s_lshl_b32 s21, s16, 7
s_add_u32 s20, s20, s21
s_add_u32 s20, s20, 101711872
v_and_b32 v200, 0xf0, v182
v_lshrrev_b32 v200, 2, v200
buffer_load_dword v193, v200, s[12:15], s20 offen sc1
buffer_load_dword v194, v200, s[12:15], s20 offen offset:64 sc1
Lat_nomerge:
s_waitcnt lgkmcnt(12)
MAavv 0,68,156
MAavv 8,72,156
MAavv 16,76,156
MAavv 24,80,156
s_waitcnt lgkmcnt(8)
MAavv 32,84,156
MAavv 40,88,156
MAavv 48,92,156
MAavv 56,96,156
s_waitcnt lgkmcnt(4)
MAavv 64,100,156
MAavv 72,104,156
MAavv 80,108,156
MAavv 88,112,156
s_waitcnt lgkmcnt(0)
MAavv 96,116,156
MAavv 104,120,156
MAavv 112,124,156
MAavv 120,128,156
MAaav 128,200,156
MAavv 4,68,160
MAavv 12,72,160
MAavv 20,76,160
MAavv 28,80,160
MAavv 36,84,160
MAavv 44,88,160
MAavv 52,92,160
MAavv 60,96,160
MAavv 68,100,160
MAavv 76,104,160
MAavv 84,108,160
MAavv 92,112,160
MAavv 100,116,160
MAavv 108,120,160
MAavv 116,124,160
MAavv 124,128,160
MAaav 132,200,160
s_cmp_eq_u32 s50, 0
s_cbranch_scc1 Lat_epF
s_cmp_eq_u32 s50, 1
s_cbranch_scc1 Lat_epP
s_nop 7
AR 198,128
AR 199,132
v_log_f32 v201, v198
s_nop 0
v_add_f32 v201, v201, v187
v_log_f32 v202, v199
s_nop 0
v_add_f32 v202, v202, v188
s_waitcnt vmcnt(0)
v_max_f32 v203, v201, v193
v_sub_f32 v201, v201, v203
v_sub_f32 v193, v193, v203
EX 201,201
EX 193,193
s_nop 0
v_add_f32 v203, v201, v193
v_rcp_f32 v203, v203
s_nop 0
v_mul_f32 v193, v193, v203
v_mul_f32 v201, v201, v203
v_max_f32 v198, 0x2081cea, v198
v_rcp_f32 v198, v198
s_nop 0
v_mul_f32 v195, v201, v198
v_max_f32 v203, v202, v194
v_sub_f32 v202, v202, v203
v_sub_f32 v194, v194, v203
EX 202,202
EX 194,194
s_nop 0
v_add_f32 v203, v202, v194
v_rcp_f32 v203, v203
s_nop 0
v_mul_f32 v194, v194, v203
v_mul_f32 v202, v202, v203
v_max_f32 v199, 0x2081cea, v199
v_rcp_f32 v199, v199
s_nop 0
v_mul_f32 v196, v202, v199
s_lshl_b32 s20, s48, 3
s_lshl_b32 s21, s16, 1
s_add_u32 s20, s20, s21
s_lshl_b32 s20, s20, 17
s_lshl_b32 s21, s49, 13
s_add_u32 s20, s20, s21
s_add_u32 s20, s20, 50331648
AR 212,0
AR 213,1
AR 214,2
AR 215,3
AR 216,8
AR 217,9
AR 218,10
AR 219,11
v_mul_f32 v212, v212, v195
v_mul_f32 v213, v213, v195
v_mul_f32 v214, v214, v195
v_mul_f32 v215, v215, v195
v_mul_f32 v216, v216, v195
v_mul_f32 v217, v217, v195
v_mul_f32 v218, v218, v195
v_mul_f32 v219, v219, v195
v_lshlrev_b32 v198, 16, v4
v_and_b32 v199, 0xffff0000, v4
v_fmac_f32 v212, v198, v193
v_fmac_f32 v213, v199, v193
v_lshlrev_b32 v198, 16, v5
v_and_b32 v199, 0xffff0000, v5
v_fmac_f32 v214, v198, v193
v_fmac_f32 v215, v199, v193
v_lshlrev_b32 v198, 16, v6
v_and_b32 v199, 0xffff0000, v6
v_fmac_f32 v216, v198, v193
v_fmac_f32 v217, v199, v193
v_lshlrev_b32 v198, 16, v7
v_and_b32 v199, 0xffff0000, v7
v_fmac_f32 v218, v198, v193
v_fmac_f32 v219, v199, v193
CV 228,212,213
CV 229,214,215
CV 230,216,217
CV 231,218,219
buffer_store_dwordx4 v[228:231], v182, s[12:15], s20 offen sc1
AR 220,16
AR 221,17
AR 222,18
AR 223,19
AR 224,24
AR 225,25
AR 226,26
AR 227,27
v_mul_f32 v220, v220, v195
v_mul_f32 v221, v221, v195
v_mul_f32 v222, v222, v195
v_mul_f32 v223, v223, v195
v_mul_f32 v224, v224, v195
v_mul_f32 v225, v225, v195
v_mul_f32 v226, v226, v195
v_mul_f32 v227, v227, v195
v_lshlrev_b32 v198, 16, v8
v_and_b32 v199, 0xffff0000, v8
v_fmac_f32 v220, v198, v193
v_fmac_f32 v221, v199, v193
v_lshlrev_b32 v198, 16, v9
v_and_b32 v199, 0xffff0000, v9
v_fmac_f32 v222, v198, v193
v_fmac_f32 v223, v199, v193
v_lshlrev_b32 v198, 16, v10
v_and_b32 v199, 0xffff0000, v10
v_fmac_f32 v224, v198, v193
v_fmac_f32 v225, v199, v193
v_lshlrev_b32 v198, 16, v11
v_and_b32 v199, 0xffff0000, v11
v_fmac_f32 v226, v198, v193
v_fmac_f32 v227, v199, v193
CV 232,220,221
CV 233,222,223
CV 234,224,225
CV 235,226,227
buffer_store_dwordx4 v[232:235], v182, s[12:15], s20 offen offset:1024 sc1
AR 212,32
AR 213,33
AR 214,34
AR 215,35
AR 216,40
AR 217,41
AR 218,42
AR 219,43
v_mul_f32 v212, v212, v195
v_mul_f32 v213, v213, v195
v_mul_f32 v214, v214, v195
v_mul_f32 v215, v215, v195
v_mul_f32 v216, v216, v195
v_mul_f32 v217, v217, v195
v_mul_f32 v218, v218, v195
v_mul_f32 v219, v219, v195
v_lshlrev_b32 v198, 16, v12
v_and_b32 v199, 0xffff0000, v12
v_fmac_f32 v212, v198, v193
v_fmac_f32 v213, v199, v193
v_lshlrev_b32 v198, 16, v13
v_and_b32 v199, 0xffff0000, v13
v_fmac_f32 v214, v198, v193
v_fmac_f32 v215, v199, v193
v_lshlrev_b32 v198, 16, v14
v_and_b32 v199, 0xffff0000, v14
v_fmac_f32 v216, v198, v193
v_fmac_f32 v217, v199, v193
v_lshlrev_b32 v198, 16, v15
v_and_b32 v199, 0xffff0000, v15
v_fmac_f32 v218, v198, v193
v_fmac_f32 v219, v199, v193
CV 236,212,213
CV 237,214,215
CV 238,216,217
CV 239,218,219
buffer_store_dwordx4 v[236:239], v182, s[12:15], s20 offen offset:2048 sc1
AR 220,48
AR 221,49
AR 222,50
AR 223,51
AR 224,56
AR 225,57
AR 226,58
AR 227,59
v_mul_f32 v220, v220, v195
v_mul_f32 v221, v221, v195
v_mul_f32 v222, v222, v195
v_mul_f32 v223, v223, v195
v_mul_f32 v224, v224, v195
v_mul_f32 v225, v225, v195
v_mul_f32 v226, v226, v195
v_mul_f32 v227, v227, v195
v_lshlrev_b32 v198, 16, v16
v_and_b32 v199, 0xffff0000, v16
v_fmac_f32 v220, v198, v193
v_fmac_f32 v221, v199, v193
v_lshlrev_b32 v198, 16, v17
v_and_b32 v199, 0xffff0000, v17
v_fmac_f32 v222, v198, v193
v_fmac_f32 v223, v199, v193
v_lshlrev_b32 v198, 16, v18
v_and_b32 v199, 0xffff0000, v18
v_fmac_f32 v224, v198, v193
v_fmac_f32 v225, v199, v193
v_lshlrev_b32 v198, 16, v19
v_and_b32 v199, 0xffff0000, v19
v_fmac_f32 v226, v198, v193
v_fmac_f32 v227, v199, v193
CV 240,220,221
CV 241,222,223
CV 242,224,225
CV 243,226,227
buffer_store_dwordx4 v[240:243], v182, s[12:15], s20 offen offset:3072 sc1
s_add_u32 s20, s20, 4096
AR 212,64
AR 213,65
AR 214,66
AR 215,67
AR 216,72
AR 217,73
AR 218,74
AR 219,75
v_mul_f32 v212, v212, v195
v_mul_f32 v213, v213, v195
v_mul_f32 v214, v214, v195
v_mul_f32 v215, v215, v195
v_mul_f32 v216, v216, v195
v_mul_f32 v217, v217, v195
v_mul_f32 v218, v218, v195
v_mul_f32 v219, v219, v195
v_lshlrev_b32 v198, 16, v20
v_and_b32 v199, 0xffff0000, v20
v_fmac_f32 v212, v198, v193
v_fmac_f32 v213, v199, v193
v_lshlrev_b32 v198, 16, v21
v_and_b32 v199, 0xffff0000, v21
v_fmac_f32 v214, v198, v193
v_fmac_f32 v215, v199, v193
v_lshlrev_b32 v198, 16, v22
v_and_b32 v199, 0xffff0000, v22
v_fmac_f32 v216, v198, v193
v_fmac_f32 v217, v199, v193
v_lshlrev_b32 v198, 16, v23
v_and_b32 v199, 0xffff0000, v23
v_fmac_f32 v218, v198, v193
v_fmac_f32 v219, v199, v193
CV 228,212,213
CV 229,214,215
CV 230,216,217
CV 231,218,219
buffer_store_dwordx4 v[228:231], v182, s[12:15], s20 offen sc1
AR 220,80
AR 221,81
AR 222,82
AR 223,83
AR 224,88
AR 225,89
AR 226,90
AR 227,91
v_mul_f32 v220, v220, v195
v_mul_f32 v221, v221, v195
v_mul_f32 v222, v222, v195
v_mul_f32 v223, v223, v195
v_mul_f32 v224, v224, v195
v_mul_f32 v225, v225, v195
v_mul_f32 v226, v226, v195
v_mul_f32 v227, v227, v195
v_lshlrev_b32 v198, 16, v24
v_and_b32 v199, 0xffff0000, v24
v_fmac_f32 v220, v198, v193
v_fmac_f32 v221, v199, v193
v_lshlrev_b32 v198, 16, v25
v_and_b32 v199, 0xffff0000, v25
v_fmac_f32 v222, v198, v193
v_fmac_f32 v223, v199, v193
v_lshlrev_b32 v198, 16, v26
v_and_b32 v199, 0xffff0000, v26
v_fmac_f32 v224, v198, v193
v_fmac_f32 v225, v199, v193
v_lshlrev_b32 v198, 16, v27
v_and_b32 v199, 0xffff0000, v27
v_fmac_f32 v226, v198, v193
v_fmac_f32 v227, v199, v193
CV 232,220,221
CV 233,222,223
CV 234,224,225
CV 235,226,227
buffer_store_dwordx4 v[232:235], v182, s[12:15], s20 offen offset:1024 sc1
AR 212,96
AR 213,97
AR 214,98
AR 215,99
AR 216,104
AR 217,105
AR 218,106
AR 219,107
v_mul_f32 v212, v212, v195
v_mul_f32 v213, v213, v195
v_mul_f32 v214, v214, v195
v_mul_f32 v215, v215, v195
v_mul_f32 v216, v216, v195
v_mul_f32 v217, v217, v195
v_mul_f32 v218, v218, v195
v_mul_f32 v219, v219, v195
v_lshlrev_b32 v198, 16, v28
v_and_b32 v199, 0xffff0000, v28
v_fmac_f32 v212, v198, v193
v_fmac_f32 v213, v199, v193
v_lshlrev_b32 v198, 16, v29
v_and_b32 v199, 0xffff0000, v29
v_fmac_f32 v214, v198, v193
v_fmac_f32 v215, v199, v193
v_lshlrev_b32 v198, 16, v30
v_and_b32 v199, 0xffff0000, v30
v_fmac_f32 v216, v198, v193
v_fmac_f32 v217, v199, v193
v_lshlrev_b32 v198, 16, v31
v_and_b32 v199, 0xffff0000, v31
v_fmac_f32 v218, v198, v193
v_fmac_f32 v219, v199, v193
CV 236,212,213
CV 237,214,215
CV 238,216,217
CV 239,218,219
buffer_store_dwordx4 v[236:239], v182, s[12:15], s20 offen offset:2048 sc1
AR 220,112
AR 221,113
AR 222,114
AR 223,115
AR 224,120
AR 225,121
AR 226,122
AR 227,123
v_mul_f32 v220, v220, v195
v_mul_f32 v221, v221, v195
v_mul_f32 v222, v222, v195
v_mul_f32 v223, v223, v195
v_mul_f32 v224, v224, v195
v_mul_f32 v225, v225, v195
v_mul_f32 v226, v226, v195
v_mul_f32 v227, v227, v195
v_lshlrev_b32 v198, 16, v32
v_and_b32 v199, 0xffff0000, v32
v_fmac_f32 v220, v198, v193
v_fmac_f32 v221, v199, v193
v_lshlrev_b32 v198, 16, v33
v_and_b32 v199, 0xffff0000, v33
v_fmac_f32 v222, v198, v193
v_fmac_f32 v223, v199, v193
v_lshlrev_b32 v198, 16, v34
v_and_b32 v199, 0xffff0000, v34
v_fmac_f32 v224, v198, v193
v_fmac_f32 v225, v199, v193
v_lshlrev_b32 v198, 16, v35
v_and_b32 v199, 0xffff0000, v35
v_fmac_f32 v226, v198, v193
v_fmac_f32 v227, v199, v193
CV 240,220,221
CV 241,222,223
CV 242,224,225
CV 243,226,227
buffer_store_dwordx4 v[240:243], v182, s[12:15], s20 offen offset:3072 sc1
s_add_u32 s20, s20, 126976
AR 212,4
AR 213,5
AR 214,6
AR 215,7
AR 216,12
AR 217,13
AR 218,14
AR 219,15
v_mul_f32 v212, v212, v196
v_mul_f32 v213, v213, v196
v_mul_f32 v214, v214, v196
v_mul_f32 v215, v215, v196
v_mul_f32 v216, v216, v196
v_mul_f32 v217, v217, v196
v_mul_f32 v218, v218, v196
v_mul_f32 v219, v219, v196
v_lshlrev_b32 v198, 16, v36
v_and_b32 v199, 0xffff0000, v36
v_fmac_f32 v212, v198, v194
v_fmac_f32 v213, v199, v194
v_lshlrev_b32 v198, 16, v37
v_and_b32 v199, 0xffff0000, v37
v_fmac_f32 v214, v198, v194
v_fmac_f32 v215, v199, v194
v_lshlrev_b32 v198, 16, v38
v_and_b32 v199, 0xffff0000, v38
v_fmac_f32 v216, v198, v194
v_fmac_f32 v217, v199, v194
v_lshlrev_b32 v198, 16, v39
v_and_b32 v199, 0xffff0000, v39
v_fmac_f32 v218, v198, v194
v_fmac_f32 v219, v199, v194
CV 228,212,213
CV 229,214,215
CV 230,216,217
CV 231,218,219
buffer_store_dwordx4 v[228:231], v182, s[12:15], s20 offen sc1
AR 220,20
AR 221,21
AR 222,22
AR 223,23
AR 224,28
AR 225,29
AR 226,30
AR 227,31
v_mul_f32 v220, v220, v196
v_mul_f32 v221, v221, v196
v_mul_f32 v222, v222, v196
v_mul_f32 v223, v223, v196
v_mul_f32 v224, v224, v196
v_mul_f32 v225, v225, v196
v_mul_f32 v226, v226, v196
v_mul_f32 v227, v227, v196
v_lshlrev_b32 v198, 16, v40
v_and_b32 v199, 0xffff0000, v40
v_fmac_f32 v220, v198, v194
v_fmac_f32 v221, v199, v194
v_lshlrev_b32 v198, 16, v41
v_and_b32 v199, 0xffff0000, v41
v_fmac_f32 v222, v198, v194
v_fmac_f32 v223, v199, v194
v_lshlrev_b32 v198, 16, v42
v_and_b32 v199, 0xffff0000, v42
v_fmac_f32 v224, v198, v194
v_fmac_f32 v225, v199, v194
v_lshlrev_b32 v198, 16, v43
v_and_b32 v199, 0xffff0000, v43
v_fmac_f32 v226, v198, v194
v_fmac_f32 v227, v199, v194
CV 232,220,221
CV 233,222,223
CV 234,224,225
CV 235,226,227
buffer_store_dwordx4 v[232:235], v182, s[12:15], s20 offen offset:1024 sc1
AR 212,36
AR 213,37
AR 214,38
AR 215,39
AR 216,44
AR 217,45
AR 218,46
AR 219,47
v_mul_f32 v212, v212, v196
v_mul_f32 v213, v213, v196
v_mul_f32 v214, v214, v196
v_mul_f32 v215, v215, v196
v_mul_f32 v216, v216, v196
v_mul_f32 v217, v217, v196
v_mul_f32 v218, v218, v196
v_mul_f32 v219, v219, v196
v_lshlrev_b32 v198, 16, v44
v_and_b32 v199, 0xffff0000, v44
v_fmac_f32 v212, v198, v194
v_fmac_f32 v213, v199, v194
v_lshlrev_b32 v198, 16, v45
v_and_b32 v199, 0xffff0000, v45
v_fmac_f32 v214, v198, v194
v_fmac_f32 v215, v199, v194
v_lshlrev_b32 v198, 16, v46
v_and_b32 v199, 0xffff0000, v46
v_fmac_f32 v216, v198, v194
v_fmac_f32 v217, v199, v194
v_lshlrev_b32 v198, 16, v47
v_and_b32 v199, 0xffff0000, v47
v_fmac_f32 v218, v198, v194
v_fmac_f32 v219, v199, v194
CV 236,212,213
CV 237,214,215
CV 238,216,217
CV 239,218,219
buffer_store_dwordx4 v[236:239], v182, s[12:15], s20 offen offset:2048 sc1
AR 220,52
AR 221,53
AR 222,54
AR 223,55
AR 224,60
AR 225,61
AR 226,62
AR 227,63
v_mul_f32 v220, v220, v196
v_mul_f32 v221, v221, v196
v_mul_f32 v222, v222, v196
v_mul_f32 v223, v223, v196
v_mul_f32 v224, v224, v196
v_mul_f32 v225, v225, v196
v_mul_f32 v226, v226, v196
v_mul_f32 v227, v227, v196
v_lshlrev_b32 v198, 16, v48
v_and_b32 v199, 0xffff0000, v48
v_fmac_f32 v220, v198, v194
v_fmac_f32 v221, v199, v194
v_lshlrev_b32 v198, 16, v49
v_and_b32 v199, 0xffff0000, v49
v_fmac_f32 v222, v198, v194
v_fmac_f32 v223, v199, v194
v_lshlrev_b32 v198, 16, v50
v_and_b32 v199, 0xffff0000, v50
v_fmac_f32 v224, v198, v194
v_fmac_f32 v225, v199, v194
v_lshlrev_b32 v198, 16, v51
v_and_b32 v199, 0xffff0000, v51
v_fmac_f32 v226, v198, v194
v_fmac_f32 v227, v199, v194
CV 240,220,221
CV 241,222,223
CV 242,224,225
CV 243,226,227
buffer_store_dwordx4 v[240:243], v182, s[12:15], s20 offen offset:3072 sc1
s_add_u32 s20, s20, 4096
AR 212,68
AR 213,69
AR 214,70
AR 215,71
AR 216,76
AR 217,77
AR 218,78
AR 219,79
v_mul_f32 v212, v212, v196
v_mul_f32 v213, v213, v196
v_mul_f32 v214, v214, v196
v_mul_f32 v215, v215, v196
v_mul_f32 v216, v216, v196
v_mul_f32 v217, v217, v196
v_mul_f32 v218, v218, v196
v_mul_f32 v219, v219, v196
v_lshlrev_b32 v198, 16, v52
v_and_b32 v199, 0xffff0000, v52
v_fmac_f32 v212, v198, v194
v_fmac_f32 v213, v199, v194
v_lshlrev_b32 v198, 16, v53
v_and_b32 v199, 0xffff0000, v53
v_fmac_f32 v214, v198, v194
v_fmac_f32 v215, v199, v194
v_lshlrev_b32 v198, 16, v54
v_and_b32 v199, 0xffff0000, v54
v_fmac_f32 v216, v198, v194
v_fmac_f32 v217, v199, v194
v_lshlrev_b32 v198, 16, v55
v_and_b32 v199, 0xffff0000, v55
v_fmac_f32 v218, v198, v194
v_fmac_f32 v219, v199, v194
CV 228,212,213
CV 229,214,215
CV 230,216,217
CV 231,218,219
buffer_store_dwordx4 v[228:231], v182, s[12:15], s20 offen sc1
AR 220,84
AR 221,85
AR 222,86
AR 223,87
AR 224,92
AR 225,93
AR 226,94
AR 227,95
v_mul_f32 v220, v220, v196
v_mul_f32 v221, v221, v196
v_mul_f32 v222, v222, v196
v_mul_f32 v223, v223, v196
v_mul_f32 v224, v224, v196
v_mul_f32 v225, v225, v196
v_mul_f32 v226, v226, v196
v_mul_f32 v227, v227, v196
v_lshlrev_b32 v198, 16, v56
v_and_b32 v199, 0xffff0000, v56
v_fmac_f32 v220, v198, v194
v_fmac_f32 v221, v199, v194
v_lshlrev_b32 v198, 16, v57
v_and_b32 v199, 0xffff0000, v57
v_fmac_f32 v222, v198, v194
v_fmac_f32 v223, v199, v194
v_lshlrev_b32 v198, 16, v58
v_and_b32 v199, 0xffff0000, v58
v_fmac_f32 v224, v198, v194
v_fmac_f32 v225, v199, v194
v_lshlrev_b32 v198, 16, v59
v_and_b32 v199, 0xffff0000, v59
v_fmac_f32 v226, v198, v194
v_fmac_f32 v227, v199, v194
CV 232,220,221
CV 233,222,223
CV 234,224,225
CV 235,226,227
buffer_store_dwordx4 v[232:235], v182, s[12:15], s20 offen offset:1024 sc1
AR 212,100
AR 213,101
AR 214,102
AR 215,103
AR 216,108
AR 217,109
AR 218,110
AR 219,111
v_mul_f32 v212, v212, v196
v_mul_f32 v213, v213, v196
v_mul_f32 v214, v214, v196
v_mul_f32 v215, v215, v196
v_mul_f32 v216, v216, v196
v_mul_f32 v217, v217, v196
v_mul_f32 v218, v218, v196
v_mul_f32 v219, v219, v196
v_lshlrev_b32 v198, 16, v60
v_and_b32 v199, 0xffff0000, v60
v_fmac_f32 v212, v198, v194
v_fmac_f32 v213, v199, v194
v_lshlrev_b32 v198, 16, v61
v_and_b32 v199, 0xffff0000, v61
v_fmac_f32 v214, v198, v194
v_fmac_f32 v215, v199, v194
v_lshlrev_b32 v198, 16, v62
v_and_b32 v199, 0xffff0000, v62
v_fmac_f32 v216, v198, v194
v_fmac_f32 v217, v199, v194
v_lshlrev_b32 v198, 16, v63
v_and_b32 v199, 0xffff0000, v63
v_fmac_f32 v218, v198, v194
v_fmac_f32 v219, v199, v194
CV 236,212,213
CV 237,214,215
CV 238,216,217
CV 239,218,219
buffer_store_dwordx4 v[236:239], v182, s[12:15], s20 offen offset:2048 sc1
AR 220,116
AR 221,117
AR 222,118
AR 223,119
AR 224,124
AR 225,125
AR 226,126
AR 227,127
v_mul_f32 v220, v220, v196
v_mul_f32 v221, v221, v196
v_mul_f32 v222, v222, v196
v_mul_f32 v223, v223, v196
v_mul_f32 v224, v224, v196
v_mul_f32 v225, v225, v196
v_mul_f32 v226, v226, v196
v_mul_f32 v227, v227, v196
v_lshlrev_b32 v198, 16, v64
v_and_b32 v199, 0xffff0000, v64
v_fmac_f32 v220, v198, v194
v_fmac_f32 v221, v199, v194
v_lshlrev_b32 v198, 16, v65
v_and_b32 v199, 0xffff0000, v65
v_fmac_f32 v222, v198, v194
v_fmac_f32 v223, v199, v194
v_lshlrev_b32 v198, 16, v66
v_and_b32 v199, 0xffff0000, v66
v_fmac_f32 v224, v198, v194
v_fmac_f32 v225, v199, v194
v_lshlrev_b32 v198, 16, v67
v_and_b32 v199, 0xffff0000, v67
v_fmac_f32 v226, v198, v194
v_fmac_f32 v227, v199, v194
CV 240,220,221
CV 241,222,223
CV 242,224,225
CV 243,226,227
buffer_store_dwordx4 v[240:243], v182, s[12:15], s20 offen offset:3072 sc1
s_branch Lat_segnext
Lat_stub1:
s_getpc_b64 s[64:65]
s_branch Lat_rare0
s_branch Lat_ret1
Lat_stub2:
s_getpc_b64 s[64:65]
s_branch Lat_rare1
s_branch Lat_ret2
Lat_stub3:
s_getpc_b64 s[64:65]
s_branch Lat_rare0
s_branch Lat_ret3
Lat_stub4:
s_getpc_b64 s[64:65]
s_branch Lat_rare1
s_branch Lat_ret4
Lat_stub5:
s_getpc_b64 s[64:65]
s_branch Lat_rare0
s_branch Lat_ret5
Lat_stub6:
s_getpc_b64 s[64:65]
s_branch Lat_rare1
s_branch Lat_ret6
Lat_stub7:
s_getpc_b64 s[64:65]
s_branch Lat_rare0
s_branch Lat_ret7
Lat_stub8:
s_getpc_b64 s[64:65]
s_branch Lat_rare1
s_branch Lat_ret8
Lat_stub9:
s_getpc_b64 s[64:65]
s_branch Lat_rare0
s_branch Lat_ret9
Lat_stub10:
s_getpc_b64 s[64:65]
s_branch Lat_rare1
s_branch Lat_ret10
Lat_stub11:
s_getpc_b64 s[64:65]
s_branch Lat_rare0
s_branch Lat_ret11
Lat_stub12:
s_getpc_b64 s[64:65]
s_branch Lat_rare1
s_branch Lat_ret12
Lat_stub13:
s_getpc_b64 s[64:65]
s_branch Lat_rare0
s_branch Lat_ret13
Lat_stub14:
s_getpc_b64 s[64:65]
s_branch Lat_rare1
s_branch Lat_ret14
Lat_stub16:
s_getpc_b64 s[64:65]
s_branch Lat_rare0
s_branch Lat_ret16
Lat_stub17:
s_getpc_b64 s[64:65]
s_branch Lat_rare1
s_branch Lat_ret17
Lat_rare0:
s_nop 7
v_max3_f32 v204, v132, v133, v134
v_max3_f32 v204, v204, v135, v136
v_max3_f32 v204, v204, v137, v138
v_max_f32 v204, v204, v139
v_mov_b32 v202, v204
v_mov_b32 v203, v204
s_nop 1
v_permlane16_swap_b32 v202, v203
s_nop 1
v_max_f32 v204, v202, v203
v_mov_b32 v202, v204
v_mov_b32 v203, v204
s_nop 1
v_permlane32_swap_b32 v202, v203
s_nop 1
v_max_f32 v204, v202, v203
v_add_f32 v204, 0x41c00000, v204
v_max_f32 v204, 0, v204
v_sub_f32 v205, 0, v204
EX 205,205
v_add_f32 v185, v185, v204
v_sub_f32 v148, 0, v185
v_sub_f32 v149, 0, v185
v_sub_f32 v150, 0, v185
v_sub_f32 v151, 0, v185
v_sub_f32 v132, v132, v204
v_sub_f32 v133, v133, v204
v_sub_f32 v134, v134, v204
v_sub_f32 v135, v135, v204
v_sub_f32 v136, v136, v204
v_sub_f32 v137, v137, v204
v_sub_f32 v138, v138, v204
v_sub_f32 v139, v139, v204
AR 198,0
AR 199,1
AR 200,2
AR 201,3
v_mul_f32 v198, v198, v205
v_mul_f32 v199, v199, v205
v_mul_f32 v200, v200, v205
v_mul_f32 v201, v201, v205
v_accvgpr_write_b32 a0, v198
v_accvgpr_write_b32 a1, v199
v_accvgpr_write_b32 a2, v200
v_accvgpr_write_b32 a3, v201
AR 198,8
AR 199,9
AR 200,10
AR 201,11
v_mul_f32 v198, v198, v205
v_mul_f32 v199, v199, v205
v_mul_f32 v200, v200, v205
v_mul_f32 v201, v201, v205
v_accvgpr_write_b32 a8, v198
v_accvgpr_write_b32 a9, v199
v_accvgpr_write_b32 a10, v200
v_accvgpr_write_b32 a11, v201
AR 198,16
AR 199,17
AR 200,18
AR 201,19
v_mul_f32 v198, v198, v205
v_mul_f32 v199, v199, v205
v_mul_f32 v200, v200, v205
v_mul_f32 v201, v201, v205
v_accvgpr_write_b32 a16, v198
v_accvgpr_write_b32 a17, v199
v_accvgpr_write_b32 a18, v200
v_accvgpr_write_b32 a19, v201
AR 198,24
AR 199,25
AR 200,26
AR 201,27
v_mul_f32 v198, v198, v205
v_mul_f32 v199, v199, v205
v_mul_f32 v200, v200, v205
v_mul_f32 v201, v201, v205
v_accvgpr_write_b32 a24, v198
v_accvgpr_write_b32 a25, v199
v_accvgpr_write_b32 a26, v200
v_accvgpr_write_b32 a27, v201
AR 198,32
AR 199,33
AR 200,34
AR 201,35
v_mul_f32 v198, v198, v205
v_mul_f32 v199, v199, v205
v_mul_f32 v200, v200, v205
v_mul_f32 v201, v201, v205
v_accvgpr_write_b32 a32, v198
v_accvgpr_write_b32 a33, v199
v_accvgpr_write_b32 a34, v200
v_accvgpr_write_b32 a35, v201
AR 198,40
AR 199,41
AR 200,42
AR 201,43
v_mul_f32 v198, v198, v205
v_mul_f32 v199, v199, v205
v_mul_f32 v200, v200, v205
v_mul_f32 v201, v201, v205
v_accvgpr_write_b32 a40, v198
v_accvgpr_write_b32 a41, v199
v_accvgpr_write_b32 a42, v200
v_accvgpr_write_b32 a43, v201
AR 198,48
AR 199,49
AR 200,50
AR 201,51
v_mul_f32 v198, v198, v205
v_mul_f32 v199, v199, v205
v_mul_f32 v200, v200, v205
v_mul_f32 v201, v201, v205
v_accvgpr_write_b32 a48, v198
v_accvgpr_write_b32 a49, v199
v_accvgpr_write_b32 a50, v200
v_accvgpr_write_b32 a51, v201
AR 198,56
AR 199,57
AR 200,58
AR 201,59
v_mul_f32 v198, v198, v205
v_mul_f32 v199, v199, v205
v_mul_f32 v200, v200, v205
v_mul_f32 v201, v201, v205
v_accvgpr_write_b32 a56, v198
v_accvgpr_write_b32 a57, v199
v_accvgpr_write_b32 a58, v200
v_accvgpr_write_b32 a59, v201
AR 198,64
AR 199,65
AR 200,66
AR 201,67
v_mul_f32 v198, v198, v205
v_mul_f32 v199, v199, v205
v_mul_f32 v200, v200, v205
v_mul_f32 v201, v201, v205
v_accvgpr_write_b32 a64, v198
v_accvgpr_write_b32 a65, v199
v_accvgpr_write_b32 a66, v200
v_accvgpr_write_b32 a67, v201
AR 198,72
AR 199,73
AR 200,74
AR 201,75
v_mul_f32 v198, v198, v205
v_mul_f32 v199, v199, v205
v_mul_f32 v200, v200, v205
v_mul_f32 v201, v201, v205
v_accvgpr_write_b32 a72, v198
v_accvgpr_write_b32 a73, v199
v_accvgpr_write_b32 a74, v200
v_accvgpr_write_b32 a75, v201
AR 198,80
AR 199,81
AR 200,82
AR 201,83
v_mul_f32 v198, v198, v205
v_mul_f32 v199, v199, v205
v_mul_f32 v200, v200, v205
v_mul_f32 v201, v201, v205
v_accvgpr_write_b32 a80, v198
v_accvgpr_write_b32 a81, v199
v_accvgpr_write_b32 a82, v200
v_accvgpr_write_b32 a83, v201
AR 198,88
AR 199,89
AR 200,90
AR 201,91
v_mul_f32 v198, v198, v205
v_mul_f32 v199, v199, v205
v_mul_f32 v200, v200, v205
v_mul_f32 v201, v201, v205
v_accvgpr_write_b32 a88, v198
v_accvgpr_write_b32 a89, v199
v_accvgpr_write_b32 a90, v200
v_accvgpr_write_b32 a91, v201
AR 198,96
AR 199,97
AR 200,98
AR 201,99
v_mul_f32 v198, v198, v205
v_mul_f32 v199, v199, v205
v_mul_f32 v200, v200, v205
v_mul_f32 v201, v201, v205
v_accvgpr_write_b32 a96, v198
v_accvgpr_write_b32 a97, v199
v_accvgpr_write_b32 a98, v200
v_accvgpr_write_b32 a99, v201
AR 198,104
AR 199,105
AR 200,106
AR 201,107
v_mul_f32 v198, v198, v205
v_mul_f32 v199, v199, v205
v_mul_f32 v200, v200, v205
v_mul_f32 v201, v201, v205
v_accvgpr_write_b32 a104, v198
v_accvgpr_write_b32 a105, v199
v_accvgpr_write_b32 a106, v200
v_accvgpr_write_b32 a107, v201
AR 198,112
AR 199,113
AR 200,114
AR 201,115
v_mul_f32 v198, v198, v205
v_mul_f32 v199, v199, v205
v_mul_f32 v200, v200, v205
v_mul_f32 v201, v201, v205
v_accvgpr_write_b32 a112, v198
v_accvgpr_write_b32 a113, v199
v_accvgpr_write_b32 a114, v200
v_accvgpr_write_b32 a115, v201
AR 198,120
AR 199,121
AR 200,122
AR 201,123
v_mul_f32 v198, v198, v205
v_mul_f32 v199, v199, v205
v_mul_f32 v200, v200, v205
v_mul_f32 v201, v201, v205
v_accvgpr_write_b32 a120, v198
v_accvgpr_write_b32 a121, v199
v_accvgpr_write_b32 a122, v200
v_accvgpr_write_b32 a123, v201
AR 198,128
AR 199,129
AR 200,130
AR 201,131
v_mul_f32 v198, v198, v205
v_mul_f32 v199, v199, v205
v_mul_f32 v200, v200, v205
v_mul_f32 v201, v201, v205
v_accvgpr_write_b32 a128, v198
v_accvgpr_write_b32 a129, v199
v_accvgpr_write_b32 a130, v200
v_accvgpr_write_b32 a131, v201
EX 164,132
EX 165,133
EX 166,134
EX 167,135
EX 168,136
EX 169,137
EX 170,138
EX 171,139
s_nop 0
CV 156,164,165
CV 157,166,167
CV 158,168,169
CV 159,170,171
s_nop 1
s_add_u32 s64, s64, 4
s_addc_u32 s65, s65, 0
s_setpc_b64 s[64:65]
Lat_rare1:
s_nop 7
v_max3_f32 v204, v140, v141, v142
v_max3_f32 v204, v204, v143, v144
v_max3_f32 v204, v204, v145, v146
v_max_f32 v204, v204, v147
v_mov_b32 v202, v204
v_mov_b32 v203, v204
s_nop 1
v_permlane16_swap_b32 v202, v203
s_nop 1
v_max_f32 v204, v202, v203
v_mov_b32 v202, v204
v_mov_b32 v203, v204
s_nop 1
v_permlane32_swap_b32 v202, v203
s_nop 1
v_max_f32 v204, v202, v203
v_add_f32 v204, 0x41c00000, v204
v_max_f32 v204, 0, v204
v_sub_f32 v205, 0, v204
EX 205,205
v_add_f32 v186, v186, v204
v_sub_f32 v152, 0, v186
v_sub_f32 v153, 0, v186
v_sub_f32 v154, 0, v186
v_sub_f32 v155, 0, v186
v_sub_f32 v140, v140, v204
v_sub_f32 v141, v141, v204
v_sub_f32 v142, v142, v204
v_sub_f32 v143, v143, v204
v_sub_f32 v144, v144, v204
v_sub_f32 v145, v145, v204
v_sub_f32 v146, v146, v204
v_sub_f32 v147, v147, v204
AR 198,4
AR 199,5
AR 200,6
AR 201,7
v_mul_f32 v198, v198, v205
v_mul_f32 v199, v199, v205
v_mul_f32 v200, v200, v205
v_mul_f32 v201, v201, v205
v_accvgpr_write_b32 a4, v198
v_accvgpr_write_b32 a5, v199
v_accvgpr_write_b32 a6, v200
v_accvgpr_write_b32 a7, v201
AR 198,12
AR 199,13
AR 200,14
AR 201,15
v_mul_f32 v198, v198, v205
v_mul_f32 v199, v199, v205
v_mul_f32 v200, v200, v205
v_mul_f32 v201, v201, v205
v_accvgpr_write_b32 a12, v198
v_accvgpr_write_b32 a13, v199
v_accvgpr_write_b32 a14, v200
v_accvgpr_write_b32 a15, v201
AR 198,20
AR 199,21
AR 200,22
AR 201,23
v_mul_f32 v198, v198, v205
v_mul_f32 v199, v199, v205
v_mul_f32 v200, v200, v205
v_mul_f32 v201, v201, v205
v_accvgpr_write_b32 a20, v198
v_accvgpr_write_b32 a21, v199
v_accvgpr_write_b32 a22, v200
v_accvgpr_write_b32 a23, v201
AR 198,28
AR 199,29
AR 200,30
AR 201,31
v_mul_f32 v198, v198, v205
v_mul_f32 v199, v199, v205
v_mul_f32 v200, v200, v205
v_mul_f32 v201, v201, v205
v_accvgpr_write_b32 a28, v198
v_accvgpr_write_b32 a29, v199
v_accvgpr_write_b32 a30, v200
v_accvgpr_write_b32 a31, v201
AR 198,36
AR 199,37
AR 200,38
AR 201,39
v_mul_f32 v198, v198, v205
v_mul_f32 v199, v199, v205
v_mul_f32 v200, v200, v205
v_mul_f32 v201, v201, v205
v_accvgpr_write_b32 a36, v198
v_accvgpr_write_b32 a37, v199
v_accvgpr_write_b32 a38, v200
v_accvgpr_write_b32 a39, v201
AR 198,44
AR 199,45
AR 200,46
AR 201,47
v_mul_f32 v198, v198, v205
v_mul_f32 v199, v199, v205
v_mul_f32 v200, v200, v205
v_mul_f32 v201, v201, v205
v_accvgpr_write_b32 a44, v198
v_accvgpr_write_b32 a45, v199
v_accvgpr_write_b32 a46, v200
v_accvgpr_write_b32 a47, v201
AR 198,52
AR 199,53
AR 200,54
AR 201,55
v_mul_f32 v198, v198, v205
v_mul_f32 v199, v199, v205
v_mul_f32 v200, v200, v205
v_mul_f32 v201, v201, v205
v_accvgpr_write_b32 a52, v198
v_accvgpr_write_b32 a53, v199
v_accvgpr_write_b32 a54, v200
v_accvgpr_write_b32 a55, v201
AR 198,60
AR 199,61
AR 200,62
AR 201,63
v_mul_f32 v198, v198, v205
v_mul_f32 v199, v199, v205
v_mul_f32 v200, v200, v205
v_mul_f32 v201, v201, v205
v_accvgpr_write_b32 a60, v198
v_accvgpr_write_b32 a61, v199
v_accvgpr_write_b32 a62, v200
v_accvgpr_write_b32 a63, v201
AR 198,68
AR 199,69
AR 200,70
AR 201,71
v_mul_f32 v198, v198, v205
v_mul_f32 v199, v199, v205
v_mul_f32 v200, v200, v205
v_mul_f32 v201, v201, v205
v_accvgpr_write_b32 a68, v198
v_accvgpr_write_b32 a69, v199
v_accvgpr_write_b32 a70, v200
v_accvgpr_write_b32 a71, v201
AR 198,76
AR 199,77
AR 200,78
AR 201,79
v_mul_f32 v198, v198, v205
v_mul_f32 v199, v199, v205
v_mul_f32 v200, v200, v205
v_mul_f32 v201, v201, v205
v_accvgpr_write_b32 a76, v198
v_accvgpr_write_b32 a77, v199
v_accvgpr_write_b32 a78, v200
v_accvgpr_write_b32 a79, v201
AR 198,84
AR 199,85
AR 200,86
AR 201,87
v_mul_f32 v198, v198, v205
v_mul_f32 v199, v199, v205
v_mul_f32 v200, v200, v205
v_mul_f32 v201, v201, v205
v_accvgpr_write_b32 a84, v198
v_accvgpr_write_b32 a85, v199
v_accvgpr_write_b32 a86, v200
v_accvgpr_write_b32 a87, v201
AR 198,92
AR 199,93
AR 200,94
AR 201,95
v_mul_f32 v198, v198, v205
v_mul_f32 v199, v199, v205
v_mul_f32 v200, v200, v205
v_mul_f32 v201, v201, v205
v_accvgpr_write_b32 a92, v198
v_accvgpr_write_b32 a93, v199
v_accvgpr_write_b32 a94, v200
v_accvgpr_write_b32 a95, v201
AR 198,100
AR 199,101
AR 200,102
AR 201,103
v_mul_f32 v198, v198, v205
v_mul_f32 v199, v199, v205
v_mul_f32 v200, v200, v205
v_mul_f32 v201, v201, v205
v_accvgpr_write_b32 a100, v198
v_accvgpr_write_b32 a101, v199
v_accvgpr_write_b32 a102, v200
v_accvgpr_write_b32 a103, v201
AR 198,108
AR 199,109
AR 200,110
AR 201,111
v_mul_f32 v198, v198, v205
v_mul_f32 v199, v199, v205
v_mul_f32 v200, v200, v205
v_mul_f32 v201, v201, v205
v_accvgpr_write_b32 a108, v198
v_accvgpr_write_b32 a109, v199
v_accvgpr_write_b32 a110, v200
)ASM"
R"ASM(v_accvgpr_write_b32 a111, v201
AR 198,116
AR 199,117
AR 200,118
AR 201,119
v_mul_f32 v198, v198, v205
v_mul_f32 v199, v199, v205
v_mul_f32 v200, v200, v205
v_mul_f32 v201, v201, v205
v_accvgpr_write_b32 a116, v198
v_accvgpr_write_b32 a117, v199
v_accvgpr_write_b32 a118, v200
v_accvgpr_write_b32 a119, v201
AR 198,124
AR 199,125
AR 200,126
AR 201,127
v_mul_f32 v198, v198, v205
v_mul_f32 v199, v199, v205
v_mul_f32 v200, v200, v205
v_mul_f32 v201, v201, v205
v_accvgpr_write_b32 a124, v198
v_accvgpr_write_b32 a125, v199
v_accvgpr_write_b32 a126, v200
v_accvgpr_write_b32 a127, v201
AR 198,132
AR 199,133
AR 200,134
AR 201,135
v_mul_f32 v198, v198, v205
v_mul_f32 v199, v199, v205
v_mul_f32 v200, v200, v205
v_mul_f32 v201, v201, v205
v_accvgpr_write_b32 a132, v198
v_accvgpr_write_b32 a133, v199
v_accvgpr_write_b32 a134, v200
v_accvgpr_write_b32 a135, v201
EX 172,140
EX 173,141
EX 174,142
EX 175,143
EX 176,144
EX 177,145
EX 178,146
EX 179,147
s_nop 0
CV 160,172,173
CV 161,174,175
CV 162,176,177
CV 163,178,179
s_nop 1
s_add_u32 s64, s64, 4
s_addc_u32 s65, s65, 0
s_setpc_b64 s[64:65]
Lp2_entry:
s_cmp_ge_u32 s19, 31
s_cbranch_scc1 Lat_done
s_load_dwordx2 s[42:43], %1, 0x8
s_load_dwordx2 s[44:45], %1, 0x10
v_and_b32 v112, 63, v1
v_lshlrev_b32 v109, 4, v112
v_and_b32 v113, 15, v112
v_lshrrev_b32 v114, 1, v113
v_lshrrev_b32 v115, 4, v112
v_xor_b32 v114, v114, v115
v_lshlrev_b32 v114, 4, v114
v_lshlrev_b32 v113, 7, v113
v_add_u32 v113, v113, v114
s_lshr_b32 s20, s16, 1
s_lshl_b32 s20, s20, 14
v_add_u32 v100, s20, v113
s_and_b32 s20, s16, 1
s_lshl_b32 s20, s20, 12
v_add_u32 v103, s20, v109
v_lshrrev_b32 v113, 3, v112
s_lshl_b32 s20, s16, 6
v_add_u32 v113, s20, v113
v_lshlrev_b32 v113, 12, v113
v_lshrrev_b32 v114, 4, v112
v_and_b32 v115, 7, v112
v_xor_b32 v115, v115, v114
v_xor_b32 v116, 4, v115
v_lshlrev_b32 v117, 4, v115
v_add_u32 v104, v117, v113
v_lshlrev_b32 v117, 4, v116
v_add_u32 v105, v117, v113
v_add_u32 v105, 31744, v105
v_lshlrev_b32 v117, 4, v115
v_add_u32 v106, v117, v113
v_add_u32 v106, 63488, v106
v_lshlrev_b32 v117, 4, v116
v_add_u32 v107, v117, v113
v_add_u32 v107, 95232, v107
s_lshl_b32 s39, s16, 11
v_add_u32 v108, s39, v109
s_waitcnt lgkmcnt(0)
s_sub_u32 s41, s19, 24
s_lshl_b32 s20, s18, 20
s_add_u32 s24, s42, s20
s_addc_u32 s25, s43, 0
s_sub_u32 s26, 8388608, s20
s_mov_b32 s27, 0x20000
s_lshl_b32 s20, s41, 13
s_add_u32 s20, s20, 75497472
s_add_u32 s28, s8, s20
s_addc_u32 s29, s9, 0
s_mov_b32 s30, 3670016
s_mov_b32 s31, 0x20000
s_mov_b32 s48, s44
s_mov_b32 s49, s45
s_mov_b32 s50, 4096
s_mov_b32 s51, 0x20000
s_mov_b32 s34, 0
s_mov_b32 s35, 0
s_lshl_b32 s20, s16, 13
s_add_u32 s36, s20, 65536
s_mov_b32 s37, 65536
s_mov_b32 m0, s36
s_nop 0
buffer_load_dwordx4 v104, s[24:27], s34 offen lds
buffer_load_dwordx4 v105, s[24:27], s34 offen offset:1024 lds
buffer_load_dwordx4 v106, s[24:27], s34 offen offset:2048 lds
buffer_load_dwordx4 v107, s[24:27], s34 offen offset:3072 lds
s_add_u32 s21, s34, 131072
s_add_u32 m0, s36, 4096
s_nop 0
buffer_load_dwordx4 v104, s[24:27], s21 offen lds
buffer_load_dwordx4 v105, s[24:27], s21 offen offset:1024 lds
buffer_load_dwordx4 v106, s[24:27], s21 offen offset:2048 lds
buffer_load_dwordx4 v107, s[24:27], s21 offen offset:3072 lds
s_add_u32 m0, s39, 0
s_nop 0
buffer_load_dwordx4 v108, s[28:31], s35 offen lds
buffer_load_dwordx4 v108, s[28:31], s35 offen offset:1024 lds
s_add_u32 s34, s34, 128
s_add_u32 s36, s36, 32768
s_mov_b32 m0, s36
s_nop 0
buffer_load_dwordx4 v104, s[24:27], s34 offen lds
buffer_load_dwordx4 v105, s[24:27], s34 offen offset:1024 lds
buffer_load_dwordx4 v106, s[24:27], s34 offen offset:2048 lds
buffer_load_dwordx4 v107, s[24:27], s34 offen offset:3072 lds
s_add_u32 s35, s35, 57344
s_add_u32 m0, s39, 8192
s_nop 0
buffer_load_dwordx4 v108, s[28:31], s35 offen lds
buffer_load_dwordx4 v108, s[28:31], s35 offen offset:1024 lds
s_add_u32 s21, s34, 131072
s_add_u32 m0, s36, 4096
s_nop 0
buffer_load_dwordx4 v104, s[24:27], s21 offen lds
buffer_load_dwordx4 v105, s[24:27], s21 offen offset:1024 lds
buffer_load_dwordx4 v106, s[24:27], s21 offen offset:2048 lds
buffer_load_dwordx4 v107, s[24:27], s21 offen offset:3072 lds
s_add_u32 s35, s35, 57344
s_add_u32 m0, s39, 16384
s_nop 0
buffer_load_dwordx4 v108, s[28:31], s35 offen lds
buffer_load_dwordx4 v108, s[28:31], s35 offen offset:1024 lds
s_waitcnt vmcnt(12)
s_barrier
s_add_u32 s34, s34, 128
s_add_u32 s36, s36, 32768
s_mov_b32 m0, s36
s_nop 0
buffer_load_dwordx4 v104, s[24:27], s34 offen lds
buffer_load_dwordx4 v105, s[24:27], s34 offen offset:1024 lds
buffer_load_dwordx4 v106, s[24:27], s34 offen offset:2048 lds
buffer_load_dwordx4 v107, s[24:27], s34 offen offset:3072 lds
s_add_u32 s35, s35, 57344
s_add_u32 m0, s39, 24576
s_nop 0
buffer_load_dwordx4 v108, s[28:31], s35 offen lds
buffer_load_dwordx4 v108, s[28:31], s35 offen offset:1024 lds
v_add_u32 v102, s37, v100
v_xor_b32 v101, 64, v102
Rv 68,103,0
Rv 72,103,1024
Rv 76,103,2048
Rv 80,103,3072
Rv 4,102,0
Rv 8,102,2048
Rv 12,102,4096
Rv 16,102,6144
Rv 20,102,8192
Rv 24,102,10240
Rv 28,102,12288
Rv 32,102,14336
s_waitcnt vmcnt(12) lgkmcnt(0)
s_barrier
MZavv 0,68,4
MZavv 4,72,4
s_add_u32 s21, s34, 131072
s_add_u32 m0, s36, 4096
Rv 84,103,8192
MZavv 8,76,4
Rv 88,103,9216
MZavv 12,80,4
buffer_load_dwordx4 v104, s[24:27], s21 offen lds
MZavv 16,68,8
Rv 92,103,10240
MZavv 20,72,8
MZavv 24,76,8
Rv 96,103,11264
MZavv 28,80,8
Rv 36,101,0
MZavv 32,68,12
buffer_load_dwordx4 v105, s[24:27], s21 offen offset:1024 lds
MZavv 36,72,12
Rv 40,101,2048
MZavv 40,76,12
MZavv 44,80,12
Rv 44,101,4096
MZavv 48,68,16
Rv 48,101,6144
MZavv 52,72,16
buffer_load_dwordx4 v106, s[24:27], s21 offen offset:2048 lds
MZavv 56,76,16
Rv 52,101,8192
MZavv 60,80,16
MZavv 64,68,20
Rv 56,101,10240
MZavv 68,72,20
Rv 60,101,12288
MZavv 72,76,20
buffer_load_dwordx4 v107, s[24:27], s21 offen offset:3072 lds
MZavv 76,80,20
Rv 64,101,14336
MZavv 80,68,24
MZavv 84,72,24
s_add_u32 s35, s35, 57344
s_add_u32 m0, s39, 0
MZavv 88,76,24
MZavv 92,80,24
buffer_load_dwordx4 v108, s[28:31], s35 offen lds
MZavv 96,68,28
MZavv 100,72,28
MZavv 104,76,28
MZavv 108,80,28
MZavv 112,68,32
buffer_load_dwordx4 v108, s[28:31], s35 offen offset:1024 lds
MZavv 116,72,32
MZavv 120,76,32
MZavv 124,80,32
s_waitcnt vmcnt(12) lgkmcnt(0)
s_barrier
MAavv 0,84,36
s_add_u32 s37, s37, 32768
s_cmp_eq_u32 s37, 163840
s_cselect_b32 s37, 65536, s37
MAavv 4,88,36
v_add_u32 v102, s37, v100
v_xor_b32 v101, 64, v102
s_add_u32 s34, s34, 128
MAavv 8,92,36
s_add_u32 s36, s36, 32768
s_cmp_ge_u32 s36, 163840
s_cselect_b32 s20, 98304, 0
MAavv 12,96,36
s_sub_u32 s36, s36, s20
s_mov_b32 m0, s36
Rv 68,103,16384
MAavv 16,84,40
Rv 72,103,17408
MAavv 20,88,40
Rv 76,103,18432
MAavv 24,92,40
Rv 80,103,19456
MAavv 28,96,40
Rv 4,102,0
MAavv 32,84,44
buffer_load_dwordx4 v104, s[24:27], s34 offen lds
MAavv 36,88,44
Rv 8,102,2048
MAavv 40,92,44
buffer_load_dwordx4 v105, s[24:27], s34 offen offset:1024 lds
MAavv 44,96,44
Rv 12,102,4096
MAavv 48,84,48
Rv 16,102,6144
MAavv 52,88,48
buffer_load_dwordx4 v106, s[24:27], s34 offen offset:2048 lds
MAavv 56,92,48
Rv 20,102,8192
MAavv 60,96,48
MAavv 64,84,52
Rv 24,102,10240
MAavv 68,88,52
Rv 28,102,12288
MAavv 72,92,52
buffer_load_dwordx4 v107, s[24:27], s34 offen offset:3072 lds
MAavv 76,96,52
Rv 32,102,14336
MAavv 80,84,56
MAavv 84,88,56
s_add_u32 s35, s35, 57344
s_add_u32 m0, s39, 8192
MAavv 88,92,56
MAavv 92,96,56
buffer_load_dwordx4 v108, s[28:31], s35 offen lds
MAavv 96,84,60
MAavv 100,88,60
MAavv 104,92,60
MAavv 108,96,60
MAavv 112,84,64
buffer_load_dwordx4 v108, s[28:31], s35 offen offset:1024 lds
MAavv 116,88,64
MAavv 120,92,64
MAavv 124,96,64
s_waitcnt vmcnt(12) lgkmcnt(0)
s_barrier
MAavv 0,68,4
MAavv 4,72,4
s_add_u32 s21, s34, 131072
s_add_u32 m0, s36, 4096
Rv 84,103,24576
MAavv 8,76,4
Rv 88,103,25600
MAavv 12,80,4
buffer_load_dwordx4 v104, s[24:27], s21 offen lds
MAavv 16,68,8
Rv 92,103,26624
MAavv 20,72,8
MAavv 24,76,8
Rv 96,103,27648
MAavv 28,80,8
Rv 36,101,0
MAavv 32,68,12
buffer_load_dwordx4 v105, s[24:27], s21 offen offset:1024 lds
MAavv 36,72,12
Rv 40,101,2048
MAavv 40,76,12
MAavv 44,80,12
Rv 44,101,4096
MAavv 48,68,16
Rv 48,101,6144
MAavv 52,72,16
buffer_load_dwordx4 v106, s[24:27], s21 offen offset:2048 lds
MAavv 56,76,16
Rv 52,101,8192
MAavv 60,80,16
MAavv 64,68,20
Rv 56,101,10240
MAavv 68,72,20
Rv 60,101,12288
MAavv 72,76,20
buffer_load_dwordx4 v107, s[24:27], s21 offen offset:3072 lds
MAavv 76,80,20
Rv 64,101,14336
MAavv 80,68,24
MAavv 84,72,24
s_add_u32 s35, s35, 57344
s_add_u32 m0, s39, 16384
MAavv 88,76,24
MAavv 92,80,24
buffer_load_dwordx4 v108, s[28:31], s35 offen lds
MAavv 96,68,28
MAavv 100,72,28
MAavv 104,76,28
MAavv 108,80,28
MAavv 112,68,32
buffer_load_dwordx4 v108, s[28:31], s35 offen offset:1024 lds
MAavv 116,72,32
MAavv 120,76,32
MAavv 124,80,32
s_waitcnt vmcnt(12) lgkmcnt(0)
s_barrier
MAavv 0,84,36
s_add_u32 s37, s37, 32768
s_cmp_eq_u32 s37, 163840
s_cselect_b32 s37, 65536, s37
MAavv 4,88,36
v_add_u32 v102, s37, v100
v_xor_b32 v101, 64, v102
s_add_u32 s34, s34, 128
MAavv 8,92,36
s_add_u32 s36, s36, 32768
s_cmp_ge_u32 s36, 163840
s_cselect_b32 s20, 98304, 0
MAavv 12,96,36
s_sub_u32 s36, s36, s20
s_mov_b32 m0, s36
Rv 68,103,0
MAavv 16,84,40
Rv 72,103,1024
MAavv 20,88,40
Rv 76,103,2048
MAavv 24,92,40
Rv 80,103,3072
MAavv 28,96,40
Rv 4,102,0
MAavv 32,84,44
buffer_load_dwordx4 v104, s[24:27], s34 offen lds
MAavv 36,88,44
Rv 8,102,2048
MAavv 40,92,44
buffer_load_dwordx4 v105, s[24:27], s34 offen offset:1024 lds
MAavv 44,96,44
Rv 12,102,4096
MAavv 48,84,48
Rv 16,102,6144
MAavv 52,88,48
buffer_load_dwordx4 v106, s[24:27], s34 offen offset:2048 lds
MAavv 56,92,48
Rv 20,102,8192
MAavv 60,96,48
MAavv 64,84,52
Rv 24,102,10240
MAavv 68,88,52
Rv 28,102,12288
MAavv 72,92,52
buffer_load_dwordx4 v107, s[24:27], s34 offen offset:3072 lds
MAavv 76,96,52
Rv 32,102,14336
MAavv 80,84,56
MAavv 84,88,56
s_add_u32 s35, s35, 57344
s_add_u32 m0, s39, 24576
MAavv 88,92,56
MAavv 92,96,56
buffer_load_dwordx4 v108, s[28:31], s35 offen lds
MAavv 96,84,60
MAavv 100,88,60
MAavv 104,92,60
MAavv 108,96,60
MAavv 112,84,64
buffer_load_dwordx4 v108, s[28:31], s35 offen offset:1024 lds
MAavv 116,88,64
MAavv 120,92,64
MAavv 124,96,64
s_mov_b32 s38, 14
Lp2_loop:
s_waitcnt vmcnt(12) lgkmcnt(0)
s_barrier
MAavv 0,68,4
MAavv 4,72,4
s_add_u32 s21, s34, 131072
s_add_u32 m0, s36, 4096
Rv 84,103,8192
MAavv 8,76,4
Rv 88,103,9216
MAavv 12,80,4
buffer_load_dwordx4 v104, s[24:27], s21 offen lds
MAavv 16,68,8
Rv 92,103,10240
MAavv 20,72,8
MAavv 24,76,8
Rv 96,103,11264
MAavv 28,80,8
Rv 36,101,0
MAavv 32,68,12
buffer_load_dwordx4 v105, s[24:27], s21 offen offset:1024 lds
MAavv 36,72,12
Rv 40,101,2048
MAavv 40,76,12
MAavv 44,80,12
Rv 44,101,4096
MAavv 48,68,16
Rv 48,101,6144
MAavv 52,72,16
buffer_load_dwordx4 v106, s[24:27], s21 offen offset:2048 lds
MAavv 56,76,16
Rv 52,101,8192
MAavv 60,80,16
MAavv 64,68,20
Rv 56,101,10240
MAavv 68,72,20
Rv 60,101,12288
MAavv 72,76,20
buffer_load_dwordx4 v107, s[24:27], s21 offen offset:3072 lds
MAavv 76,80,20
Rv 64,101,14336
MAavv 80,68,24
MAavv 84,72,24
s_add_u32 s35, s35, 57344
s_add_u32 m0, s39, 0
MAavv 88,76,24
MAavv 92,80,24
buffer_load_dwordx4 v108, s[28:31], s35 offen lds
MAavv 96,68,28
MAavv 100,72,28
MAavv 104,76,28
MAavv 108,80,28
MAavv 112,68,32
buffer_load_dwordx4 v108, s[28:31], s35 offen offset:1024 lds
MAavv 116,72,32
MAavv 120,76,32
MAavv 124,80,32
s_waitcnt vmcnt(12) lgkmcnt(0)
s_barrier
MAavv 0,84,36
s_add_u32 s37, s37, 32768
s_cmp_eq_u32 s37, 163840
s_cselect_b32 s37, 65536, s37
MAavv 4,88,36
v_add_u32 v102, s37, v100
v_xor_b32 v101, 64, v102
s_add_u32 s34, s34, 128
MAavv 8,92,36
s_add_u32 s36, s36, 32768
s_cmp_ge_u32 s36, 163840
s_cselect_b32 s20, 98304, 0
MAavv 12,96,36
s_sub_u32 s36, s36, s20
s_mov_b32 m0, s36
Rv 68,103,16384
MAavv 16,84,40
Rv 72,103,17408
MAavv 20,88,40
Rv 76,103,18432
MAavv 24,92,40
Rv 80,103,19456
MAavv 28,96,40
Rv 4,102,0
MAavv 32,84,44
buffer_load_dwordx4 v104, s[24:27], s34 offen lds
MAavv 36,88,44
Rv 8,102,2048
MAavv 40,92,44
buffer_load_dwordx4 v105, s[24:27], s34 offen offset:1024 lds
MAavv 44,96,44
Rv 12,102,4096
MAavv 48,84,48
Rv 16,102,6144
MAavv 52,88,48
buffer_load_dwordx4 v106, s[24:27], s34 offen offset:2048 lds
MAavv 56,92,48
Rv 20,102,8192
MAavv 60,96,48
MAavv 64,84,52
Rv 24,102,10240
MAavv 68,88,52
Rv 28,102,12288
MAavv 72,92,52
buffer_load_dwordx4 v107, s[24:27], s34 offen offset:3072 lds
MAavv 76,96,52
Rv 32,102,14336
MAavv 80,84,56
MAavv 84,88,56
s_add_u32 s35, s35, 57344
s_add_u32 m0, s39, 8192
MAavv 88,92,56
MAavv 92,96,56
buffer_load_dwordx4 v108, s[28:31], s35 offen lds
MAavv 96,84,60
MAavv 100,88,60
MAavv 104,92,60
MAavv 108,96,60
MAavv 112,84,64
buffer_load_dwordx4 v108, s[28:31], s35 offen offset:1024 lds
MAavv 116,88,64
MAavv 120,92,64
MAavv 124,96,64
s_waitcnt vmcnt(12) lgkmcnt(0)
s_barrier
MAavv 0,68,4
MAavv 4,72,4
s_add_u32 s21, s34, 131072
s_add_u32 m0, s36, 4096
Rv 84,103,24576
MAavv 8,76,4
Rv 88,103,25600
MAavv 12,80,4
buffer_load_dwordx4 v104, s[24:27], s21 offen lds
MAavv 16,68,8
Rv 92,103,26624
MAavv 20,72,8
MAavv 24,76,8
Rv 96,103,27648
MAavv 28,80,8
Rv 36,101,0
MAavv 32,68,12
buffer_load_dwordx4 v105, s[24:27], s21 offen offset:1024 lds
MAavv 36,72,12
Rv 40,101,2048
MAavv 40,76,12
MAavv 44,80,12
Rv 44,101,4096
MAavv 48,68,16
Rv 48,101,6144
MAavv 52,72,16
buffer_load_dwordx4 v106, s[24:27], s21 offen offset:2048 lds
MAavv 56,76,16
Rv 52,101,8192
MAavv 60,80,16
MAavv 64,68,20
Rv 56,101,10240
MAavv 68,72,20
Rv 60,101,12288
MAavv 72,76,20
buffer_load_dwordx4 v107, s[24:27], s21 offen offset:3072 lds
MAavv 76,80,20
Rv 64,101,14336
MAavv 80,68,24
MAavv 84,72,24
s_add_u32 s35, s35, 57344
s_add_u32 m0, s39, 16384
MAavv 88,76,24
MAavv 92,80,24
buffer_load_dwordx4 v108, s[28:31], s35 offen lds
MAavv 96,68,28
MAavv 100,72,28
MAavv 104,76,28
MAavv 108,80,28
MAavv 112,68,32
buffer_load_dwordx4 v108, s[28:31], s35 offen offset:1024 lds
MAavv 116,72,32
MAavv 120,76,32
MAavv 124,80,32
s_waitcnt vmcnt(12) lgkmcnt(0)
s_barrier
MAavv 0,84,36
s_add_u32 s37, s37, 32768
s_cmp_eq_u32 s37, 163840
s_cselect_b32 s37, 65536, s37
MAavv 4,88,36
v_add_u32 v102, s37, v100
v_xor_b32 v101, 64, v102
s_add_u32 s34, s34, 128
MAavv 8,92,36
s_add_u32 s36, s36, 32768
s_cmp_ge_u32 s36, 163840
s_cselect_b32 s20, 98304, 0
MAavv 12,96,36
s_sub_u32 s36, s36, s20
s_mov_b32 m0, s36
Rv 68,103,0
MAavv 16,84,40
Rv 72,103,1024
MAavv 20,88,40
Rv 76,103,2048
MAavv 24,92,40
Rv 80,103,3072
MAavv 28,96,40
Rv 4,102,0
MAavv 32,84,44
buffer_load_dwordx4 v104, s[24:27], s34 offen lds
MAavv 36,88,44
Rv 8,102,2048
MAavv 40,92,44
buffer_load_dwordx4 v105, s[24:27], s34 offen offset:1024 lds
MAavv 44,96,44
Rv 12,102,4096
MAavv 48,84,48
Rv 16,102,6144
MAavv 52,88,48
buffer_load_dwordx4 v106, s[24:27], s34 offen offset:2048 lds
MAavv 56,92,48
Rv 20,102,8192
MAavv 60,96,48
MAavv 64,84,52
Rv 24,102,10240
MAavv 68,88,52
Rv 28,102,12288
MAavv 72,92,52
buffer_load_dwordx4 v107, s[24:27], s34 offen offset:3072 lds
MAavv 76,96,52
Rv 32,102,14336
MAavv 80,84,56
MAavv 84,88,56
s_add_u32 s35, s35, 57344
s_add_u32 m0, s39, 24576
MAavv 88,92,56
MAavv 92,96,56
buffer_load_dwordx4 v108, s[28:31], s35 offen lds
MAavv 96,84,60
MAavv 100,88,60
MAavv 104,92,60
MAavv 108,96,60
MAavv 112,84,64
buffer_load_dwordx4 v108, s[28:31], s35 offen offset:1024 lds
MAavv 116,88,64
MAavv 120,92,64
MAavv 124,96,64
s_sub_u32 s38, s38, 1
s_cmp_lg_u32 s38, 0
s_cbranch_scc1 Lp2_loop
s_waitcnt vmcnt(12) lgkmcnt(0)
s_barrier
MAavv 0,68,4
MAavv 4,72,4
Rv 84,103,8192
MAavv 8,76,4
Rv 88,103,9216
MAavv 12,80,4
MAavv 16,68,8
Rv 92,103,10240
MAavv 20,72,8
MAavv 24,76,8
Rv 96,103,11264
MAavv 28,80,8
Rv 36,101,0
MAavv 32,68,12
MAavv 36,72,12
Rv 40,101,2048
MAavv 40,76,12
MAavv 44,80,12
Rv 44,101,4096
MAavv 48,68,16
Rv 48,101,6144
MAavv 52,72,16
MAavv 56,76,16
Rv 52,101,8192
MAavv 60,80,16
MAavv 64,68,20
Rv 56,101,10240
MAavv 68,72,20
Rv 60,101,12288
MAavv 72,76,20
MAavv 76,80,20
Rv 64,101,14336
MAavv 80,68,24
MAavv 84,72,24
MAavv 88,76,24
MAavv 92,80,24
MAavv 96,68,28
MAavv 100,72,28
MAavv 104,76,28
MAavv 108,80,28
MAavv 112,68,32
MAavv 116,72,32
MAavv 120,76,32
MAavv 124,80,32
s_waitcnt vmcnt(6) lgkmcnt(0)
s_barrier
MAavv 0,84,36
s_add_u32 s37, s37, 32768
s_cmp_eq_u32 s37, 163840
s_cselect_b32 s37, 65536, s37
MAavv 4,88,36
v_add_u32 v102, s37, v100
v_xor_b32 v101, 64, v102
MAavv 8,92,36
Rv 68,103,16384
MAavv 12,96,36
Rv 72,103,17408
MAavv 16,84,40
Rv 76,103,18432
MAavv 20,88,40
MAavv 24,92,40
Rv 80,103,19456
MAavv 28,96,40
Rv 4,102,0
MAavv 32,84,44
MAavv 36,88,44
Rv 8,102,2048
MAavv 40,92,44
MAavv 44,96,44
Rv 12,102,4096
MAavv 48,84,48
Rv 16,102,6144
MAavv 52,88,48
MAavv 56,92,48
Rv 20,102,8192
MAavv 60,96,48
MAavv 64,84,52
Rv 24,102,10240
MAavv 68,88,52
Rv 28,102,12288
MAavv 72,92,52
MAavv 76,96,52
Rv 32,102,14336
MAavv 80,84,56
MAavv 84,88,56
MAavv 88,92,56
MAavv 92,96,56
MAavv 96,84,60
MAavv 100,88,60
MAavv 104,92,60
MAavv 108,96,60
MAavv 112,84,64
MAavv 116,88,64
MAavv 120,92,64
MAavv 124,96,64
s_waitcnt vmcnt(0) lgkmcnt(0)
s_barrier
MAavv 0,68,4
MAavv 4,72,4
Rv 84,103,24576
MAavv 8,76,4
Rv 88,103,25600
MAavv 12,80,4
MAavv 16,68,8
Rv 92,103,26624
MAavv 20,72,8
MAavv 24,76,8
Rv 96,103,27648
MAavv 28,80,8
Rv 36,101,0
MAavv 32,68,12
MAavv 36,72,12
Rv 40,101,2048
MAavv 40,76,12
MAavv 44,80,12
Rv 44,101,4096
MAavv 48,68,16
Rv 48,101,6144
MAavv 52,72,16
MAavv 56,76,16
Rv 52,101,8192
MAavv 60,80,16
MAavv 64,68,20
Rv 56,101,10240
MAavv 68,72,20
Rv 60,101,12288
MAavv 72,76,20
MAavv 76,80,20
Rv 64,101,14336
MAavv 80,68,24
MAavv 84,72,24
MAavv 88,76,24
MAavv 92,80,24
MAavv 96,68,28
MAavv 100,72,28
MAavv 104,76,28
MAavv 108,80,28
MAavv 112,68,32
MAavv 116,72,32
MAavv 120,76,32
MAavv 124,80,32
s_waitcnt vmcnt(0) lgkmcnt(0)
s_barrier
MAavv 0,84,36
s_add_u32 s37, s37, 32768
s_cmp_eq_u32 s37, 163840
s_cselect_b32 s37, 65536, s37
MAavv 4,88,36
v_add_u32 v102, s37, v100
v_xor_b32 v101, 64, v102
MAavv 8,92,36
MAavv 12,96,36
MAavv 16,84,40
MAavv 20,88,40
MAavv 24,92,40
MAavv 28,96,40
MAavv 32,84,44
MAavv 36,88,44
MAavv 40,92,44
MAavv 44,96,44
MAavv 48,84,48
MAavv 52,88,48
MAavv 56,92,48
MAavv 60,96,48
MAavv 64,84,52
MAavv 68,88,52
MAavv 72,92,52
MAavv 76,96,52
MAavv 80,84,56
MAavv 84,88,56
MAavv 88,92,56
MAavv 92,96,56
MAavv 96,84,60
MAavv 100,88,60
MAavv 104,92,60
MAavv 108,96,60
MAavv 112,84,64
MAavv 116,88,64
MAavv 120,92,64
MAavv 124,96,64
s_nop 7
s_mov_b32 s40, 0xbdd2d3e8
v_mov_b32 v108, 0xc0135761
s_lshl_b32 s20, s18, 9
s_lshr_b32 s21, s16, 1
s_lshl_b32 s21, s21, 8
s_add_u32 s20, s20, s21
v_lshrrev_b32 v112, 4, v109
v_and_b32 v112, 15, v112
v_lshlrev_b32 v112, 1, v112
buffer_load_ushort v128, v112, s[48:51], s20 offen
buffer_load_ushort v129, v112, s[48:51], s20 offen offset:32
buffer_load_ushort v130, v112, s[48:51], s20 offen offset:64
buffer_load_ushort v131, v112, s[48:51], s20 offen offset:96
buffer_load_ushort v132, v112, s[48:51], s20 offen offset:128
buffer_load_ushort v133, v112, s[48:51], s20 offen offset:160
buffer_load_ushort v134, v112, s[48:51], s20 offen offset:192
buffer_load_ushort v135, v112, s[48:51], s20 offen offset:224
s_lshl_b32 s20, s18, 4
s_lshr_b32 s21, s16, 1
s_lshl_b32 s21, s21, 3
s_add_u32 s20, s20, s21
s_mul_i32 s20, s20, 28
s_lshl_b32 s21, s41, 2
s_add_u32 s20, s20, s21
s_and_b32 s21, s16, 1
s_lshl_b32 s21, s21, 1
s_add_u32 s20, s20, s21
s_lshl_b32 s20, s20, 10
s_add_u32 s22, s20, 67108864
s_add_u32 s38, s20, 79691776
buffer_load_dwordx4 v[136:139], v109, s[12:15], s22 offen
buffer_load_dwordx4 v[140:143], v109, s[12:15], s22 offen offset:1024
s_add_u32 s22, s22, 28672
buffer_load_dwordx4 v[144:147], v109, s[12:15], s22 offen
buffer_load_dwordx4 v[148:151], v109, s[12:15], s22 offen offset:1024
s_add_u32 s22, s22, 28672
buffer_load_dwordx4 v[152:155], v109, s[12:15], s22 offen
buffer_load_dwordx4 v[156:159], v109, s[12:15], s22 offen offset:1024
s_add_u32 s22, s22, 28672
buffer_load_dwordx4 v[160:163], v109, s[12:15], s22 offen
buffer_load_dwordx4 v[164:167], v109, s[12:15], s22 offen offset:1024
s_add_u32 s22, s22, 28672
buffer_load_dwordx4 v[168:171], v109, s[12:15], s22 offen
buffer_load_dwordx4 v[172:175], v109, s[12:15], s22 offen offset:1024
s_add_u32 s22, s22, 28672
buffer_load_dwordx4 v[176:179], v109, s[12:15], s22 offen
buffer_load_dwordx4 v[180:183], v109, s[12:15], s22 offen offset:1024
s_add_u32 s22, s22, 28672
buffer_load_dwordx4 v[184:187], v109, s[12:15], s22 offen
buffer_load_dwordx4 v[188:191], v109, s[12:15], s22 offen offset:1024
s_add_u32 s22, s22, 28672
buffer_load_dwordx4 v[192:195], v109, s[12:15], s22 offen
buffer_load_dwordx4 v[196:199], v109, s[12:15], s22 offen offset:1024
s_waitcnt vmcnt(16)
v_lshlrev_b32 v128, 16, v128
v_lshlrev_b32 v129, 16, v129
v_lshlrev_b32 v130, 16, v130
v_lshlrev_b32 v131, 16, v131
v_lshlrev_b32 v132, 16, v132
v_lshlrev_b32 v133, 16, v133
v_lshlrev_b32 v134, 16, v134
v_lshlrev_b32 v135, 16, v135
s_waitcnt vmcnt(0)
v_lshlrev_b32 v4, 16, v136
v_and_b32 v5, 0xffff0000, v136
v_lshlrev_b32 v6, 16, v137
v_and_b32 v7, 0xffff0000, v137
v_lshlrev_b32 v8, 16, v138
v_and_b32 v9, 0xffff0000, v138
v_lshlrev_b32 v10, 16, v139
v_and_b32 v11, 0xffff0000, v139
v_mul_f32 v120, v4, v4
v_mul_f32 v121, v5, v5
v_mul_f32 v122, v6, v6
v_mul_f32 v123, v7, v7
v_mul_f32 v124, v8, v8
v_mul_f32 v125, v9, v9
v_mul_f32 v126, v10, v10
v_mul_f32 v127, v11, v11
v_fma_f32 v120, v120, s40, v108
v_fma_f32 v121, v121, s40, v108
v_fma_f32 v122, v122, s40, v108
v_fma_f32 v123, v123, s40, v108
v_fma_f32 v124, v124, s40, v108
v_fma_f32 v125, v125, s40, v108
v_fma_f32 v126, v126, s40, v108
v_fma_f32 v127, v127, s40, v108
v_mul_f32 v120, v120, v4
v_mul_f32 v121, v121, v5
v_mul_f32 v122, v122, v6
v_mul_f32 v123, v123, v7
v_mul_f32 v124, v124, v8
v_mul_f32 v125, v125, v9
v_mul_f32 v126, v126, v10
v_mul_f32 v127, v127, v11
EX 120,120
EX 121,121
EX 122,122
EX 123,123
EX 124,124
EX 125,125
EX 126,126
EX 127,127
AR 36,0
AR 37,1
AR 38,2
AR 39,3
AR 40,4
AR 41,5
AR 42,6
AR 43,7
v_add_f32 v120, 1.0, v120
v_add_f32 v121, 1.0, v121
v_add_f32 v122, 1.0, v122
v_add_f32 v123, 1.0, v123
v_add_f32 v124, 1.0, v124
v_add_f32 v125, 1.0, v125
v_add_f32 v126, 1.0, v126
v_add_f32 v127, 1.0, v127
v_rcp_f32 v120, v120
v_rcp_f32 v121, v121
v_rcp_f32 v122, v122
v_rcp_f32 v123, v123
v_rcp_f32 v124, v124
v_rcp_f32 v125, v125
v_rcp_f32 v126, v126
v_rcp_f32 v127, v127
v_add_f32 v36, v36, v128
v_add_f32 v37, v37, v128
v_add_f32 v38, v38, v128
v_add_f32 v39, v39, v128
v_add_f32 v40, v40, v128
v_add_f32 v41, v41, v128
v_add_f32 v42, v42, v128
v_add_f32 v43, v43, v128
v_mul_f32 v4, v4, v120
v_mul_f32 v5, v5, v121
v_mul_f32 v6, v6, v122
v_mul_f32 v7, v7, v123
v_mul_f32 v8, v8, v124
v_mul_f32 v9, v9, v125
v_mul_f32 v10, v10, v126
v_mul_f32 v11, v11, v127
v_mul_f32 v4, v4, v36
v_mul_f32 v5, v5, v37
v_mul_f32 v6, v6, v38
v_mul_f32 v7, v7, v39
v_mul_f32 v8, v8, v40
v_mul_f32 v9, v9, v41
v_mul_f32 v10, v10, v42
v_mul_f32 v11, v11, v43
CV 68,4,5
CV 69,6,7
CV 70,8,9
CV 71,10,11
buffer_store_dwordx4 v[68:71], v109, s[12:15], s38 offen sc1
v_lshlrev_b32 v12, 16, v140
v_and_b32 v13, 0xffff0000, v140
v_lshlrev_b32 v14, 16, v141
v_and_b32 v15, 0xffff0000, v141
v_lshlrev_b32 v16, 16, v142
v_and_b32 v17, 0xffff0000, v142
v_lshlrev_b32 v18, 16, v143
v_and_b32 v19, 0xffff0000, v143
v_mul_f32 v120, v12, v12
v_mul_f32 v121, v13, v13
v_mul_f32 v122, v14, v14
v_mul_f32 v123, v15, v15
v_mul_f32 v124, v16, v16
v_mul_f32 v125, v17, v17
v_mul_f32 v126, v18, v18
v_mul_f32 v127, v19, v19
v_fma_f32 v120, v120, s40, v108
v_fma_f32 v121, v121, s40, v108
v_fma_f32 v122, v122, s40, v108
v_fma_f32 v123, v123, s40, v108
v_fma_f32 v124, v124, s40, v108
v_fma_f32 v125, v125, s40, v108
v_fma_f32 v126, v126, s40, v108
v_fma_f32 v127, v127, s40, v108
v_mul_f32 v120, v120, v12
v_mul_f32 v121, v121, v13
v_mul_f32 v122, v122, v14
v_mul_f32 v123, v123, v15
v_mul_f32 v124, v124, v16
v_mul_f32 v125, v125, v17
v_mul_f32 v126, v126, v18
v_mul_f32 v127, v127, v19
EX 120,120
EX 121,121
EX 122,122
EX 123,123
EX 124,124
EX 125,125
EX 126,126
EX 127,127
AR 44,8
AR 45,9
AR 46,10
AR 47,11
AR 48,12
AR 49,13
AR 50,14
AR 51,15
v_add_f32 v120, 1.0, v120
v_add_f32 v121, 1.0, v121
v_add_f32 v122, 1.0, v122
v_add_f32 v123, 1.0, v123
v_add_f32 v124, 1.0, v124
v_add_f32 v125, 1.0, v125
v_add_f32 v126, 1.0, v126
v_add_f32 v127, 1.0, v127
v_rcp_f32 v120, v120
v_rcp_f32 v121, v121
v_rcp_f32 v122, v122
v_rcp_f32 v123, v123
v_rcp_f32 v124, v124
v_rcp_f32 v125, v125
v_rcp_f32 v126, v126
v_rcp_f32 v127, v127
v_add_f32 v44, v44, v128
v_add_f32 v45, v45, v128
v_add_f32 v46, v46, v128
v_add_f32 v47, v47, v128
v_add_f32 v48, v48, v128
v_add_f32 v49, v49, v128
v_add_f32 v50, v50, v128
v_add_f32 v51, v51, v128
v_mul_f32 v12, v12, v120
v_mul_f32 v13, v13, v121
v_mul_f32 v14, v14, v122
v_mul_f32 v15, v15, v123
v_mul_f32 v16, v16, v124
v_mul_f32 v17, v17, v125
v_mul_f32 v18, v18, v126
v_mul_f32 v19, v19, v127
v_mul_f32 v12, v12, v44
v_mul_f32 v13, v13, v45
v_mul_f32 v14, v14, v46
v_mul_f32 v15, v15, v47
v_mul_f32 v16, v16, v48
v_mul_f32 v17, v17, v49
v_mul_f32 v18, v18, v50
v_mul_f32 v19, v19, v51
CV 72,12,13
CV 73,14,15
CV 74,16,17
CV 75,18,19
buffer_store_dwordx4 v[72:75], v109, s[12:15], s38 offen offset:1024 sc1
s_add_u32 s38, s38, 28672
v_lshlrev_b32 v4, 16, v144
v_and_b32 v5, 0xffff0000, v144
v_lshlrev_b32 v6, 16, v145
v_and_b32 v7, 0xffff0000, v145
v_lshlrev_b32 v8, 16, v146
v_and_b32 v9, 0xffff0000, v146
v_lshlrev_b32 v10, 16, v147
v_and_b32 v11, 0xffff0000, v147
v_mul_f32 v120, v4, v4
v_mul_f32 v121, v5, v5
v_mul_f32 v122, v6, v6
v_mul_f32 v123, v7, v7
v_mul_f32 v124, v8, v8
v_mul_f32 v125, v9, v9
v_mul_f32 v126, v10, v10
v_mul_f32 v127, v11, v11
v_fma_f32 v120, v120, s40, v108
v_fma_f32 v121, v121, s40, v108
v_fma_f32 v122, v122, s40, v108
v_fma_f32 v123, v123, s40, v108
v_fma_f32 v124, v124, s40, v108
v_fma_f32 v125, v125, s40, v108
v_fma_f32 v126, v126, s40, v108
v_fma_f32 v127, v127, s40, v108
v_mul_f32 v120, v120, v4
v_mul_f32 v121, v121, v5
v_mul_f32 v122, v122, v6
v_mul_f32 v123, v123, v7
v_mul_f32 v124, v124, v8
v_mul_f32 v125, v125, v9
v_mul_f32 v126, v126, v10
v_mul_f32 v127, v127, v11
EX 120,120
EX 121,121
EX 122,122
EX 123,123
EX 124,124
EX 125,125
EX 126,126
EX 127,127
AR 36,16
AR 37,17
AR 38,18
AR 39,19
AR 40,20
AR 41,21
AR 42,22
AR 43,23
v_add_f32 v120, 1.0, v120
v_add_f32 v121, 1.0, v121
v_add_f32 v122, 1.0, v122
v_add_f32 v123, 1.0, v123
v_add_f32 v124, 1.0, v124
v_add_f32 v125, 1.0, v125
v_add_f32 v126, 1.0, v126
v_add_f32 v127, 1.0, v127
v_rcp_f32 v120, v120
v_rcp_f32 v121, v121
v_rcp_f32 v122, v122
v_rcp_f32 v123, v123
v_rcp_f32 v124, v124
v_rcp_f32 v125, v125
v_rcp_f32 v126, v126
v_rcp_f32 v127, v127
v_add_f32 v36, v36, v129
v_add_f32 v37, v37, v129
v_add_f32 v38, v38, v129
v_add_f32 v39, v39, v129
v_add_f32 v40, v40, v129
v_add_f32 v41, v41, v129
v_add_f32 v42, v42, v129
v_add_f32 v43, v43, v129
v_mul_f32 v4, v4, v120
v_mul_f32 v5, v5, v121
v_mul_f32 v6, v6, v122
v_mul_f32 v7, v7, v123
v_mul_f32 v8, v8, v124
v_mul_f32 v9, v9, v125
v_mul_f32 v10, v10, v126
v_mul_f32 v11, v11, v127
v_mul_f32 v4, v4, v36
v_mul_f32 v5, v5, v37
v_mul_f32 v6, v6, v38
v_mul_f32 v7, v7, v39
v_mul_f32 v8, v8, v40
v_mul_f32 v9, v9, v41
v_mul_f32 v10, v10, v42
v_mul_f32 v11, v11, v43
CV 76,4,5
CV 77,6,7
CV 78,8,9
CV 79,10,11
buffer_store_dwordx4 v[76:79], v109, s[12:15], s38 offen sc1
v_lshlrev_b32 v12, 16, v148
v_and_b32 v13, 0xffff0000, v148
v_lshlrev_b32 v14, 16, v149
v_and_b32 v15, 0xffff0000, v149
v_lshlrev_b32 v16, 16, v150
v_and_b32 v17, 0xffff0000, v150
v_lshlrev_b32 v18, 16, v151
v_and_b32 v19, 0xffff0000, v151
v_mul_f32 v120, v12, v12
v_mul_f32 v121, v13, v13
v_mul_f32 v122, v14, v14
v_mul_f32 v123, v15, v15
v_mul_f32 v124, v16, v16
v_mul_f32 v125, v17, v17
v_mul_f32 v126, v18, v18
v_mul_f32 v127, v19, v19
v_fma_f32 v120, v120, s40, v108
v_fma_f32 v121, v121, s40, v108
v_fma_f32 v122, v122, s40, v108
v_fma_f32 v123, v123, s40, v108
v_fma_f32 v124, v124, s40, v108
v_fma_f32 v125, v125, s40, v108
v_fma_f32 v126, v126, s40, v108
v_fma_f32 v127, v127, s40, v108
v_mul_f32 v120, v120, v12
v_mul_f32 v121, v121, v13
v_mul_f32 v122, v122, v14
v_mul_f32 v123, v123, v15
v_mul_f32 v124, v124, v16
v_mul_f32 v125, v125, v17
v_mul_f32 v126, v126, v18
v_mul_f32 v127, v127, v19
EX 120,120
EX 121,121
EX 122,122
EX 123,123
EX 124,124
EX 125,125
EX 126,126
EX 127,127
AR 44,24
AR 45,25
AR 46,26
AR 47,27
AR 48,28
AR 49,29
AR 50,30
AR 51,31
v_add_f32 v120, 1.0, v120
v_add_f32 v121, 1.0, v121
v_add_f32 v122, 1.0, v122
v_add_f32 v123, 1.0, v123
v_add_f32 v124, 1.0, v124
v_add_f32 v125, 1.0, v125
v_add_f32 v126, 1.0, v126
v_add_f32 v127, 1.0, v127
v_rcp_f32 v120, v120
v_rcp_f32 v121, v121
v_rcp_f32 v122, v122
v_rcp_f32 v123, v123
v_rcp_f32 v124, v124
v_rcp_f32 v125, v125
v_rcp_f32 v126, v126
v_rcp_f32 v127, v127
v_add_f32 v44, v44, v129
v_add_f32 v45, v45, v129
v_add_f32 v46, v46, v129
v_add_f32 v47, v47, v129
v_add_f32 v48, v48, v129
v_add_f32 v49, v49, v129
v_add_f32 v50, v50, v129
v_add_f32 v51, v51, v129
v_mul_f32 v12, v12, v120
v_mul_f32 v13, v13, v121
v_mul_f32 v14, v14, v122
v_mul_f32 v15, v15, v123
v_mul_f32 v16, v16, v124
v_mul_f32 v17, v17, v125
v_mul_f32 v18, v18, v126
v_mul_f32 v19, v19, v127
v_mul_f32 v12, v12, v44
v_mul_f32 v13, v13, v45
v_mul_f32 v14, v14, v46
v_mul_f32 v15, v15, v47
v_mul_f32 v16, v16, v48
v_mul_f32 v17, v17, v49
v_mul_f32 v18, v18, v50
v_mul_f32 v19, v19, v51
CV 80,12,13
CV 81,14,15
CV 82,16,17
CV 83,18,19
buffer_store_dwordx4 v[80:83], v109, s[12:15], s38 offen offset:1024 sc1
s_add_u32 s38, s38, 28672
v_lshlrev_b32 v4, 16, v152
v_and_b32 v5, 0xffff0000, v152
v_lshlrev_b32 v6, 16, v153
v_and_b32 v7, 0xffff0000, v153
v_lshlrev_b32 v8, 16, v154
v_and_b32 v9, 0xffff0000, v154
v_lshlrev_b32 v10, 16, v155
v_and_b32 v11, 0xffff0000, v155
v_mul_f32 v120, v4, v4
v_mul_f32 v121, v5, v5
v_mul_f32 v122, v6, v6
v_mul_f32 v123, v7, v7
v_mul_f32 v124, v8, v8
v_mul_f32 v125, v9, v9
v_mul_f32 v126, v10, v10
v_mul_f32 v127, v11, v11
v_fma_f32 v120, v120, s40, v108
v_fma_f32 v121, v121, s40, v108
v_fma_f32 v122, v122, s40, v108
v_fma_f32 v123, v123, s40, v108
v_fma_f32 v124, v124, s40, v108
v_fma_f32 v125, v125, s40, v108
v_fma_f32 v126, v126, s40, v108
v_fma_f32 v127, v127, s40, v108
v_mul_f32 v120, v120, v4
v_mul_f32 v121, v121, v5
v_mul_f32 v122, v122, v6
v_mul_f32 v123, v123, v7
v_mul_f32 v124, v124, v8
v_mul_f32 v125, v125, v9
v_mul_f32 v126, v126, v10
)ASM"
R"ASM(v_mul_f32 v127, v127, v11
EX 120,120
EX 121,121
EX 122,122
EX 123,123
EX 124,124
EX 125,125
EX 126,126
EX 127,127
AR 36,32
AR 37,33
AR 38,34
AR 39,35
AR 40,36
AR 41,37
AR 42,38
AR 43,39
v_add_f32 v120, 1.0, v120
v_add_f32 v121, 1.0, v121
v_add_f32 v122, 1.0, v122
v_add_f32 v123, 1.0, v123
v_add_f32 v124, 1.0, v124
v_add_f32 v125, 1.0, v125
v_add_f32 v126, 1.0, v126
v_add_f32 v127, 1.0, v127
v_rcp_f32 v120, v120
v_rcp_f32 v121, v121
v_rcp_f32 v122, v122
v_rcp_f32 v123, v123
v_rcp_f32 v124, v124
v_rcp_f32 v125, v125
v_rcp_f32 v126, v126
v_rcp_f32 v127, v127
v_add_f32 v36, v36, v130
v_add_f32 v37, v37, v130
v_add_f32 v38, v38, v130
v_add_f32 v39, v39, v130
v_add_f32 v40, v40, v130
v_add_f32 v41, v41, v130
v_add_f32 v42, v42, v130
v_add_f32 v43, v43, v130
v_mul_f32 v4, v4, v120
v_mul_f32 v5, v5, v121
v_mul_f32 v6, v6, v122
v_mul_f32 v7, v7, v123
v_mul_f32 v8, v8, v124
v_mul_f32 v9, v9, v125
v_mul_f32 v10, v10, v126
v_mul_f32 v11, v11, v127
v_mul_f32 v4, v4, v36
v_mul_f32 v5, v5, v37
v_mul_f32 v6, v6, v38
v_mul_f32 v7, v7, v39
v_mul_f32 v8, v8, v40
v_mul_f32 v9, v9, v41
v_mul_f32 v10, v10, v42
v_mul_f32 v11, v11, v43
CV 68,4,5
CV 69,6,7
CV 70,8,9
CV 71,10,11
buffer_store_dwordx4 v[68:71], v109, s[12:15], s38 offen sc1
v_lshlrev_b32 v12, 16, v156
v_and_b32 v13, 0xffff0000, v156
v_lshlrev_b32 v14, 16, v157
v_and_b32 v15, 0xffff0000, v157
v_lshlrev_b32 v16, 16, v158
v_and_b32 v17, 0xffff0000, v158
v_lshlrev_b32 v18, 16, v159
v_and_b32 v19, 0xffff0000, v159
v_mul_f32 v120, v12, v12
v_mul_f32 v121, v13, v13
v_mul_f32 v122, v14, v14
v_mul_f32 v123, v15, v15
v_mul_f32 v124, v16, v16
v_mul_f32 v125, v17, v17
v_mul_f32 v126, v18, v18
v_mul_f32 v127, v19, v19
v_fma_f32 v120, v120, s40, v108
v_fma_f32 v121, v121, s40, v108
v_fma_f32 v122, v122, s40, v108
v_fma_f32 v123, v123, s40, v108
v_fma_f32 v124, v124, s40, v108
v_fma_f32 v125, v125, s40, v108
v_fma_f32 v126, v126, s40, v108
v_fma_f32 v127, v127, s40, v108
v_mul_f32 v120, v120, v12
v_mul_f32 v121, v121, v13
v_mul_f32 v122, v122, v14
v_mul_f32 v123, v123, v15
v_mul_f32 v124, v124, v16
v_mul_f32 v125, v125, v17
v_mul_f32 v126, v126, v18
v_mul_f32 v127, v127, v19
EX 120,120
EX 121,121
EX 122,122
EX 123,123
EX 124,124
EX 125,125
EX 126,126
EX 127,127
AR 44,40
AR 45,41
AR 46,42
AR 47,43
AR 48,44
AR 49,45
AR 50,46
AR 51,47
v_add_f32 v120, 1.0, v120
v_add_f32 v121, 1.0, v121
v_add_f32 v122, 1.0, v122
v_add_f32 v123, 1.0, v123
v_add_f32 v124, 1.0, v124
v_add_f32 v125, 1.0, v125
v_add_f32 v126, 1.0, v126
v_add_f32 v127, 1.0, v127
v_rcp_f32 v120, v120
v_rcp_f32 v121, v121
v_rcp_f32 v122, v122
v_rcp_f32 v123, v123
v_rcp_f32 v124, v124
v_rcp_f32 v125, v125
v_rcp_f32 v126, v126
v_rcp_f32 v127, v127
v_add_f32 v44, v44, v130
v_add_f32 v45, v45, v130
v_add_f32 v46, v46, v130
v_add_f32 v47, v47, v130
v_add_f32 v48, v48, v130
v_add_f32 v49, v49, v130
v_add_f32 v50, v50, v130
v_add_f32 v51, v51, v130
v_mul_f32 v12, v12, v120
v_mul_f32 v13, v13, v121
v_mul_f32 v14, v14, v122
v_mul_f32 v15, v15, v123
v_mul_f32 v16, v16, v124
v_mul_f32 v17, v17, v125
v_mul_f32 v18, v18, v126
v_mul_f32 v19, v19, v127
v_mul_f32 v12, v12, v44
v_mul_f32 v13, v13, v45
v_mul_f32 v14, v14, v46
v_mul_f32 v15, v15, v47
v_mul_f32 v16, v16, v48
v_mul_f32 v17, v17, v49
v_mul_f32 v18, v18, v50
v_mul_f32 v19, v19, v51
CV 72,12,13
CV 73,14,15
CV 74,16,17
CV 75,18,19
buffer_store_dwordx4 v[72:75], v109, s[12:15], s38 offen offset:1024 sc1
s_add_u32 s38, s38, 28672
v_lshlrev_b32 v4, 16, v160
v_and_b32 v5, 0xffff0000, v160
v_lshlrev_b32 v6, 16, v161
v_and_b32 v7, 0xffff0000, v161
v_lshlrev_b32 v8, 16, v162
v_and_b32 v9, 0xffff0000, v162
v_lshlrev_b32 v10, 16, v163
v_and_b32 v11, 0xffff0000, v163
v_mul_f32 v120, v4, v4
v_mul_f32 v121, v5, v5
v_mul_f32 v122, v6, v6
v_mul_f32 v123, v7, v7
v_mul_f32 v124, v8, v8
v_mul_f32 v125, v9, v9
v_mul_f32 v126, v10, v10
v_mul_f32 v127, v11, v11
v_fma_f32 v120, v120, s40, v108
v_fma_f32 v121, v121, s40, v108
v_fma_f32 v122, v122, s40, v108
v_fma_f32 v123, v123, s40, v108
v_fma_f32 v124, v124, s40, v108
v_fma_f32 v125, v125, s40, v108
v_fma_f32 v126, v126, s40, v108
v_fma_f32 v127, v127, s40, v108
v_mul_f32 v120, v120, v4
v_mul_f32 v121, v121, v5
v_mul_f32 v122, v122, v6
v_mul_f32 v123, v123, v7
v_mul_f32 v124, v124, v8
v_mul_f32 v125, v125, v9
v_mul_f32 v126, v126, v10
v_mul_f32 v127, v127, v11
EX 120,120
EX 121,121
EX 122,122
EX 123,123
EX 124,124
EX 125,125
EX 126,126
EX 127,127
AR 36,48
AR 37,49
AR 38,50
AR 39,51
AR 40,52
AR 41,53
AR 42,54
AR 43,55
v_add_f32 v120, 1.0, v120
v_add_f32 v121, 1.0, v121
v_add_f32 v122, 1.0, v122
v_add_f32 v123, 1.0, v123
v_add_f32 v124, 1.0, v124
v_add_f32 v125, 1.0, v125
v_add_f32 v126, 1.0, v126
v_add_f32 v127, 1.0, v127
v_rcp_f32 v120, v120
v_rcp_f32 v121, v121
v_rcp_f32 v122, v122
v_rcp_f32 v123, v123
v_rcp_f32 v124, v124
v_rcp_f32 v125, v125
v_rcp_f32 v126, v126
v_rcp_f32 v127, v127
v_add_f32 v36, v36, v131
v_add_f32 v37, v37, v131
v_add_f32 v38, v38, v131
v_add_f32 v39, v39, v131
v_add_f32 v40, v40, v131
v_add_f32 v41, v41, v131
v_add_f32 v42, v42, v131
v_add_f32 v43, v43, v131
v_mul_f32 v4, v4, v120
v_mul_f32 v5, v5, v121
v_mul_f32 v6, v6, v122
v_mul_f32 v7, v7, v123
v_mul_f32 v8, v8, v124
v_mul_f32 v9, v9, v125
v_mul_f32 v10, v10, v126
v_mul_f32 v11, v11, v127
v_mul_f32 v4, v4, v36
v_mul_f32 v5, v5, v37
v_mul_f32 v6, v6, v38
v_mul_f32 v7, v7, v39
v_mul_f32 v8, v8, v40
v_mul_f32 v9, v9, v41
v_mul_f32 v10, v10, v42
v_mul_f32 v11, v11, v43
CV 76,4,5
CV 77,6,7
CV 78,8,9
CV 79,10,11
buffer_store_dwordx4 v[76:79], v109, s[12:15], s38 offen sc1
v_lshlrev_b32 v12, 16, v164
v_and_b32 v13, 0xffff0000, v164
v_lshlrev_b32 v14, 16, v165
v_and_b32 v15, 0xffff0000, v165
v_lshlrev_b32 v16, 16, v166
v_and_b32 v17, 0xffff0000, v166
v_lshlrev_b32 v18, 16, v167
v_and_b32 v19, 0xffff0000, v167
v_mul_f32 v120, v12, v12
v_mul_f32 v121, v13, v13
v_mul_f32 v122, v14, v14
v_mul_f32 v123, v15, v15
v_mul_f32 v124, v16, v16
v_mul_f32 v125, v17, v17
v_mul_f32 v126, v18, v18
v_mul_f32 v127, v19, v19
v_fma_f32 v120, v120, s40, v108
v_fma_f32 v121, v121, s40, v108
v_fma_f32 v122, v122, s40, v108
v_fma_f32 v123, v123, s40, v108
v_fma_f32 v124, v124, s40, v108
v_fma_f32 v125, v125, s40, v108
v_fma_f32 v126, v126, s40, v108
v_fma_f32 v127, v127, s40, v108
v_mul_f32 v120, v120, v12
v_mul_f32 v121, v121, v13
v_mul_f32 v122, v122, v14
v_mul_f32 v123, v123, v15
v_mul_f32 v124, v124, v16
v_mul_f32 v125, v125, v17
v_mul_f32 v126, v126, v18
v_mul_f32 v127, v127, v19
EX 120,120
EX 121,121
EX 122,122
EX 123,123
EX 124,124
EX 125,125
EX 126,126
EX 127,127
AR 44,56
AR 45,57
AR 46,58
AR 47,59
AR 48,60
AR 49,61
AR 50,62
AR 51,63
v_add_f32 v120, 1.0, v120
v_add_f32 v121, 1.0, v121
v_add_f32 v122, 1.0, v122
v_add_f32 v123, 1.0, v123
v_add_f32 v124, 1.0, v124
v_add_f32 v125, 1.0, v125
v_add_f32 v126, 1.0, v126
v_add_f32 v127, 1.0, v127
v_rcp_f32 v120, v120
v_rcp_f32 v121, v121
v_rcp_f32 v122, v122
v_rcp_f32 v123, v123
v_rcp_f32 v124, v124
v_rcp_f32 v125, v125
v_rcp_f32 v126, v126
v_rcp_f32 v127, v127
v_add_f32 v44, v44, v131
v_add_f32 v45, v45, v131
v_add_f32 v46, v46, v131
v_add_f32 v47, v47, v131
v_add_f32 v48, v48, v131
v_add_f32 v49, v49, v131
v_add_f32 v50, v50, v131
v_add_f32 v51, v51, v131
v_mul_f32 v12, v12, v120
v_mul_f32 v13, v13, v121
v_mul_f32 v14, v14, v122
v_mul_f32 v15, v15, v123
v_mul_f32 v16, v16, v124
v_mul_f32 v17, v17, v125
v_mul_f32 v18, v18, v126
v_mul_f32 v19, v19, v127
v_mul_f32 v12, v12, v44
v_mul_f32 v13, v13, v45
v_mul_f32 v14, v14, v46
v_mul_f32 v15, v15, v47
v_mul_f32 v16, v16, v48
v_mul_f32 v17, v17, v49
v_mul_f32 v18, v18, v50
v_mul_f32 v19, v19, v51
CV 80,12,13
CV 81,14,15
CV 82,16,17
CV 83,18,19
buffer_store_dwordx4 v[80:83], v109, s[12:15], s38 offen offset:1024 sc1
s_add_u32 s38, s38, 28672
v_lshlrev_b32 v4, 16, v168
v_and_b32 v5, 0xffff0000, v168
v_lshlrev_b32 v6, 16, v169
v_and_b32 v7, 0xffff0000, v169
v_lshlrev_b32 v8, 16, v170
v_and_b32 v9, 0xffff0000, v170
v_lshlrev_b32 v10, 16, v171
v_and_b32 v11, 0xffff0000, v171
v_mul_f32 v120, v4, v4
v_mul_f32 v121, v5, v5
v_mul_f32 v122, v6, v6
v_mul_f32 v123, v7, v7
v_mul_f32 v124, v8, v8
v_mul_f32 v125, v9, v9
v_mul_f32 v126, v10, v10
v_mul_f32 v127, v11, v11
v_fma_f32 v120, v120, s40, v108
v_fma_f32 v121, v121, s40, v108
v_fma_f32 v122, v122, s40, v108
v_fma_f32 v123, v123, s40, v108
v_fma_f32 v124, v124, s40, v108
v_fma_f32 v125, v125, s40, v108
v_fma_f32 v126, v126, s40, v108
v_fma_f32 v127, v127, s40, v108
v_mul_f32 v120, v120, v4
v_mul_f32 v121, v121, v5
v_mul_f32 v122, v122, v6
v_mul_f32 v123, v123, v7
v_mul_f32 v124, v124, v8
v_mul_f32 v125, v125, v9
v_mul_f32 v126, v126, v10
v_mul_f32 v127, v127, v11
EX 120,120
EX 121,121
EX 122,122
EX 123,123
EX 124,124
EX 125,125
EX 126,126
EX 127,127
AR 36,64
AR 37,65
AR 38,66
AR 39,67
AR 40,68
AR 41,69
AR 42,70
AR 43,71
v_add_f32 v120, 1.0, v120
v_add_f32 v121, 1.0, v121
v_add_f32 v122, 1.0, v122
v_add_f32 v123, 1.0, v123
v_add_f32 v124, 1.0, v124
v_add_f32 v125, 1.0, v125
v_add_f32 v126, 1.0, v126
v_add_f32 v127, 1.0, v127
v_rcp_f32 v120, v120
v_rcp_f32 v121, v121
v_rcp_f32 v122, v122
v_rcp_f32 v123, v123
v_rcp_f32 v124, v124
v_rcp_f32 v125, v125
v_rcp_f32 v126, v126
v_rcp_f32 v127, v127
v_add_f32 v36, v36, v132
v_add_f32 v37, v37, v132
v_add_f32 v38, v38, v132
v_add_f32 v39, v39, v132
v_add_f32 v40, v40, v132
v_add_f32 v41, v41, v132
v_add_f32 v42, v42, v132
v_add_f32 v43, v43, v132
v_mul_f32 v4, v4, v120
v_mul_f32 v5, v5, v121
v_mul_f32 v6, v6, v122
v_mul_f32 v7, v7, v123
v_mul_f32 v8, v8, v124
v_mul_f32 v9, v9, v125
v_mul_f32 v10, v10, v126
v_mul_f32 v11, v11, v127
v_mul_f32 v4, v4, v36
v_mul_f32 v5, v5, v37
v_mul_f32 v6, v6, v38
v_mul_f32 v7, v7, v39
v_mul_f32 v8, v8, v40
v_mul_f32 v9, v9, v41
v_mul_f32 v10, v10, v42
v_mul_f32 v11, v11, v43
CV 68,4,5
CV 69,6,7
CV 70,8,9
CV 71,10,11
buffer_store_dwordx4 v[68:71], v109, s[12:15], s38 offen sc1
v_lshlrev_b32 v12, 16, v172
v_and_b32 v13, 0xffff0000, v172
v_lshlrev_b32 v14, 16, v173
v_and_b32 v15, 0xffff0000, v173
v_lshlrev_b32 v16, 16, v174
v_and_b32 v17, 0xffff0000, v174
v_lshlrev_b32 v18, 16, v175
v_and_b32 v19, 0xffff0000, v175
v_mul_f32 v120, v12, v12
v_mul_f32 v121, v13, v13
v_mul_f32 v122, v14, v14
v_mul_f32 v123, v15, v15
v_mul_f32 v124, v16, v16
v_mul_f32 v125, v17, v17
v_mul_f32 v126, v18, v18
v_mul_f32 v127, v19, v19
v_fma_f32 v120, v120, s40, v108
v_fma_f32 v121, v121, s40, v108
v_fma_f32 v122, v122, s40, v108
v_fma_f32 v123, v123, s40, v108
v_fma_f32 v124, v124, s40, v108
v_fma_f32 v125, v125, s40, v108
v_fma_f32 v126, v126, s40, v108
v_fma_f32 v127, v127, s40, v108
v_mul_f32 v120, v120, v12
v_mul_f32 v121, v121, v13
v_mul_f32 v122, v122, v14
v_mul_f32 v123, v123, v15
v_mul_f32 v124, v124, v16
v_mul_f32 v125, v125, v17
v_mul_f32 v126, v126, v18
v_mul_f32 v127, v127, v19
EX 120,120
EX 121,121
EX 122,122
EX 123,123
EX 124,124
EX 125,125
EX 126,126
EX 127,127
AR 44,72
AR 45,73
AR 46,74
AR 47,75
AR 48,76
AR 49,77
AR 50,78
AR 51,79
v_add_f32 v120, 1.0, v120
v_add_f32 v121, 1.0, v121
v_add_f32 v122, 1.0, v122
v_add_f32 v123, 1.0, v123
v_add_f32 v124, 1.0, v124
v_add_f32 v125, 1.0, v125
v_add_f32 v126, 1.0, v126
v_add_f32 v127, 1.0, v127
v_rcp_f32 v120, v120
v_rcp_f32 v121, v121
v_rcp_f32 v122, v122
v_rcp_f32 v123, v123
v_rcp_f32 v124, v124
v_rcp_f32 v125, v125
v_rcp_f32 v126, v126
v_rcp_f32 v127, v127
v_add_f32 v44, v44, v132
v_add_f32 v45, v45, v132
v_add_f32 v46, v46, v132
v_add_f32 v47, v47, v132
v_add_f32 v48, v48, v132
v_add_f32 v49, v49, v132
v_add_f32 v50, v50, v132
v_add_f32 v51, v51, v132
v_mul_f32 v12, v12, v120
v_mul_f32 v13, v13, v121
v_mul_f32 v14, v14, v122
v_mul_f32 v15, v15, v123
v_mul_f32 v16, v16, v124
v_mul_f32 v17, v17, v125
v_mul_f32 v18, v18, v126
v_mul_f32 v19, v19, v127
v_mul_f32 v12, v12, v44
v_mul_f32 v13, v13, v45
v_mul_f32 v14, v14, v46
v_mul_f32 v15, v15, v47
v_mul_f32 v16, v16, v48
v_mul_f32 v17, v17, v49
v_mul_f32 v18, v18, v50
v_mul_f32 v19, v19, v51
CV 72,12,13
CV 73,14,15
CV 74,16,17
CV 75,18,19
buffer_store_dwordx4 v[72:75], v109, s[12:15], s38 offen offset:1024 sc1
s_add_u32 s38, s38, 28672
v_lshlrev_b32 v4, 16, v176
v_and_b32 v5, 0xffff0000, v176
v_lshlrev_b32 v6, 16, v177
v_and_b32 v7, 0xffff0000, v177
v_lshlrev_b32 v8, 16, v178
v_and_b32 v9, 0xffff0000, v178
v_lshlrev_b32 v10, 16, v179
v_and_b32 v11, 0xffff0000, v179
v_mul_f32 v120, v4, v4
v_mul_f32 v121, v5, v5
v_mul_f32 v122, v6, v6
v_mul_f32 v123, v7, v7
v_mul_f32 v124, v8, v8
v_mul_f32 v125, v9, v9
v_mul_f32 v126, v10, v10
v_mul_f32 v127, v11, v11
v_fma_f32 v120, v120, s40, v108
v_fma_f32 v121, v121, s40, v108
v_fma_f32 v122, v122, s40, v108
v_fma_f32 v123, v123, s40, v108
v_fma_f32 v124, v124, s40, v108
v_fma_f32 v125, v125, s40, v108
v_fma_f32 v126, v126, s40, v108
v_fma_f32 v127, v127, s40, v108
v_mul_f32 v120, v120, v4
v_mul_f32 v121, v121, v5
v_mul_f32 v122, v122, v6
v_mul_f32 v123, v123, v7
v_mul_f32 v124, v124, v8
v_mul_f32 v125, v125, v9
v_mul_f32 v126, v126, v10
v_mul_f32 v127, v127, v11
EX 120,120
EX 121,121
EX 122,122
EX 123,123
EX 124,124
EX 125,125
EX 126,126
EX 127,127
AR 36,80
AR 37,81
AR 38,82
AR 39,83
AR 40,84
AR 41,85
AR 42,86
AR 43,87
v_add_f32 v120, 1.0, v120
v_add_f32 v121, 1.0, v121
v_add_f32 v122, 1.0, v122
v_add_f32 v123, 1.0, v123
v_add_f32 v124, 1.0, v124
v_add_f32 v125, 1.0, v125
v_add_f32 v126, 1.0, v126
v_add_f32 v127, 1.0, v127
v_rcp_f32 v120, v120
v_rcp_f32 v121, v121
v_rcp_f32 v122, v122
v_rcp_f32 v123, v123
v_rcp_f32 v124, v124
v_rcp_f32 v125, v125
v_rcp_f32 v126, v126
v_rcp_f32 v127, v127
v_add_f32 v36, v36, v133
v_add_f32 v37, v37, v133
v_add_f32 v38, v38, v133
v_add_f32 v39, v39, v133
v_add_f32 v40, v40, v133
v_add_f32 v41, v41, v133
v_add_f32 v42, v42, v133
v_add_f32 v43, v43, v133
v_mul_f32 v4, v4, v120
v_mul_f32 v5, v5, v121
v_mul_f32 v6, v6, v122
v_mul_f32 v7, v7, v123
v_mul_f32 v8, v8, v124
v_mul_f32 v9, v9, v125
v_mul_f32 v10, v10, v126
v_mul_f32 v11, v11, v127
v_mul_f32 v4, v4, v36
v_mul_f32 v5, v5, v37
v_mul_f32 v6, v6, v38
v_mul_f32 v7, v7, v39
v_mul_f32 v8, v8, v40
v_mul_f32 v9, v9, v41
v_mul_f32 v10, v10, v42
v_mul_f32 v11, v11, v43
CV 76,4,5
CV 77,6,7
CV 78,8,9
CV 79,10,11
buffer_store_dwordx4 v[76:79], v109, s[12:15], s38 offen sc1
v_lshlrev_b32 v12, 16, v180
v_and_b32 v13, 0xffff0000, v180
v_lshlrev_b32 v14, 16, v181
v_and_b32 v15, 0xffff0000, v181
v_lshlrev_b32 v16, 16, v182
v_and_b32 v17, 0xffff0000, v182
v_lshlrev_b32 v18, 16, v183
v_and_b32 v19, 0xffff0000, v183
v_mul_f32 v120, v12, v12
v_mul_f32 v121, v13, v13
v_mul_f32 v122, v14, v14
v_mul_f32 v123, v15, v15
v_mul_f32 v124, v16, v16
v_mul_f32 v125, v17, v17
v_mul_f32 v126, v18, v18
v_mul_f32 v127, v19, v19
v_fma_f32 v120, v120, s40, v108
v_fma_f32 v121, v121, s40, v108
v_fma_f32 v122, v122, s40, v108
v_fma_f32 v123, v123, s40, v108
v_fma_f32 v124, v124, s40, v108
v_fma_f32 v125, v125, s40, v108
v_fma_f32 v126, v126, s40, v108
v_fma_f32 v127, v127, s40, v108
v_mul_f32 v120, v120, v12
v_mul_f32 v121, v121, v13
v_mul_f32 v122, v122, v14
v_mul_f32 v123, v123, v15
v_mul_f32 v124, v124, v16
v_mul_f32 v125, v125, v17
v_mul_f32 v126, v126, v18
v_mul_f32 v127, v127, v19
EX 120,120
EX 121,121
EX 122,122
EX 123,123
EX 124,124
EX 125,125
EX 126,126
EX 127,127
AR 44,88
AR 45,89
AR 46,90
AR 47,91
AR 48,92
AR 49,93
AR 50,94
AR 51,95
v_add_f32 v120, 1.0, v120
v_add_f32 v121, 1.0, v121
v_add_f32 v122, 1.0, v122
v_add_f32 v123, 1.0, v123
v_add_f32 v124, 1.0, v124
v_add_f32 v125, 1.0, v125
v_add_f32 v126, 1.0, v126
v_add_f32 v127, 1.0, v127
v_rcp_f32 v120, v120
v_rcp_f32 v121, v121
v_rcp_f32 v122, v122
v_rcp_f32 v123, v123
v_rcp_f32 v124, v124
v_rcp_f32 v125, v125
v_rcp_f32 v126, v126
v_rcp_f32 v127, v127
v_add_f32 v44, v44, v133
v_add_f32 v45, v45, v133
v_add_f32 v46, v46, v133
v_add_f32 v47, v47, v133
v_add_f32 v48, v48, v133
v_add_f32 v49, v49, v133
v_add_f32 v50, v50, v133
v_add_f32 v51, v51, v133
v_mul_f32 v12, v12, v120
v_mul_f32 v13, v13, v121
v_mul_f32 v14, v14, v122
v_mul_f32 v15, v15, v123
v_mul_f32 v16, v16, v124
v_mul_f32 v17, v17, v125
v_mul_f32 v18, v18, v126
v_mul_f32 v19, v19, v127
v_mul_f32 v12, v12, v44
v_mul_f32 v13, v13, v45
v_mul_f32 v14, v14, v46
v_mul_f32 v15, v15, v47
v_mul_f32 v16, v16, v48
v_mul_f32 v17, v17, v49
v_mul_f32 v18, v18, v50
v_mul_f32 v19, v19, v51
CV 80,12,13
CV 81,14,15
CV 82,16,17
CV 83,18,19
buffer_store_dwordx4 v[80:83], v109, s[12:15], s38 offen offset:1024 sc1
s_add_u32 s38, s38, 28672
v_lshlrev_b32 v4, 16, v184
v_and_b32 v5, 0xffff0000, v184
v_lshlrev_b32 v6, 16, v185
v_and_b32 v7, 0xffff0000, v185
v_lshlrev_b32 v8, 16, v186
v_and_b32 v9, 0xffff0000, v186
v_lshlrev_b32 v10, 16, v187
v_and_b32 v11, 0xffff0000, v187
v_mul_f32 v120, v4, v4
v_mul_f32 v121, v5, v5
v_mul_f32 v122, v6, v6
v_mul_f32 v123, v7, v7
v_mul_f32 v124, v8, v8
v_mul_f32 v125, v9, v9
v_mul_f32 v126, v10, v10
v_mul_f32 v127, v11, v11
v_fma_f32 v120, v120, s40, v108
v_fma_f32 v121, v121, s40, v108
v_fma_f32 v122, v122, s40, v108
v_fma_f32 v123, v123, s40, v108
v_fma_f32 v124, v124, s40, v108
v_fma_f32 v125, v125, s40, v108
v_fma_f32 v126, v126, s40, v108
v_fma_f32 v127, v127, s40, v108
v_mul_f32 v120, v120, v4
v_mul_f32 v121, v121, v5
v_mul_f32 v122, v122, v6
v_mul_f32 v123, v123, v7
v_mul_f32 v124, v124, v8
v_mul_f32 v125, v125, v9
v_mul_f32 v126, v126, v10
v_mul_f32 v127, v127, v11
EX 120,120
EX 121,121
EX 122,122
EX 123,123
EX 124,124
EX 125,125
EX 126,126
EX 127,127
AR 36,96
AR 37,97
AR 38,98
AR 39,99
AR 40,100
AR 41,101
AR 42,102
AR 43,103
v_add_f32 v120, 1.0, v120
v_add_f32 v121, 1.0, v121
v_add_f32 v122, 1.0, v122
v_add_f32 v123, 1.0, v123
v_add_f32 v124, 1.0, v124
v_add_f32 v125, 1.0, v125
v_add_f32 v126, 1.0, v126
v_add_f32 v127, 1.0, v127
v_rcp_f32 v120, v120
v_rcp_f32 v121, v121
v_rcp_f32 v122, v122
v_rcp_f32 v123, v123
v_rcp_f32 v124, v124
v_rcp_f32 v125, v125
v_rcp_f32 v126, v126
v_rcp_f32 v127, v127
v_add_f32 v36, v36, v134
v_add_f32 v37, v37, v134
v_add_f32 v38, v38, v134
v_add_f32 v39, v39, v134
v_add_f32 v40, v40, v134
v_add_f32 v41, v41, v134
v_add_f32 v42, v42, v134
v_add_f32 v43, v43, v134
v_mul_f32 v4, v4, v120
v_mul_f32 v5, v5, v121
v_mul_f32 v6, v6, v122
v_mul_f32 v7, v7, v123
v_mul_f32 v8, v8, v124
v_mul_f32 v9, v9, v125
v_mul_f32 v10, v10, v126
v_mul_f32 v11, v11, v127
v_mul_f32 v4, v4, v36
v_mul_f32 v5, v5, v37
v_mul_f32 v6, v6, v38
v_mul_f32 v7, v7, v39
v_mul_f32 v8, v8, v40
v_mul_f32 v9, v9, v41
v_mul_f32 v10, v10, v42
v_mul_f32 v11, v11, v43
CV 68,4,5
CV 69,6,7
CV 70,8,9
CV 71,10,11
buffer_store_dwordx4 v[68:71], v109, s[12:15], s38 offen sc1
v_lshlrev_b32 v12, 16, v188
v_and_b32 v13, 0xffff0000, v188
v_lshlrev_b32 v14, 16, v189
v_and_b32 v15, 0xffff0000, v189
v_lshlrev_b32 v16, 16, v190
v_and_b32 v17, 0xffff0000, v190
v_lshlrev_b32 v18, 16, v191
v_and_b32 v19, 0xffff0000, v191
v_mul_f32 v120, v12, v12
v_mul_f32 v121, v13, v13
v_mul_f32 v122, v14, v14
v_mul_f32 v123, v15, v15
v_mul_f32 v124, v16, v16
v_mul_f32 v125, v17, v17
v_mul_f32 v126, v18, v18
v_mul_f32 v127, v19, v19
v_fma_f32 v120, v120, s40, v108
v_fma_f32 v121, v121, s40, v108
v_fma_f32 v122, v122, s40, v108
v_fma_f32 v123, v123, s40, v108
v_fma_f32 v124, v124, s40, v108
v_fma_f32 v125, v125, s40, v108
v_fma_f32 v126, v126, s40, v108
v_fma_f32 v127, v127, s40, v108
v_mul_f32 v120, v120, v12
v_mul_f32 v121, v121, v13
v_mul_f32 v122, v122, v14
v_mul_f32 v123, v123, v15
v_mul_f32 v124, v124, v16
v_mul_f32 v125, v125, v17
v_mul_f32 v126, v126, v18
v_mul_f32 v127, v127, v19
EX 120,120
EX 121,121
EX 122,122
EX 123,123
EX 124,124
EX 125,125
EX 126,126
EX 127,127
AR 44,104
AR 45,105
AR 46,106
AR 47,107
AR 48,108
AR 49,109
AR 50,110
AR 51,111
v_add_f32 v120, 1.0, v120
v_add_f32 v121, 1.0, v121
v_add_f32 v122, 1.0, v122
v_add_f32 v123, 1.0, v123
v_add_f32 v124, 1.0, v124
v_add_f32 v125, 1.0, v125
v_add_f32 v126, 1.0, v126
v_add_f32 v127, 1.0, v127
v_rcp_f32 v120, v120
v_rcp_f32 v121, v121
v_rcp_f32 v122, v122
v_rcp_f32 v123, v123
v_rcp_f32 v124, v124
v_rcp_f32 v125, v125
v_rcp_f32 v126, v126
v_rcp_f32 v127, v127
v_add_f32 v44, v44, v134
v_add_f32 v45, v45, v134
v_add_f32 v46, v46, v134
v_add_f32 v47, v47, v134
v_add_f32 v48, v48, v134
v_add_f32 v49, v49, v134
v_add_f32 v50, v50, v134
v_add_f32 v51, v51, v134
v_mul_f32 v12, v12, v120
v_mul_f32 v13, v13, v121
v_mul_f32 v14, v14, v122
v_mul_f32 v15, v15, v123
v_mul_f32 v16, v16, v124
v_mul_f32 v17, v17, v125
v_mul_f32 v18, v18, v126
v_mul_f32 v19, v19, v127
v_mul_f32 v12, v12, v44
v_mul_f32 v13, v13, v45
v_mul_f32 v14, v14, v46
v_mul_f32 v15, v15, v47
v_mul_f32 v16, v16, v48
v_mul_f32 v17, v17, v49
v_mul_f32 v18, v18, v50
v_mul_f32 v19, v19, v51
CV 72,12,13
CV 73,14,15
CV 74,16,17
CV 75,18,19
buffer_store_dwordx4 v[72:75], v109, s[12:15], s38 offen offset:1024 sc1
s_add_u32 s38, s38, 28672
v_lshlrev_b32 v4, 16, v192
v_and_b32 v5, 0xffff0000, v192
v_lshlrev_b32 v6, 16, v193
v_and_b32 v7, 0xffff0000, v193
v_lshlrev_b32 v8, 16, v194
v_and_b32 v9, 0xffff0000, v194
v_lshlrev_b32 v10, 16, v195
v_and_b32 v11, 0xffff0000, v195
v_mul_f32 v120, v4, v4
v_mul_f32 v121, v5, v5
v_mul_f32 v122, v6, v6
v_mul_f32 v123, v7, v7
v_mul_f32 v124, v8, v8
v_mul_f32 v125, v9, v9
v_mul_f32 v126, v10, v10
v_mul_f32 v127, v11, v11
v_fma_f32 v120, v120, s40, v108
v_fma_f32 v121, v121, s40, v108
v_fma_f32 v122, v122, s40, v108
v_fma_f32 v123, v123, s40, v108
v_fma_f32 v124, v124, s40, v108
v_fma_f32 v125, v125, s40, v108
v_fma_f32 v126, v126, s40, v108
v_fma_f32 v127, v127, s40, v108
v_mul_f32 v120, v120, v4
v_mul_f32 v121, v121, v5
v_mul_f32 v122, v122, v6
v_mul_f32 v123, v123, v7
v_mul_f32 v124, v124, v8
v_mul_f32 v125, v125, v9
v_mul_f32 v126, v126, v10
v_mul_f32 v127, v127, v11
EX 120,120
EX 121,121
EX 122,122
EX 123,123
EX 124,124
EX 125,125
EX 126,126
EX 127,127
AR 36,112
AR 37,113
AR 38,114
AR 39,115
AR 40,116
AR 41,117
AR 42,118
AR 43,119
v_add_f32 v120, 1.0, v120
v_add_f32 v121, 1.0, v121
v_add_f32 v122, 1.0, v122
v_add_f32 v123, 1.0, v123
v_add_f32 v124, 1.0, v124
v_add_f32 v125, 1.0, v125
v_add_f32 v126, 1.0, v126
v_add_f32 v127, 1.0, v127
v_rcp_f32 v120, v120
v_rcp_f32 v121, v121
v_rcp_f32 v122, v122
v_rcp_f32 v123, v123
v_rcp_f32 v124, v124
v_rcp_f32 v125, v125
v_rcp_f32 v126, v126
v_rcp_f32 v127, v127
v_add_f32 v36, v36, v135
v_add_f32 v37, v37, v135
v_add_f32 v38, v38, v135
v_add_f32 v39, v39, v135
v_add_f32 v40, v40, v135
v_add_f32 v41, v41, v135
v_add_f32 v42, v42, v135
v_add_f32 v43, v43, v135
v_mul_f32 v4, v4, v120
v_mul_f32 v5, v5, v121
v_mul_f32 v6, v6, v122
v_mul_f32 v7, v7, v123
v_mul_f32 v8, v8, v124
v_mul_f32 v9, v9, v125
v_mul_f32 v10, v10, v126
v_mul_f32 v11, v11, v127
v_mul_f32 v4, v4, v36
v_mul_f32 v5, v5, v37
v_mul_f32 v6, v6, v38
v_mul_f32 v7, v7, v39
v_mul_f32 v8, v8, v40
v_mul_f32 v9, v9, v41
v_mul_f32 v10, v10, v42
v_mul_f32 v11, v11, v43
CV 76,4,5
CV 77,6,7
CV 78,8,9
CV 79,10,11
buffer_store_dwordx4 v[76:79], v109, s[12:15], s38 offen sc1
v_lshlrev_b32 v12, 16, v196
v_and_b32 v13, 0xffff0000, v196
v_lshlrev_b32 v14, 16, v197
v_and_b32 v15, 0xffff0000, v197
v_lshlrev_b32 v16, 16, v198
v_and_b32 v17, 0xffff0000, v198
v_lshlrev_b32 v18, 16, v199
v_and_b32 v19, 0xffff0000, v199
v_mul_f32 v120, v12, v12
v_mul_f32 v121, v13, v13
v_mul_f32 v122, v14, v14
v_mul_f32 v123, v15, v15
v_mul_f32 v124, v16, v16
v_mul_f32 v125, v17, v17
v_mul_f32 v126, v18, v18
v_mul_f32 v127, v19, v19
v_fma_f32 v120, v120, s40, v108
v_fma_f32 v121, v121, s40, v108
v_fma_f32 v122, v122, s40, v108
v_fma_f32 v123, v123, s40, v108
v_fma_f32 v124, v124, s40, v108
v_fma_f32 v125, v125, s40, v108
v_fma_f32 v126, v126, s40, v108
v_fma_f32 v127, v127, s40, v108
v_mul_f32 v120, v120, v12
v_mul_f32 v121, v121, v13
v_mul_f32 v122, v122, v14
v_mul_f32 v123, v123, v15
v_mul_f32 v124, v124, v16
v_mul_f32 v125, v125, v17
v_mul_f32 v126, v126, v18
v_mul_f32 v127, v127, v19
EX 120,120
EX 121,121
EX 122,122
EX 123,123
EX 124,124
EX 125,125
EX 126,126
EX 127,127
AR 44,120
AR 45,121
AR 46,122
AR 47,123
AR 48,124
AR 49,125
AR 50,126
AR 51,127
v_add_f32 v120, 1.0, v120
v_add_f32 v121, 1.0, v121
v_add_f32 v122, 1.0, v122
v_add_f32 v123, 1.0, v123
v_add_f32 v124, 1.0, v124
v_add_f32 v125, 1.0, v125
v_add_f32 v126, 1.0, v126
v_add_f32 v127, 1.0, v127
v_rcp_f32 v120, v120
v_rcp_f32 v121, v121
v_rcp_f32 v122, v122
v_rcp_f32 v123, v123
v_rcp_f32 v124, v124
v_rcp_f32 v125, v125
v_rcp_f32 v126, v126
v_rcp_f32 v127, v127
v_add_f32 v44, v44, v135
v_add_f32 v45, v45, v135
v_add_f32 v46, v46, v135
v_add_f32 v47, v47, v135
v_add_f32 v48, v48, v135
v_add_f32 v49, v49, v135
v_add_f32 v50, v50, v135
v_add_f32 v51, v51, v135
v_mul_f32 v12, v12, v120
v_mul_f32 v13, v13, v121
v_mul_f32 v14, v14, v122
v_mul_f32 v15, v15, v123
v_mul_f32 v16, v16, v124
v_mul_f32 v17, v17, v125
v_mul_f32 v18, v18, v126
v_mul_f32 v19, v19, v127
v_mul_f32 v12, v12, v44
v_mul_f32 v13, v13, v45
v_mul_f32 v14, v14, v46
v_mul_f32 v15, v15, v47
v_mul_f32 v16, v16, v48
v_mul_f32 v17, v17, v49
v_mul_f32 v18, v18, v50
v_mul_f32 v19, v19, v51
CV 80,12,13
CV 81,14,15
CV 82,16,17
CV 83,18,19
buffer_store_dwordx4 v[80:83], v109, s[12:15], s38 offen offset:1024 sc1
Lat_done:
s_waitcnt vmcnt(0) lgkmcnt(0)
v_mov_b32 %0, v1
.purgem MZaaa
.purgem MAaaa
.purgem MZaav
.purgem MAaav
.purgem MZava
.purgem MAava
.purgem MZavv
.purgem MAavv
.purgem MZvaa
.purgem MAvaa
.purgem MZvav
.purgem MAvav
.purgem MZvva
.purgem MAvva
.purgem MZvvv
.purgem MAvvv
.purgem Ra
.purgem Ta
.purgem Rv
.purgem Tv
.purgem AR
.purgem CV
.purgem EX
)ASM"
  : "+v"(tid) : "s"(ka), "s"(bid), "s"((unsigned)(size_t)lds) :
"v1", "v2", "v3", "v4", "v5", "v6", "v7", "v8", "v9", "v10", "v11", "v12", "v13", "v14", "v15", "v16", "v17", "v18", "v19", "v20", "v21", "v22", "v23", "v24",
"v25", "v26", "v27", "v28", "v29", "v30", "v31", "v32", "v33", "v34", "v35", "v36", "v37", "v38", "v39", "v40", "v41", "v42", "v43", "v44", "v45", "v46", "v47", "v48",
"v49", "v50", "v51", "v52", "v53", "v54", "v55", "v56", "v57", "v58", "v59", "v60", "v61", "v62", "v63", "v64", "v65", "v66", "v67", "v68", "v69", "v70", "v71", "v72",
"v73", "v74", "v75", "v76", "v77", "v78", "v79", "v80", "v81", "v82", "v83", "v84", "v85", "v86", "v87", "v88", "v89", "v90", "v91", "v92", "v93", "v94", "v95", "v96",
"v97", "v98", "v99", "v100", "v101", "v102", "v103", "v104", "v105", "v106", "v107", "v108", "v109", "v110", "v111", "v112", "v113", "v114", "v115", "v116", "v117", "v118", "v119", "v120",
"v121", "v122", "v123", "v124", "v125", "v126", "v127", "v128", "v129", "v130", "v131", "v132", "v133", "v134", "v135", "v136", "v137", "v138", "v139", "v140", "v141", "v142", "v143", "v144",
"v145", "v146", "v147", "v148", "v149", "v150", "v151", "v152", "v153", "v154", "v155", "v156", "v157", "v158", "v159", "v160", "v161", "v162", "v163", "v164", "v165", "v166", "v167", "v168",
"v169", "v170", "v171", "v172", "v173", "v174", "v175", "v176", "v177", "v178", "v179", "v180", "v181", "v182", "v183", "v184", "v185", "v186", "v187", "v188", "v189", "v190", "v191", "v192",
"v193", "v194", "v195", "v196", "v197", "v198", "v199", "v200", "v201", "v202", "v203", "v204", "v205", "v206", "v207", "v208", "v209", "v210", "v211", "v212", "v213", "v214", "v215", "v216",
"v217", "v218", "v219", "v220", "v221", "v222", "v223", "v224", "v225", "v226", "v227", "v228", "v229", "v230", "v231", "v232", "v233", "v234", "v235", "v236", "v237", "v238", "v239", "v240",
"v241", "v242", "v243", "v244", "v245", "v246", "v247", "v248", "v249", "v250", "v251", "v252", "v253", "v254", "v255", "a0", "a1", "a2", "a3", "a4", "a5", "a6", "a7", "a8",
"a9", "a10", "a11", "a12", "a13", "a14", "a15", "a16", "a17", "a18", "a19", "a20", "a21", "a22", "a23", "a24", "a25", "a26", "a27", "a28", "a29", "a30", "a31", "a32",
"a33", "a34", "a35", "a36", "a37", "a38", "a39", "a40", "a41", "a42", "a43", "a44", "a45", "a46", "a47", "a48", "a49", "a50", "a51", "a52", "a53", "a54", "a55", "a56",
"a57", "a58", "a59", "a60", "a61", "a62", "a63", "a64", "a65", "a66", "a67", "a68", "a69", "a70", "a71", "a72", "a73", "a74", "a75", "a76", "a77", "a78", "a79", "a80",
"a81", "a82", "a83", "a84", "a85", "a86", "a87", "a88", "a89", "a90", "a91", "a92", "a93", "a94", "a95", "a96", "a97", "a98", "a99", "a100", "a101", "a102", "a103", "a104",
"a105", "a106", "a107", "a108", "a109", "a110", "a111", "a112", "a113", "a114", "a115", "a116", "a117", "a118", "a119", "a120", "a121", "a122", "a123", "a124", "a125", "a126", "a127", "a128",
"a129", "a130", "a131", "a132", "a133", "a134", "a135", "a136", "a137", "a138", "a139", "a140", "a141", "a142", "a143", "a144", "a145", "a146", "a147", "a148", "a149", "a150", "a151", "a152",
"a153", "a154", "a155", "a156", "a157", "a158", "a159", "a160", "a161", "a162", "a163", "a164", "a165", "a166", "a167", "a168", "a169", "a170", "a171", "a172", "a173", "a174", "a175", "a176",
"a177", "a178", "a179", "a180", "a181", "a182", "a183", "a184", "a185", "a186", "a187", "a188", "a189", "a190", "a191", "a192", "a193", "a194", "a195", "a196", "a197", "a198", "a199", "a200",
"a201", "a202", "a203", "a204", "a205", "a206", "a207", "a208", "a209", "a210", "a211", "a212", "a213", "a214", "a215", "a216", "a217", "a218", "a219", "a220", "a221", "a222", "a223", "a224",
"a225", "a226", "a227", "a228", "a229", "a230", "a231", "a232", "a233", "a234", "a235", "a236", "a237", "a238", "a239", "a240", "a241", "a242", "a243", "a244", "a245", "a246", "a247", "a248",
"a249", "a250", "a251", "a252", "a253", "a254", "a255", "s8", "s9", "s10", "s11", "s12", "s13", "s14", "s15", "s16", "s17", "s18", "s19", "s20", "s21", "s22", "s23", "s24",
"s25", "s26", "s27", "s28", "s29", "s30", "s31", "s34", "s35", "s36", "s37", "s38", "s39", "s40", "s41", "s42", "s43", "s44", "s45", "s46", "s47", "s48", "s49", "s50",
"s51", "s52", "s53", "s54", "s55", "s56", "s57", "s58", "s59", "s60", "s61", "s62", "s63", "s64", "s65", "s66", "s67", "s68", "s69", "s70", "s71", "s72", "s73", "s74",
"s75", "s76", "s77", "s78", "s79", "s80", "s81", "s82", "s83", "s84", "s85", "s86", "s87", "s88", "s89", "s90", "s91", "s92", "s93", "s94", "s95", "s96", "s97", "s98",
"s99", "vcc", "scc", "memory"
  );

}
__global__ __launch_bounds__(256, 1) void k3_out(K3Args p) {
  __shared__ __attribute__((aligned(16))) unsigned char lds[163840];

  unsigned tid = threadIdx.x;
  const __attribute__((address_space(4))) void* ka = (const __attribute__((address_space(4))) void*)__builtin_amdgcn_kernarg_segment_ptr();
  unsigned bid = blockIdx.x;
  asm volatile(
R"ASM(.macro MZaaa d,a,b
v_mfma_f32_16x16x32_bf16 a[\d:\d+3], a[\a:\a+3], a[\b:\b+3], 0
.endm
.macro MAaaa d,a,b
v_mfma_f32_16x16x32_bf16 a[\d:\d+3], a[\a:\a+3], a[\b:\b+3], a[\d:\d+3]
.endm
.macro MZaav d,a,b
v_mfma_f32_16x16x32_bf16 a[\d:\d+3], a[\a:\a+3], v[\b:\b+3], 0
.endm
.macro MAaav d,a,b
v_mfma_f32_16x16x32_bf16 a[\d:\d+3], a[\a:\a+3], v[\b:\b+3], a[\d:\d+3]
.endm
.macro MZava d,a,b
v_mfma_f32_16x16x32_bf16 a[\d:\d+3], v[\a:\a+3], a[\b:\b+3], 0
.endm
.macro MAava d,a,b
v_mfma_f32_16x16x32_bf16 a[\d:\d+3], v[\a:\a+3], a[\b:\b+3], a[\d:\d+3]
.endm
.macro MZavv d,a,b
v_mfma_f32_16x16x32_bf16 a[\d:\d+3], v[\a:\a+3], v[\b:\b+3], 0
.endm
.macro MAavv d,a,b
v_mfma_f32_16x16x32_bf16 a[\d:\d+3], v[\a:\a+3], v[\b:\b+3], a[\d:\d+3]
.endm
.macro MZvaa d,a,b
v_mfma_f32_16x16x32_bf16 v[\d:\d+3], a[\a:\a+3], a[\b:\b+3], 0
.endm
.macro MAvaa d,a,b
v_mfma_f32_16x16x32_bf16 v[\d:\d+3], a[\a:\a+3], a[\b:\b+3], v[\d:\d+3]
.endm
.macro MZvav d,a,b
v_mfma_f32_16x16x32_bf16 v[\d:\d+3], a[\a:\a+3], v[\b:\b+3], 0
.endm
.macro MAvav d,a,b
v_mfma_f32_16x16x32_bf16 v[\d:\d+3], a[\a:\a+3], v[\b:\b+3], v[\d:\d+3]
.endm
.macro MZvva d,a,b
v_mfma_f32_16x16x32_bf16 v[\d:\d+3], v[\a:\a+3], a[\b:\b+3], 0
.endm
.macro MAvva d,a,b
v_mfma_f32_16x16x32_bf16 v[\d:\d+3], v[\a:\a+3], a[\b:\b+3], v[\d:\d+3]
.endm
.macro MZvvv d,a,b
v_mfma_f32_16x16x32_bf16 v[\d:\d+3], v[\a:\a+3], v[\b:\b+3], 0
.endm
.macro MAvvv d,a,b
v_mfma_f32_16x16x32_bf16 v[\d:\d+3], v[\a:\a+3], v[\b:\b+3], v[\d:\d+3]
.endm
.macro Ra d,a,o
ds_read_b128 a[\d:\d+3], v\a offset:\o
.endm
.macro Ta d,a,o
ds_read_b64_tr_b16 a[\d:\d+1], v\a offset:\o
.endm
.macro Rv d,a,o
ds_read_b128 v[\d:\d+3], v\a offset:\o
.endm
.macro Tv d,a,o
ds_read_b64_tr_b16 v[\d:\d+1], v\a offset:\o
.endm
.macro AR d,a
v_accvgpr_read_b32 v\d, a\a
.endm
.macro CV d,a,b
v_cvt_pk_bf16_f32 v\d, v\a, v\b
.endm
.macro EX d,a
v_exp_f32 v\d, v\a
.endm
v_mov_b32 v1, %0
s_mov_b32 s29, %2
s_load_dwordx4 s[8:11], %1, 0x0
v_lshrrev_b32 v158, 6, v1
s_nop 0
v_readfirstlane_b32 s28, v158
v_and_b32 v158, 63, v1
v_lshlrev_b32 v154, 4, v158
s_and_b32 s34, s29, 3
s_bfe_u32 s31, s29, 131075
s_lshr_b32 s42, s29, 5
s_bfe_u32 s30, s29, 65538
s_lshl_b32 s30, s30, 3
s_add_u32 s30, s30, s42
s_lshl_b32 s35, s30, 2
s_add_u32 s35, s35, s31
s_lshl_b32 s42, s28, 1
s_add_u32 s43, s42, 0
s_lshl_b32 s44, s43, 17
s_sub_u32 s44, s44, 0
v_add_u32 v148, s44, v154
s_mul_i32 s44, s43, 28672
s_sub_u32 s44, s44, 0
v_add_u32 v150, s44, v154
s_lshl_b32 s44, s43, 10
s_sub_u32 s44, s44, 0
s_cmp_eq_u32 s43, 7
s_cselect_b32 s44, 0x7ff00000, s44
v_add_u32 v152, s44, v154
s_add_u32 s43, s42, 1
s_lshl_b32 s44, s43, 17
s_sub_u32 s44, s44, 1024
v_add_u32 v149, s44, v154
s_mul_i32 s44, s43, 28672
s_sub_u32 s44, s44, 1024
v_add_u32 v151, s44, v154
s_lshl_b32 s44, s43, 10
s_sub_u32 s44, s44, 1024
s_cmp_eq_u32 s43, 7
s_cselect_b32 s44, 0x7ff00000, s44
v_add_u32 v153, s44, v154
s_lshl_b32 s39, s28, 11
s_mul_i32 s37, s34, 28672
s_mul_i32 s42, s31, 7168
s_add_u32 s37, s37, s42
s_lshl_b32 s36, s30, 20
s_lshl_b32 s38, s34, 10
s_add_u32 s36, s36, s38
s_add_u32 s36, s36, 50331648
s_waitcnt lgkmcnt(0)
s_mov_b32 s12, s8
s_mov_b32 s13, s9
s_mov_b32 s14, 130023424
s_mov_b32 s15, 0x20000
s_add_u32 s20, s8, 104857600
s_addc_u32 s21, s9, 0
s_mov_b32 s22, 4472832
s_mov_b32 s23, 0x20000
s_mul_i32 s42, s30, 229376
s_add_u32 s42, s42, 79691776
s_add_u32 s24, s8, s42
s_addc_u32 s25, s9, 0
s_mov_b32 s26, 229376
s_mov_b32 s27, 0x20000
s_add_u32 m0, s39, 0
s_nop 0
buffer_load_dwordx4 v148, s[12:15], s36 offen lds
buffer_load_dwordx4 v149, s[12:15], s36 offen offset:1024 lds
s_add_u32 s36, s36, 4096
s_add_u32 m0, s39, 8192
s_nop 0
buffer_load_dwordx4 v152, s[20:23], s37 offen lds
buffer_load_dwordx4 v153, s[20:23], s37 offen offset:1024 lds
s_add_u32 s37, s37, 114688
s_add_u32 m0, s39, 16384
s_nop 0
buffer_load_dwordx4 v148, s[12:15], s36 offen lds
buffer_load_dwordx4 v149, s[12:15], s36 offen offset:1024 lds
s_add_u32 s36, s36, 4096
s_add_u32 m0, s39, 24576
s_nop 0
buffer_load_dwordx4 v152, s[20:23], s37 offen lds
buffer_load_dwordx4 v153, s[20:23], s37 offen offset:1024 lds
s_add_u32 s37, s37, 114688
s_add_u32 m0, s39, 32768
s_nop 0
buffer_load_dwordx4 v148, s[12:15], s36 offen lds
buffer_load_dwordx4 v149, s[12:15], s36 offen offset:1024 lds
s_add_u32 s36, s36, 4096
s_add_u32 m0, s39, 40960
s_nop 0
buffer_load_dwordx4 v152, s[20:23], s37 offen lds
buffer_load_dwordx4 v153, s[20:23], s37 offen offset:1024 lds
s_add_u32 s37, s37, 114688
s_add_u32 m0, s39, 49152
s_nop 0
buffer_load_dwordx4 v148, s[12:15], s36 offen lds
buffer_load_dwordx4 v149, s[12:15], s36 offen offset:1024 lds
s_add_u32 s36, s36, 4096
s_add_u32 m0, s39, 57344
s_nop 0
buffer_load_dwordx4 v152, s[20:23], s37 offen lds
buffer_load_dwordx4 v153, s[20:23], s37 offen offset:1024 lds
s_add_u32 s37, s37, 114688
s_add_u32 m0, s39, 65536
s_nop 0
buffer_load_dwordx4 v148, s[12:15], s36 offen lds
buffer_load_dwordx4 v149, s[12:15], s36 offen offset:1024 lds
s_add_u32 s36, s36, 4096
s_add_u32 m0, s39, 73728
s_nop 0
buffer_load_dwordx4 v152, s[20:23], s37 offen lds
buffer_load_dwordx4 v153, s[20:23], s37 offen offset:1024 lds
s_add_u32 s37, s37, 114688
s_add_u32 m0, s39, 81920
s_nop 0
buffer_load_dwordx4 v148, s[12:15], s36 offen lds
buffer_load_dwordx4 v149, s[12:15], s36 offen offset:1024 lds
s_add_u32 s36, s36, 4096
s_add_u32 m0, s39, 90112
s_nop 0
buffer_load_dwordx4 v152, s[20:23], s37 offen lds
buffer_load_dwordx4 v153, s[20:23], s37 offen offset:1024 lds
s_add_u32 s37, s37, 114688
s_mov_b32 s16, s10
s_mov_b32 s17, s11
s_mov_b32 s18, 1835008
s_mov_b32 s19, 0x20000
s_lshl_b32 s42, s28, 11
v_add_u32 v155, s42, v154
v_mov_b32 v156, v154
v_add_u32 v168, s42, v154
v_mov_b32 v170, v154
v_add_u32 v168, 65536, v168
v_add_u32 v170, 65536, v170
v_add_u32 v169, s42, v154
v_mov_b32 v171, v154
v_add_u32 v169, 131072, v169
v_add_u32 v171, 131072, v171
s_lshl_b32 s42, s30, 9
s_lshl_b32 s43, s28, 7
s_add_u32 s42, s42, s43
s_add_u32 s42, s42, 83886080
v_and_b32 v159, 0xf0, v154
v_lshrrev_b32 v159, 2, v159
buffer_load_dword v166, v159, s[12:15], s42 offen
buffer_load_dword v167, v159, s[12:15], s42 offen offset:64
s_waitcnt vmcnt(18)
s_barrier
Rv 4,155,0
Rv 8,155,1024
Rv 12,156,8192
Rv 16,156,9216
Rv 20,156,10240
Rv 24,156,11264
Rv 28,156,12288
Rv 32,156,13312
Rv 36,156,14336
Rv 40,155,16384
Rv 44,155,17408
Rv 48,156,24576
Rv 52,156,25600
Rv 56,156,26624
Rv 60,156,27648
Rv 64,156,28672
Rv 68,156,29696
Rv 72,156,30720
s_waitcnt vmcnt(10) lgkmcnt(0)
s_barrier
MZavv 0,12,4
s_add_u32 m0, s39, 98304
Rv 76,155,32768
MZavv 4,16,4
buffer_load_dwordx4 v148, s[12:15], s36 offen lds
MZavv 8,20,4
buffer_load_dwordx4 v149, s[12:15], s36 offen offset:1024 lds
s_add_u32 s36, s36, 4096
s_add_u32 m0, s39, 106496
MZavv 12,24,4
buffer_load_dwordx4 v152, s[20:23], s37 offen lds
MZavv 16,28,4
buffer_load_dwordx4 v153, s[20:23], s37 offen offset:1024 lds
s_add_u32 s37, s37, 114688
s_add_u32 m0, s39, 114688
MZavv 20,32,4
buffer_load_dwordx4 v148, s[12:15], s36 offen lds
MZavv 24,36,4
buffer_load_dwordx4 v149, s[12:15], s36 offen offset:1024 lds
s_add_u32 s36, s36, 4096
s_add_u32 m0, s39, 122880
MZavv 28,12,8
buffer_load_dwordx4 v152, s[20:23], s37 offen lds
MZavv 32,16,8
buffer_load_dwordx4 v153, s[20:23], s37 offen offset:1024 lds
s_add_u32 s37, s37, 114688
s_add_u32 m0, s39, 131072
MZavv 36,20,8
buffer_load_dwordx4 v148, s[12:15], s36 offen lds
MZavv 40,24,8
buffer_load_dwordx4 v149, s[12:15], s36 offen offset:1024 lds
s_add_u32 s36, s36, 4096
s_add_u32 m0, s39, 139264
MZavv 44,28,8
buffer_load_dwordx4 v152, s[20:23], s37 offen lds
MZavv 48,32,8
buffer_load_dwordx4 v153, s[20:23], s37 offen offset:1024 lds
s_add_u32 s37, s37, 114688
s_add_u32 m0, s39, 147456
MZavv 52,36,8
buffer_load_dwordx4 v148, s[12:15], s36 offen lds
MAavv 0,48,40
Rv 80,155,33792
MAavv 4,52,40
Rv 84,156,40960
MAavv 8,56,40
Rv 88,156,41984
MAavv 12,60,40
Rv 92,156,43008
MAavv 16,64,40
Rv 96,156,44032
MAavv 20,68,40
Rv 100,156,45056
MAavv 24,72,40
Rv 104,156,46080
MAavv 28,48,44
Rv 108,156,47104
MAavv 32,52,44
Rv 112,155,49152
MAavv 36,56,44
Rv 116,155,50176
MAavv 40,60,44
Rv 120,156,57344
Rv 124,156,58368
Rv 128,156,59392
Rv 132,156,60416
Rv 136,156,61440
Rv 140,156,62464
Rv 144,156,63488
MAavv 44,64,44
buffer_load_dwordx4 v149, s[12:15], s36 offen offset:1024 lds
s_add_u32 s36, s36, 4096
s_add_u32 m0, s39, 155648
MAavv 48,68,44
buffer_load_dwordx4 v152, s[20:23], s37 offen lds
MAavv 52,72,44
buffer_load_dwordx4 v153, s[20:23], s37 offen offset:1024 lds
s_add_u32 s37, s37, 114688
s_waitcnt vmcnt(18) lgkmcnt(0)
s_barrier
MAavv 0,84,76
s_add_u32 m0, s39, 0
Rv 4,168,0
MAavv 4,88,76
buffer_load_dwordx4 v148, s[12:15], s36 offen lds
MAavv 8,92,76
buffer_load_dwordx4 v149, s[12:15], s36 offen offset:1024 lds
s_add_u32 s36, s36, 4096
s_add_u32 m0, s39, 8192
MAavv 12,96,76
buffer_load_dwordx4 v152, s[20:23], s37 offen lds
MAavv 16,100,76
buffer_load_dwordx4 v153, s[20:23], s37 offen offset:1024 lds
s_add_u32 s37, s37, 114688
s_add_u32 m0, s39, 16384
MAavv 20,104,76
buffer_load_dwordx4 v148, s[12:15], s36 offen lds
MAavv 24,108,76
buffer_load_dwordx4 v149, s[12:15], s36 offen offset:1024 lds
s_add_u32 s36, s36, 4096
s_add_u32 m0, s39, 24576
MAavv 28,84,80
buffer_load_dwordx4 v152, s[20:23], s37 offen lds
MAavv 32,88,80
buffer_load_dwordx4 v153, s[20:23], s37 offen offset:1024 lds
s_add_u32 s37, s37, 114688
s_add_u32 m0, s39, 32768
MAavv 36,92,80
buffer_load_dwordx4 v148, s[12:15], s36 offen lds
MAavv 40,96,80
buffer_load_dwordx4 v149, s[12:15], s36 offen offset:1024 lds
s_add_u32 s36, s36, 4096
s_add_u32 m0, s39, 40960
MAavv 44,100,80
buffer_load_dwordx4 v152, s[20:23], s37 offen lds
MAavv 48,104,80
buffer_load_dwordx4 v153, s[20:23], s37 offen offset:1024 lds
s_add_u32 s37, s37, 114688
s_add_u32 m0, s39, 49152
MAavv 52,108,80
buffer_load_dwordx4 v148, s[12:15], s36 offen lds
MAavv 0,120,112
Rv 8,168,1024
MAavv 4,124,112
Rv 12,170,8192
MAavv 8,128,112
Rv 16,170,9216
MAavv 12,132,112
Rv 20,170,10240
MAavv 16,136,112
Rv 24,170,11264
MAavv 20,140,112
Rv 28,170,12288
MAavv 24,144,112
Rv 32,170,13312
MAavv 28,120,116
Rv 36,170,14336
MAavv 32,124,116
Rv 40,168,16384
MAavv 36,128,116
Rv 44,168,17408
MAavv 40,132,116
Rv 48,170,24576
Rv 52,170,25600
Rv 56,170,26624
Rv 60,170,27648
Rv 64,170,28672
Rv 68,170,29696
Rv 72,170,30720
MAavv 44,136,116
buffer_load_dwordx4 v149, s[12:15], s36 offen offset:1024 lds
s_add_u32 s36, s36, 4096
s_add_u32 m0, s39, 57344
MAavv 48,140,116
buffer_load_dwordx4 v152, s[20:23], s37 offen lds
MAavv 52,144,116
buffer_load_dwordx4 v153, s[20:23], s37 offen offset:1024 lds
s_add_u32 s37, s37, 114688
s_waitcnt vmcnt(24) lgkmcnt(0)
s_barrier
MAavv 0,12,4
s_add_u32 m0, s39, 65536
Rv 76,168,32768
MAavv 4,16,4
buffer_load_dwordx4 v148, s[12:15], s36 offen lds
MAavv 8,20,4
buffer_load_dwordx4 v149, s[12:15], s36 offen offset:1024 lds
s_add_u32 s36, s36, 4096
s_add_u32 m0, s39, 73728
MAavv 12,24,4
buffer_load_dwordx4 v152, s[20:23], s37 offen lds
MAavv 16,28,4
buffer_load_dwordx4 v153, s[20:23], s37 offen offset:1024 lds
s_add_u32 s37, s37, 114688
s_add_u32 m0, s39, 81920
MAavv 20,32,4
buffer_load_dwordx4 v148, s[12:15], s36 offen lds
MAavv 24,36,4
Rv 80,168,33792
MAavv 28,12,8
Rv 84,170,40960
MAavv 32,16,8
Rv 88,170,41984
MAavv 36,20,8
Rv 92,170,43008
MAavv 40,24,8
Rv 96,170,44032
MAavv 44,28,8
Rv 100,170,45056
MAavv 48,32,8
Rv 104,170,46080
MAavv 52,36,8
Rv 108,170,47104
MAavv 0,48,40
Rv 112,168,49152
MAavv 4,52,40
Rv 116,168,50176
MAavv 8,56,40
Rv 120,170,57344
MAavv 12,60,40
Rv 124,170,58368
MAavv 16,64,40
Rv 128,170,59392
MAavv 20,68,40
Rv 132,170,60416
MAavv 24,72,40
Rv 136,170,61440
MAavv 28,48,44
Rv 140,170,62464
MAavv 32,52,44
Rv 144,170,63488
MAavv 36,56,44
buffer_load_dwordx4 v149, s[12:15], s36 offen offset:1024 lds
s_add_u32 s36, s36, 4096
s_add_u32 m0, s39, 90112
MAavv 40,60,44
buffer_load_dwordx4 v152, s[20:23], s37 offen lds
MAavv 44,64,44
buffer_load_dwordx4 v153, s[20:23], s37 offen offset:1024 lds
s_add_u32 s37, s37, 114688
MAavv 48,68,44
MAavv 52,72,44
s_waitcnt vmcnt(24) lgkmcnt(0)
s_barrier
MAavv 0,84,76
s_add_u32 m0, s39, 98304
Rv 4,169,0
MAavv 4,88,76
buffer_load_dwordx4 v148, s[12:15], s36 offen lds
MAavv 8,92,76
buffer_load_dwordx4 v149, s[12:15], s36 offen offset:1024 lds
s_add_u32 s36, s36, 4096
s_add_u32 m0, s39, 106496
MAavv 12,96,76
buffer_load_dwordx4 v152, s[20:23], s37 offen lds
MAavv 16,100,76
buffer_load_dwordx4 v153, s[20:23], s37 offen offset:1024 lds
s_add_u32 s37, s37, 114688
s_add_u32 m0, s39, 114688
MAavv 20,104,76
buffer_load_dwordx4 v148, s[12:15], s36 offen lds
MAavv 24,108,76
Rv 8,169,1024
MAavv 28,84,80
Rv 12,171,8192
MAavv 32,88,80
Rv 16,171,9216
MAavv 36,92,80
Rv 20,171,10240
MAavv 40,96,80
Rv 24,171,11264
MAavv 44,100,80
Rv 28,171,12288
MAavv 48,104,80
Rv 32,171,13312
MAavv 52,108,80
Rv 36,171,14336
MAavv 0,120,112
Rv 40,169,16384
MAavv 4,124,112
Rv 44,169,17408
MAavv 8,128,112
Rv 48,171,24576
MAavv 12,132,112
Rv 52,171,25600
MAavv 16,136,112
Rv 56,171,26624
MAavv 20,140,112
Rv 60,171,27648
MAavv 24,144,112
Rv 64,171,28672
MAavv 28,120,116
Rv 68,171,29696
MAavv 32,124,116
Rv 72,171,30720
MAavv 36,128,116
buffer_load_dwordx4 v149, s[12:15], s36 offen offset:1024 lds
s_add_u32 s36, s36, 4096
s_add_u32 m0, s39, 122880
MAavv 40,132,116
buffer_load_dwordx4 v152, s[20:23], s37 offen lds
MAavv 44,136,116
buffer_load_dwordx4 v153, s[20:23], s37 offen offset:1024 lds
s_add_u32 s37, s37, 114688
MAavv 48,140,116
MAavv 52,144,116
s_waitcnt vmcnt(24) lgkmcnt(0)
s_barrier
MAavv 0,12,4
s_add_u32 m0, s39, 131072
Rv 76,155,0
MAavv 4,16,4
buffer_load_dwordx4 v148, s[12:15], s36 offen lds
MAavv 8,20,4
buffer_load_dwordx4 v149, s[12:15], s36 offen offset:1024 lds
s_add_u32 s36, s36, 4096
s_add_u32 m0, s39, 139264
MAavv 12,24,4
buffer_load_dwordx4 v152, s[20:23], s37 offen lds
MAavv 16,28,4
buffer_load_dwordx4 v153, s[20:23], s37 offen offset:1024 lds
s_add_u32 s37, s37, 114688
s_add_u32 m0, s39, 147456
MAavv 20,32,4
buffer_load_dwordx4 v148, s[12:15], s36 offen lds
MAavv 24,36,4
Rv 80,155,1024
MAavv 28,12,8
Rv 84,156,8192
MAavv 32,16,8
Rv 88,156,9216
MAavv 36,20,8
Rv 92,156,10240
MAavv 40,24,8
Rv 96,156,11264
MAavv 44,28,8
Rv 100,156,12288
MAavv 48,32,8
Rv 104,156,13312
MAavv 52,36,8
Rv 108,156,14336
MAavv 0,48,40
Rv 112,155,16384
MAavv 4,52,40
Rv 116,155,17408
MAavv 8,56,40
Rv 120,156,24576
MAavv 12,60,40
Rv 124,156,25600
MAavv 16,64,40
Rv 128,156,26624
MAavv 20,68,40
Rv 132,156,27648
MAavv 24,72,40
Rv 136,156,28672
MAavv 28,48,44
Rv 140,156,29696
MAavv 32,52,44
Rv 144,156,30720
MAavv 36,56,44
buffer_load_dwordx4 v149, s[12:15], s36 offen offset:1024 lds
s_add_u32 s36, s36, 4096
s_add_u32 m0, s39, 155648
MAavv 40,60,44
buffer_load_dwordx4 v152, s[20:23], s37 offen lds
MAavv 44,64,44
buffer_load_dwordx4 v153, s[20:23], s37 offen offset:1024 lds
s_add_u32 s37, s37, 114688
MAavv 48,68,44
MAavv 52,72,44
s_waitcnt vmcnt(24) lgkmcnt(0)
s_barrier
MAavv 0,84,76
s_add_u32 m0, s39, 0
Rv 4,155,32768
MAavv 4,88,76
buffer_load_dwordx4 v148, s[12:15], s36 offen lds
MAavv 8,92,76
buffer_load_dwordx4 v149, s[12:15], s36 offen offset:1024 lds
s_add_u32 s36, s36, 4096
s_add_u32 m0, s39, 8192
MAavv 12,96,76
buffer_load_dwordx4 v152, s[20:23], s37 offen lds
MAavv 16,100,76
buffer_load_dwordx4 v153, s[20:23], s37 offen offset:1024 lds
s_add_u32 s37, s37, 114688
s_add_u32 m0, s39, 16384
MAavv 20,104,76
buffer_load_dwordx4 v148, s[12:15], s36 offen lds
MAavv 24,108,76
Rv 8,155,33792
MAavv 28,84,80
Rv 12,156,40960
MAavv 32,88,80
Rv 16,156,41984
MAavv 36,92,80
Rv 20,156,43008
MAavv 40,96,80
Rv 24,156,44032
MAavv 44,100,80
Rv 28,156,45056
MAavv 48,104,80
Rv 32,156,46080
MAavv 52,108,80
Rv 36,156,47104
MAavv 0,120,112
Rv 40,155,49152
MAavv 4,124,112
Rv 44,155,50176
MAavv 8,128,112
Rv 48,156,57344
MAavv 12,132,112
Rv 52,156,58368
MAavv 16,136,112
Rv 56,156,59392
MAavv 20,140,112
Rv 60,156,60416
MAavv 24,144,112
Rv 64,156,61440
MAavv 28,120,116
Rv 68,156,62464
MAavv 32,124,116
Rv 72,156,63488
MAavv 36,128,116
buffer_load_dwordx4 v149, s[12:15], s36 offen offset:1024 lds
s_add_u32 s36, s36, 4096
s_add_u32 m0, s39, 24576
MAavv 40,132,116
buffer_load_dwordx4 v152, s[20:23], s37 offen lds
MAavv 44,136,116
buffer_load_dwordx4 v153, s[20:23], s37 offen offset:1024 lds
s_add_u32 s37, s37, 114688
MAavv 48,140,116
MAavv 52,144,116
s_waitcnt vmcnt(24) lgkmcnt(0)
s_barrier
MAavv 0,12,4
s_add_u32 m0, s39, 32768
Rv 76,168,0
MAavv 4,16,4
buffer_load_dwordx4 v148, s[12:15], s36 offen lds
MAavv 8,20,4
buffer_load_dwordx4 v149, s[12:15], s36 offen offset:1024 lds
s_add_u32 s36, s36, 4096
s_add_u32 m0, s39, 40960
MAavv 12,24,4
buffer_load_dwordx4 v152, s[20:23], s37 offen lds
MAavv 16,28,4
buffer_load_dwordx4 v153, s[20:23], s37 offen offset:1024 lds
s_add_u32 s37, s37, 114688
s_add_u32 m0, s39, 49152
MAavv 20,32,4
buffer_load_dwordx4 v148, s[12:15], s36 offen lds
MAavv 24,36,4
Rv 80,168,1024
MAavv 28,12,8
Rv 84,170,8192
MAavv 32,16,8
Rv 88,170,9216
MAavv 36,20,8
Rv 92,170,10240
MAavv 40,24,8
Rv 96,170,11264
MAavv 44,28,8
Rv 100,170,12288
MAavv 48,32,8
Rv 104,170,13312
MAavv 52,36,8
Rv 108,170,14336
MAavv 0,48,40
Rv 112,168,16384
MAavv 4,52,40
Rv 116,168,17408
MAavv 8,56,40
Rv 120,170,24576
MAavv 12,60,40
Rv 124,170,25600
MAavv 16,64,40
Rv 128,170,26624
MAavv 20,68,40
Rv 132,170,27648
MAavv 24,72,40
Rv 136,170,28672
MAavv 28,48,44
Rv 140,170,29696
MAavv 32,52,44
Rv 144,170,30720
MAavv 36,56,44
buffer_load_dwordx4 v149, s[12:15], s36 offen offset:1024 lds
s_add_u32 s36, s36, 4096
s_add_u32 m0, s39, 57344
MAavv 40,60,44
buffer_load_dwordx4 v152, s[20:23], s37 offen lds
MAavv 44,64,44
buffer_load_dwordx4 v153, s[20:23], s37 offen offset:1024 lds
s_add_u32 s37, s37, 114688
MAavv 48,68,44
MAavv 52,72,44
s_waitcnt vmcnt(24) lgkmcnt(0)
s_barrier
MAavv 0,84,76
s_add_u32 m0, s39, 65536
Rv 4,168,32768
MAavv 4,88,76
buffer_load_dwordx4 v148, s[12:15], s36 offen lds
MAavv 8,92,76
buffer_load_dwordx4 v149, s[12:15], s36 offen offset:1024 lds
s_add_u32 s36, s36, 4096
s_add_u32 m0, s39, 73728
MAavv 12,96,76
buffer_load_dwordx4 v152, s[20:23], s37 offen lds
MAavv 16,100,76
buffer_load_dwordx4 v153, s[20:23], s37 offen offset:1024 lds
s_add_u32 s37, s37, 114688
s_add_u32 m0, s39, 81920
MAavv 20,104,76
buffer_load_dwordx4 v148, s[12:15], s36 offen lds
MAavv 24,108,76
Rv 8,168,33792
MAavv 28,84,80
Rv 12,170,40960
MAavv 32,88,80
Rv 16,170,41984
MAavv 36,92,80
Rv 20,170,43008
MAavv 40,96,80
Rv 24,170,44032
MAavv 44,100,80
Rv 28,170,45056
MAavv 48,104,80
Rv 32,170,46080
MAavv 52,108,80
Rv 36,170,47104
MAavv 0,120,112
Rv 40,168,49152
MAavv 4,124,112
Rv 44,168,50176
MAavv 8,128,112
Rv 48,170,57344
MAavv 12,132,112
Rv 52,170,58368
MAavv 16,136,112
Rv 56,170,59392
MAavv 20,140,112
Rv 60,170,60416
MAavv 24,144,112
Rv 64,170,61440
MAavv 28,120,116
Rv 68,170,62464
MAavv 32,124,116
Rv 72,170,63488
MAavv 36,128,116
buffer_load_dwordx4 v149, s[12:15], s36 offen offset:1024 lds
s_add_u32 s36, s36, 4096
s_add_u32 m0, s39, 90112
MAavv 40,132,116
buffer_load_dwordx4 v152, s[20:23], s37 offen lds
MAavv 44,136,116
buffer_load_dwordx4 v153, s[20:23], s37 offen offset:1024 lds
s_add_u32 s37, s37, 114688
MAavv 48,140,116
MAavv 52,144,116
s_waitcnt vmcnt(24) lgkmcnt(0)
s_barrier
MAavv 0,12,4
s_add_u32 m0, s39, 98304
Rv 76,169,0
MAavv 4,16,4
buffer_load_dwordx4 v148, s[12:15], s36 offen lds
MAavv 8,20,4
buffer_load_dwordx4 v149, s[12:15], s36 offen offset:1024 lds
s_add_u32 s36, s36, 4096
s_add_u32 m0, s39, 106496
MAavv 12,24,4
buffer_load_dwordx4 v152, s[20:23], s37 offen lds
MAavv 16,28,4
buffer_load_dwordx4 v153, s[20:23], s37 offen offset:1024 lds
s_add_u32 s37, s37, 114688
s_add_u32 m0, s39, 114688
MAavv 20,32,4
buffer_load_dwordx4 v148, s[12:15], s36 offen lds
MAavv 24,36,4
Rv 80,169,1024
MAavv 28,12,8
Rv 84,171,8192
MAavv 32,16,8
Rv 88,171,9216
MAavv 36,20,8
Rv 92,171,10240
MAavv 40,24,8
Rv 96,171,11264
MAavv 44,28,8
Rv 100,171,12288
MAavv 48,32,8
Rv 104,171,13312
MAavv 52,36,8
Rv 108,171,14336
MAavv 0,48,40
Rv 112,169,16384
MAavv 4,52,40
Rv 116,169,17408
MAavv 8,56,40
Rv 120,171,24576
MAavv 12,60,40
Rv 124,171,25600
MAavv 16,64,40
Rv 128,171,26624
MAavv 20,68,40
Rv 132,171,27648
MAavv 24,72,40
Rv 136,171,28672
MAavv 28,48,44
Rv 140,171,29696
MAavv 32,52,44
Rv 144,171,30720
MAavv 36,56,44
buffer_load_dwordx4 v149, s[12:15], s36 offen offset:1024 lds
s_add_u32 s36, s36, 4096
s_add_u32 m0, s39, 122880
MAavv 40,60,44
buffer_load_dwordx4 v152, s[20:23], s37 offen lds
MAavv 44,64,44
buffer_load_dwordx4 v153, s[20:23], s37 offen offset:1024 lds
s_add_u32 s37, s37, 114688
MAavv 48,68,44
MAavv 52,72,44
s_waitcnt vmcnt(24) lgkmcnt(0)
s_barrier
MAavv 0,84,76
s_add_u32 m0, s39, 131072
Rv 4,155,0
MAavv 4,88,76
buffer_load_dwordx4 v148, s[12:15], s36 offen lds
MAavv 8,92,76
buffer_load_dwordx4 v149, s[12:15], s36 offen offset:1024 lds
s_add_u32 s36, s36, 4096
s_add_u32 m0, s39, 139264
MAavv 12,96,76
buffer_load_dwordx4 v152, s[20:23], s37 offen lds
MAavv 16,100,76
buffer_load_dwordx4 v153, s[20:23], s37 offen offset:1024 lds
s_add_u32 s37, s37, 114688
s_add_u32 m0, s39, 147456
MAavv 20,104,76
buffer_load_dwordx4 v148, s[12:15], s36 offen lds
MAavv 24,108,76
Rv 8,155,1024
MAavv 28,84,80
Rv 12,156,8192
MAavv 32,88,80
Rv 16,156,9216
MAavv 36,92,80
Rv 20,156,10240
MAavv 40,96,80
Rv 24,156,11264
MAavv 44,100,80
Rv 28,156,12288
MAavv 48,104,80
Rv 32,156,13312
MAavv 52,108,80
Rv 36,156,14336
MAavv 0,120,112
Rv 40,155,16384
MAavv 4,124,112
Rv 44,155,17408
MAavv 8,128,112
Rv 48,156,24576
MAavv 12,132,112
Rv 52,156,25600
MAavv 16,136,112
Rv 56,156,26624
MAavv 20,140,112
Rv 60,156,27648
MAavv 24,144,112
Rv 64,156,28672
MAavv 28,120,116
Rv 68,156,29696
MAavv 32,124,116
Rv 72,156,30720
MAavv 36,128,116
buffer_load_dwordx4 v149, s[12:15], s36 offen offset:1024 lds
s_add_u32 s36, s36, 4096
s_add_u32 m0, s39, 155648
MAavv 40,132,116
buffer_load_dwordx4 v152, s[20:23], s37 offen lds
MAavv 44,136,116
buffer_load_dwordx4 v153, s[20:23], s37 offen offset:1024 lds
s_add_u32 s37, s37, 114688
MAavv 48,140,116
MAavv 52,144,116
s_waitcnt vmcnt(24) lgkmcnt(0)
s_barrier
MAavv 0,12,4
s_add_u32 m0, s39, 0
Rv 76,155,32768
MAavv 4,16,4
buffer_load_dwordx4 v148, s[12:15], s36 offen lds
MAavv 8,20,4
buffer_load_dwordx4 v149, s[12:15], s36 offen offset:1024 lds
s_add_u32 s36, s36, 4096
s_add_u32 m0, s39, 8192
MAavv 12,24,4
buffer_load_dwordx4 v152, s[20:23], s37 offen lds
MAavv 16,28,4
buffer_load_dwordx4 v153, s[20:23], s37 offen offset:1024 lds
s_add_u32 s37, s37, 114688
s_add_u32 m0, s39, 16384
MAavv 20,32,4
buffer_load_dwordx4 v148, s[12:15], s36 offen lds
MAavv 24,36,4
Rv 80,155,33792
MAavv 28,12,8
Rv 84,156,40960
MAavv 32,16,8
Rv 88,156,41984
MAavv 36,20,8
Rv 92,156,43008
MAavv 40,24,8
Rv 96,156,44032
MAavv 44,28,8
Rv 100,156,45056
MAavv 48,32,8
Rv 104,156,46080
MAavv 52,36,8
Rv 108,156,47104
MAavv 0,48,40
Rv 112,155,49152
MAavv 4,52,40
Rv 116,155,50176
MAavv 8,56,40
Rv 120,156,57344
MAavv 12,60,40
Rv 124,156,58368
MAavv 16,64,40
Rv 128,156,59392
MAavv 20,68,40
Rv 132,156,60416
MAavv 24,72,40
Rv 136,156,61440
MAavv 28,48,44
Rv 140,156,62464
MAavv 32,52,44
Rv 144,156,63488
MAavv 36,56,44
buffer_load_dwordx4 v149, s[12:15], s36 offen offset:1024 lds
s_add_u32 s36, s36, 4096
s_add_u32 m0, s39, 24576
MAavv 40,60,44
buffer_load_dwordx4 v152, s[20:23], s37 offen lds
MAavv 44,64,44
buffer_load_dwordx4 v153, s[20:23], s37 offen offset:1024 lds
s_add_u32 s37, s37, 114688
MAavv 48,68,44
MAavv 52,72,44
s_waitcnt vmcnt(24) lgkmcnt(0)
s_barrier
MAavv 0,84,76
s_add_u32 m0, s39, 32768
Rv 4,168,0
MAavv 4,88,76
buffer_load_dwordx4 v150, s[24:27], s38 offen lds
MAavv 8,92,76
buffer_load_dwordx4 v151, s[24:27], s38 offen offset:1024 lds
s_add_u32 s38, s38, 4096
s_add_u32 m0, s39, 40960
MAavv 12,96,76
buffer_load_dwordx4 v152, s[20:23], s37 offen lds
MAavv 16,100,76
buffer_load_dwordx4 v153, s[20:23], s37 offen offset:1024 lds
s_add_u32 s37, s37, 114688
s_add_u32 m0, s39, 49152
MAavv 20,104,76
buffer_load_dwordx4 v150, s[24:27], s38 offen lds
MAavv 24,108,76
Rv 8,168,1024
MAavv 28,84,80
Rv 12,170,8192
MAavv 32,88,80
Rv 16,170,9216
MAavv 36,92,80
Rv 20,170,10240
MAavv 40,96,80
Rv 24,170,11264
MAavv 44,100,80
Rv 28,170,12288
MAavv 48,104,80
Rv 32,170,13312
MAavv 52,108,80
Rv 36,170,14336
MAavv 0,120,112
Rv 40,168,16384
MAavv 4,124,112
Rv 44,168,17408
MAavv 8,128,112
Rv 48,170,24576
MAavv 12,132,112
Rv 52,170,25600
MAavv 16,136,112
Rv 56,170,26624
MAavv 20,140,112
Rv 60,170,27648
MAavv 24,144,112
Rv 64,170,28672
MAavv 28,120,116
Rv 68,170,29696
MAavv 32,124,116
Rv 72,170,30720
MAavv 36,128,116
buffer_load_dwordx4 v151, s[24:27], s38 offen offset:1024 lds
s_add_u32 s38, s38, 4096
s_add_u32 m0, s39, 57344
MAavv 40,132,116
buffer_load_dwordx4 v152, s[20:23], s37 offen lds
MAavv 44,136,116
buffer_load_dwordx4 v153, s[20:23], s37 offen offset:1024 lds
s_add_u32 s37, s37, 114688
MAavv 48,140,116
MAavv 52,144,116
s_waitcnt vmcnt(24) lgkmcnt(0)
s_barrier
MAavv 0,12,4
s_add_u32 m0, s39, 65536
Rv 76,168,32768
MAavv 4,16,4
buffer_load_dwordx4 v150, s[24:27], s38 offen lds
MAavv 8,20,4
buffer_load_dwordx4 v151, s[24:27], s38 offen offset:1024 lds
s_add_u32 s38, s38, 4096
s_add_u32 m0, s39, 73728
MAavv 12,24,4
buffer_load_dwordx4 v152, s[20:23], s37 offen lds
MAavv 16,28,4
buffer_load_dwordx4 v153, s[20:23], s37 offen offset:1024 lds
s_add_u32 s37, s37, 114688
s_add_u32 m0, s39, 81920
MAavv 20,32,4
buffer_load_dwordx4 v150, s[24:27], s38 offen lds
MAavv 24,36,4
Rv 80,168,33792
MAavv 28,12,8
Rv 84,170,40960
MAavv 32,16,8
Rv 88,170,41984
MAavv 36,20,8
Rv 92,170,43008
MAavv 40,24,8
Rv 96,170,44032
MAavv 44,28,8
Rv 100,170,45056
MAavv 48,32,8
Rv 104,170,46080
MAavv 52,36,8
Rv 108,170,47104
MAavv 0,48,40
Rv 112,168,49152
MAavv 4,52,40
Rv 116,168,50176
MAavv 8,56,40
Rv 120,170,57344
MAavv 12,60,40
Rv 124,170,58368
MAavv 16,64,40
Rv 128,170,59392
MAavv 20,68,40
Rv 132,170,60416
MAavv 24,72,40
Rv 136,170,61440
MAavv 28,48,44
Rv 140,170,62464
MAavv 32,52,44
Rv 144,170,63488
MAavv 36,56,44
buffer_load_dwordx4 v151, s[24:27], s38 offen offset:1024 lds
s_add_u32 s38, s38, 4096
s_add_u32 m0, s39, 90112
MAavv 40,60,44
buffer_load_dwordx4 v152, s[20:23], s37 offen lds
MAavv 44,64,44
buffer_load_dwordx4 v153, s[20:23], s37 offen offset:1024 lds
s_add_u32 s37, s37, 114688
MAavv 48,68,44
MAavv 52,72,44
s_waitcnt vmcnt(24) lgkmcnt(0)
s_barrier
MAavv 0,84,76
s_add_u32 m0, s39, 98304
Rv 4,169,0
MAavv 4,88,76
buffer_load_dwordx4 v150, s[24:27], s38 offen lds
MAavv 8,92,76
buffer_load_dwordx4 v151, s[24:27], s38 offen offset:1024 lds
s_add_u32 s38, s38, 4096
s_add_u32 m0, s39, 106496
MAavv 12,96,76
buffer_load_dwordx4 v152, s[20:23], s37 offen lds
MAavv 16,100,76
buffer_load_dwordx4 v153, s[20:23], s37 offen offset:1024 lds
s_add_u32 s37, s37, 114688
s_add_u32 m0, s39, 114688
MAavv 20,104,76
buffer_load_dwordx4 v150, s[24:27], s38 offen lds
MAavv 24,108,76
Rv 8,169,1024
MAavv 28,84,80
Rv 12,171,8192
MAavv 32,88,80
Rv 16,171,9216
MAavv 36,92,80
Rv 20,171,10240
MAavv 40,96,80
Rv 24,171,11264
MAavv 44,100,80
Rv 28,171,12288
MAavv 48,104,80
Rv 32,171,13312
MAavv 52,108,80
Rv 36,171,14336
MAavv 0,120,112
Rv 40,169,16384
MAavv 4,124,112
Rv 44,169,17408
MAavv 8,128,112
Rv 48,171,24576
MAavv 12,132,112
Rv 52,171,25600
MAavv 16,136,112
Rv 56,171,26624
MAavv 20,140,112
Rv 60,171,27648
MAavv 24,144,112
Rv 64,171,28672
MAavv 28,120,116
Rv 68,171,29696
MAavv 32,124,116
Rv 72,171,30720
MAavv 36,128,116
buffer_load_dwordx4 v151, s[24:27], s38 offen offset:1024 lds
s_add_u32 s38, s38, 4096
s_add_u32 m0, s39, 122880
MAavv 40,132,116
buffer_load_dwordx4 v152, s[20:23], s37 offen lds
MAavv 44,136,116
buffer_load_dwordx4 v153, s[20:23], s37 offen offset:1024 lds
s_add_u32 s37, s37, 114688
MAavv 48,140,116
MAavv 52,144,116
s_waitcnt vmcnt(24) lgkmcnt(0)
s_barrier
MAavv 0,12,4
s_add_u32 m0, s39, 131072
Rv 76,155,0
MAavv 4,16,4
buffer_load_dwordx4 v150, s[24:27], s38 offen lds
MAavv 8,20,4
Rv 80,155,1024
MAavv 12,24,4
Rv 84,156,8192
MAavv 16,28,4
Rv 88,156,9216
MAavv 20,32,4
Rv 92,156,10240
MAavv 24,36,4
Rv 96,156,11264
MAavv 28,12,8
Rv 100,156,12288
MAavv 32,16,8
Rv 104,156,13312
MAavv 36,20,8
buffer_load_dwordx4 v151, s[24:27], s38 offen offset:1024 lds
s_add_u32 s38, s38, 4096
s_add_u32 m0, s39, 139264
MAavv 40,24,8
Rv 108,156,14336
MAavv 44,28,8
Rv 112,155,16384
MAavv 48,32,8
Rv 116,155,17408
MAavv 52,36,8
buffer_load_dwordx4 v152, s[20:23], s37 offen lds
MAavv 0,48,40
Rv 120,156,24576
MAavv 4,52,40
Rv 124,156,25600
MAavv 8,56,40
Rv 128,156,26624
MAavv 12,60,40
Rv 132,156,27648
MAavv 16,64,40
buffer_load_dwordx4 v153, s[20:23], s37 offen offset:1024 lds
s_add_u32 s37, s37, 114688
MAavv 20,68,40
Rv 136,156,28672
MAavv 24,72,40
Rv 140,156,29696
MAavv 28,48,44
Rv 144,156,30720
MAavv 32,52,44
MAavv 36,56,44
MAavv 40,60,44
MAavv 44,64,44
MAavv 48,68,44
MAavv 52,72,44
s_waitcnt vmcnt(20) lgkmcnt(0)
s_barrier
MAavv 0,84,76
Rv 4,155,32768
MAavv 4,88,76
Rv 8,155,33792
MAavv 8,92,76
Rv 12,156,40960
MAavv 12,96,76
Rv 16,156,41984
MAavv 16,100,76
MAavv 20,104,76
Rv 20,156,43008
MAavv 24,108,76
Rv 24,156,44032
MAavv 28,84,80
Rv 28,156,45056
MAavv 32,88,80
Rv 32,156,46080
MAavv 36,92,80
MAavv 40,96,80
Rv 36,156,47104
MAavv 44,100,80
Rv 40,155,49152
MAavv 48,104,80
Rv 44,155,50176
MAavv 52,108,80
MAavv 0,120,112
Rv 48,156,57344
MAavv 4,124,112
Rv 52,156,58368
MAavv 8,128,112
Rv 56,156,59392
MAavv 12,132,112
Rv 60,156,60416
MAavv 16,136,112
MAavv 20,140,112
Rv 64,156,61440
MAavv 24,144,112
Rv 68,156,62464
MAavv 28,120,116
Rv 72,156,63488
MAavv 32,124,116
MAavv 36,128,116
MAavv 40,132,116
MAavv 44,136,116
MAavv 48,140,116
MAavv 52,144,116
s_waitcnt vmcnt(12) lgkmcnt(0)
s_barrier
MZavv 56,12,4
Rv 76,168,0
MZavv 60,16,4
Rv 80,168,1024
MZavv 64,20,4
Rv 84,170,8192
MZavv 68,24,4
Rv 88,170,9216
MZavv 72,28,4
MZavv 76,32,4
Rv 92,170,10240
MZavv 80,36,4
Rv 96,170,11264
MZavv 84,12,8
Rv 100,170,12288
MZavv 88,16,8
Rv 104,170,13312
MZavv 92,20,8
MZavv 96,24,8
Rv 108,170,14336
MZavv 100,28,8
Rv 112,168,16384
MZavv 104,32,8
Rv 116,168,17408
MZavv 108,36,8
MAavv 56,48,40
Rv 120,170,24576
MAavv 60,52,40
Rv 124,170,25600
MAavv 64,56,40
Rv 128,170,26624
MAavv 68,60,40
Rv 132,170,27648
MAavv 72,64,40
MAavv 76,68,40
Rv 136,170,28672
MAavv 80,72,40
Rv 140,170,29696
MAavv 84,48,44
Rv 144,170,30720
MAavv 88,52,44
MAavv 92,56,44
MAavv 96,60,44
MAavv 100,64,44
MAavv 104,68,44
MAavv 108,72,44
s_waitcnt vmcnt(4) lgkmcnt(0)
)ASM"
R"ASM(s_barrier
MAavv 56,84,76
Rv 4,168,32768
MAavv 60,88,76
Rv 8,168,33792
MAavv 64,92,76
Rv 12,170,40960
MAavv 68,96,76
Rv 16,170,41984
MAavv 72,100,76
MAavv 76,104,76
Rv 20,170,43008
MAavv 80,108,76
Rv 24,170,44032
MAavv 84,84,80
Rv 28,170,45056
MAavv 88,88,80
Rv 32,170,46080
MAavv 92,92,80
MAavv 96,96,80
Rv 36,170,47104
MAavv 100,100,80
Rv 40,168,49152
MAavv 104,104,80
Rv 44,168,50176
MAavv 108,108,80
MAavv 56,120,112
Rv 48,170,57344
MAavv 60,124,112
Rv 52,170,58368
MAavv 64,128,112
Rv 56,170,59392
MAavv 68,132,112
Rv 60,170,60416
MAavv 72,136,112
MAavv 76,140,112
Rv 64,170,61440
MAavv 80,144,112
Rv 68,170,62464
MAavv 84,120,116
Rv 72,170,63488
MAavv 88,124,116
MAavv 92,128,116
MAavv 96,132,116
MAavv 100,136,116
MAavv 104,140,116
MAavv 108,144,116
s_waitcnt vmcnt(0) lgkmcnt(0)
s_barrier
MAavv 56,12,4
Rv 76,169,0
MAavv 60,16,4
MAavv 64,20,4
Rv 80,169,1024
MAavv 68,24,4
MAavv 72,28,4
MAavv 76,32,4
Rv 84,171,8192
MAavv 80,36,4
MAavv 84,12,8
Rv 88,171,9216
MAavv 88,16,8
MAavv 92,20,8
MAavv 96,24,8
Rv 92,171,10240
MAavv 100,28,8
MAavv 104,32,8
Rv 96,171,11264
MAavv 108,36,8
MAavv 56,48,40
MAavv 60,52,40
Rv 100,171,12288
MAavv 64,56,40
MAavv 68,60,40
Rv 104,171,13312
MAavv 72,64,40
MAavv 76,68,40
MAavv 80,72,40
Rv 108,171,14336
MAavv 84,48,44
MAavv 88,52,44
MAavv 92,56,44
MAavv 96,60,44
MAavv 100,64,44
MAavv 104,68,44
MAavv 108,72,44
s_waitcnt vmcnt(0) lgkmcnt(0)
s_barrier
MAavv 56,84,76
MAavv 60,88,76
MAavv 64,92,76
MAavv 68,96,76
MAavv 72,100,76
MAavv 76,104,76
MAavv 80,108,76
MAavv 84,84,80
MAavv 88,88,80
MAavv 92,92,80
MAavv 96,96,80
MAavv 100,100,80
MAavv 104,104,80
MAavv 108,108,80
s_nop 7
s_waitcnt vmcnt(0)
v_sub_f32 v162, 1.0, v166
AR 4,0
AR 5,1
AR 6,2
AR 7,3
AR 116,56
AR 117,57
AR 118,58
AR 119,59
v_mul_f32 v4, v4, v166
v_mul_f32 v5, v5, v166
v_mul_f32 v6, v6, v166
v_mul_f32 v7, v7, v166
v_fmac_f32 v4, v116, v162
v_fmac_f32 v5, v117, v162
v_fmac_f32 v6, v118, v162
v_fmac_f32 v7, v119, v162
AR 8,4
AR 9,5
AR 10,6
AR 11,7
AR 120,60
AR 121,61
AR 122,62
AR 123,63
v_mul_f32 v8, v8, v166
v_mul_f32 v9, v9, v166
v_mul_f32 v10, v10, v166
v_mul_f32 v11, v11, v166
v_fmac_f32 v8, v120, v162
v_fmac_f32 v9, v121, v162
v_fmac_f32 v10, v122, v162
v_fmac_f32 v11, v123, v162
AR 12,8
AR 13,9
AR 14,10
AR 15,11
AR 124,64
AR 125,65
AR 126,66
AR 127,67
v_mul_f32 v12, v12, v166
v_mul_f32 v13, v13, v166
v_mul_f32 v14, v14, v166
v_mul_f32 v15, v15, v166
v_fmac_f32 v12, v124, v162
v_fmac_f32 v13, v125, v162
v_fmac_f32 v14, v126, v162
v_fmac_f32 v15, v127, v162
AR 16,12
AR 17,13
AR 18,14
AR 19,15
AR 128,68
AR 129,69
AR 130,70
AR 131,71
v_mul_f32 v16, v16, v166
v_mul_f32 v17, v17, v166
v_mul_f32 v18, v18, v166
v_mul_f32 v19, v19, v166
v_fmac_f32 v16, v128, v162
v_fmac_f32 v17, v129, v162
v_fmac_f32 v18, v130, v162
v_fmac_f32 v19, v131, v162
AR 20,16
AR 21,17
AR 22,18
AR 23,19
AR 116,72
AR 117,73
AR 118,74
AR 119,75
v_mul_f32 v20, v20, v166
v_mul_f32 v21, v21, v166
v_mul_f32 v22, v22, v166
v_mul_f32 v23, v23, v166
v_fmac_f32 v20, v116, v162
v_fmac_f32 v21, v117, v162
v_fmac_f32 v22, v118, v162
v_fmac_f32 v23, v119, v162
AR 24,20
AR 25,21
AR 26,22
AR 27,23
AR 120,76
AR 121,77
AR 122,78
AR 123,79
v_mul_f32 v24, v24, v166
v_mul_f32 v25, v25, v166
v_mul_f32 v26, v26, v166
v_mul_f32 v27, v27, v166
v_fmac_f32 v24, v120, v162
v_fmac_f32 v25, v121, v162
v_fmac_f32 v26, v122, v162
v_fmac_f32 v27, v123, v162
AR 28,24
AR 29,25
AR 30,26
AR 31,27
AR 124,80
AR 125,81
AR 126,82
AR 127,83
v_mul_f32 v28, v28, v166
v_mul_f32 v29, v29, v166
v_mul_f32 v30, v30, v166
v_mul_f32 v31, v31, v166
v_fmac_f32 v28, v124, v162
v_fmac_f32 v29, v125, v162
v_fmac_f32 v30, v126, v162
v_fmac_f32 v31, v127, v162
v_sub_f32 v162, 1.0, v167
AR 32,28
AR 33,29
AR 34,30
AR 35,31
AR 128,84
AR 129,85
AR 130,86
AR 131,87
v_mul_f32 v32, v32, v167
v_mul_f32 v33, v33, v167
v_mul_f32 v34, v34, v167
v_mul_f32 v35, v35, v167
v_fmac_f32 v32, v128, v162
v_fmac_f32 v33, v129, v162
v_fmac_f32 v34, v130, v162
v_fmac_f32 v35, v131, v162
AR 36,32
AR 37,33
AR 38,34
AR 39,35
AR 116,88
AR 117,89
AR 118,90
AR 119,91
v_mul_f32 v36, v36, v167
v_mul_f32 v37, v37, v167
v_mul_f32 v38, v38, v167
v_mul_f32 v39, v39, v167
v_fmac_f32 v36, v116, v162
v_fmac_f32 v37, v117, v162
v_fmac_f32 v38, v118, v162
v_fmac_f32 v39, v119, v162
AR 40,36
AR 41,37
AR 42,38
AR 43,39
AR 120,92
AR 121,93
AR 122,94
AR 123,95
v_mul_f32 v40, v40, v167
v_mul_f32 v41, v41, v167
v_mul_f32 v42, v42, v167
v_mul_f32 v43, v43, v167
v_fmac_f32 v40, v120, v162
v_fmac_f32 v41, v121, v162
v_fmac_f32 v42, v122, v162
v_fmac_f32 v43, v123, v162
AR 44,40
AR 45,41
AR 46,42
AR 47,43
AR 124,96
AR 125,97
AR 126,98
AR 127,99
v_mul_f32 v44, v44, v167
v_mul_f32 v45, v45, v167
v_mul_f32 v46, v46, v167
v_mul_f32 v47, v47, v167
v_fmac_f32 v44, v124, v162
v_fmac_f32 v45, v125, v162
v_fmac_f32 v46, v126, v162
v_fmac_f32 v47, v127, v162
AR 48,44
AR 49,45
AR 50,46
AR 51,47
AR 128,100
AR 129,101
AR 130,102
AR 131,103
v_mul_f32 v48, v48, v167
v_mul_f32 v49, v49, v167
v_mul_f32 v50, v50, v167
v_mul_f32 v51, v51, v167
v_fmac_f32 v48, v128, v162
v_fmac_f32 v49, v129, v162
v_fmac_f32 v50, v130, v162
v_fmac_f32 v51, v131, v162
AR 52,48
AR 53,49
AR 54,50
AR 55,51
AR 116,104
AR 117,105
AR 118,106
AR 119,107
v_mul_f32 v52, v52, v167
v_mul_f32 v53, v53, v167
v_mul_f32 v54, v54, v167
v_mul_f32 v55, v55, v167
v_fmac_f32 v52, v116, v162
v_fmac_f32 v53, v117, v162
v_fmac_f32 v54, v118, v162
v_fmac_f32 v55, v119, v162
AR 56,52
AR 57,53
AR 58,54
AR 59,55
AR 120,108
AR 121,109
AR 122,110
AR 123,111
v_mul_f32 v56, v56, v167
v_mul_f32 v57, v57, v167
v_mul_f32 v58, v58, v167
v_mul_f32 v59, v59, v167
v_fmac_f32 v56, v120, v162
v_fmac_f32 v57, v121, v162
v_fmac_f32 v58, v122, v162
v_fmac_f32 v59, v123, v162
s_lshl_b32 s44, s35, 2
s_cmp_eq_u32 s34, 1
s_cbranch_scc1 Lk3_ks1
s_cmp_eq_u32 s34, 2
s_cbranch_scc1 Lk3_ks2
s_cmp_eq_u32 s34, 3
s_cbranch_scc1 Lk3_ks3
Lk3_ks0:
s_add_u32 s42, s44, 1
s_lshl_b32 s42, s42, 2
s_add_u32 s42, s42, 0
s_lshl_b32 s42, s42, 2
s_add_u32 s42, s42, s28
s_lshl_b32 s42, s42, 12
s_add_u32 s42, s42, 84934656
buffer_store_dwordx4 v[12:15], v154, s[12:15], s42 offen sc1
buffer_store_dwordx4 v[16:19], v154, s[12:15], s42 offen offset:1024 sc1
buffer_store_dwordx4 v[40:43], v154, s[12:15], s42 offen offset:2048 sc1
buffer_store_dwordx4 v[44:47], v154, s[12:15], s42 offen offset:3072 sc1
s_add_u32 s42, s44, 2
s_lshl_b32 s42, s42, 2
s_add_u32 s42, s42, 0
s_lshl_b32 s42, s42, 2
s_add_u32 s42, s42, s28
s_lshl_b32 s42, s42, 12
s_add_u32 s42, s42, 84934656
buffer_store_dwordx4 v[20:23], v154, s[12:15], s42 offen sc1
buffer_store_dwordx4 v[24:27], v154, s[12:15], s42 offen offset:1024 sc1
buffer_store_dwordx4 v[48:51], v154, s[12:15], s42 offen offset:2048 sc1
buffer_store_dwordx4 v[52:55], v154, s[12:15], s42 offen offset:3072 sc1
s_add_u32 s42, s44, 3
s_lshl_b32 s42, s42, 2
s_add_u32 s42, s42, 0
s_lshl_b32 s42, s42, 2
s_add_u32 s42, s42, s28
s_lshl_b32 s42, s42, 12
s_add_u32 s42, s42, 84934656
buffer_store_dwordx4 v[28:31], v154, s[12:15], s42 offen sc1
buffer_store_dwordx4 v[56:59], v154, s[12:15], s42 offen offset:1024 sc1
s_waitcnt vmcnt(0)
v_mov_b32 v159, 1
v_lshrrev_b32 v158, 4, v154
v_lshlrev_b32 v158, 8, v158
s_lshl_b32 s42, s44, 8
s_add_u32 s42, s42, 125829184
s_mov_b64 exec, 14
buffer_atomic_add v159, v158, s[12:15], s42 offen
s_mov_b64 exec, -1
s_add_u32 s42, s42, 0
v_mov_b32 v158, 0
s_mov_b32 s36, 0
Lk3_poll0:
buffer_load_dword v159, v158, s[12:15], s42 offen sc1
s_waitcnt vmcnt(0)
v_readfirstlane_b32 s43, v159
s_cmp_ge_u32 s43, 12
s_cbranch_scc1 Lk3_got0
s_add_u32 s36, s36, 1
s_cmp_lt_u32 s36, 1048576
s_sleep 1
s_cbranch_scc1 Lk3_poll0
Lk3_got0:
s_barrier
s_add_u32 s42, s44, 0
s_lshl_b32 s42, s42, 2
s_add_u32 s42, s42, 1
s_lshl_b32 s42, s42, 2
s_add_u32 s42, s42, s28
s_lshl_b32 s42, s42, 12
s_add_u32 s42, s42, 84934656
buffer_load_dwordx4 v[60:63], v154, s[12:15], s42 offen sc1
buffer_load_dwordx4 v[64:67], v154, s[12:15], s42 offen offset:1024 sc1
buffer_load_dwordx4 v[68:71], v154, s[12:15], s42 offen offset:2048 sc1
buffer_load_dwordx4 v[72:75], v154, s[12:15], s42 offen offset:3072 sc1
s_add_u32 s42, s44, 0
s_lshl_b32 s42, s42, 2
s_add_u32 s42, s42, 2
s_lshl_b32 s42, s42, 2
s_add_u32 s42, s42, s28
s_lshl_b32 s42, s42, 12
s_add_u32 s42, s42, 84934656
buffer_load_dwordx4 v[76:79], v154, s[12:15], s42 offen sc1
buffer_load_dwordx4 v[80:83], v154, s[12:15], s42 offen offset:1024 sc1
buffer_load_dwordx4 v[84:87], v154, s[12:15], s42 offen offset:2048 sc1
buffer_load_dwordx4 v[88:91], v154, s[12:15], s42 offen offset:3072 sc1
s_add_u32 s42, s44, 0
s_lshl_b32 s42, s42, 2
s_add_u32 s42, s42, 3
s_lshl_b32 s42, s42, 2
s_add_u32 s42, s42, s28
s_lshl_b32 s42, s42, 12
s_add_u32 s42, s42, 84934656
buffer_load_dwordx4 v[92:95], v154, s[12:15], s42 offen sc1
buffer_load_dwordx4 v[96:99], v154, s[12:15], s42 offen offset:1024 sc1
buffer_load_dwordx4 v[100:103], v154, s[12:15], s42 offen offset:2048 sc1
buffer_load_dwordx4 v[104:107], v154, s[12:15], s42 offen offset:3072 sc1
v_lshrrev_b32 v158, 4, v154
v_and_b32 v159, 15, v158
v_lshrrev_b32 v158, 4, v158
v_mul_u32_u24 v159, 896, v159
v_lshlrev_b32 v160, 4, v158
v_add_u32 v160, v159, v160
s_lshl_b32 s42, s30, 2
s_add_u32 s42, s42, s28
s_mul_i32 s42, s42, 28672
s_mul_i32 s43, s31, 224
s_add_u32 s42, s42, s43
s_add_u32 s42, s42, 0
s_waitcnt vmcnt(0)
v_add_f32 v4, v4, v60
v_add_f32 v5, v5, v61
v_add_f32 v6, v6, v62
v_add_f32 v7, v7, v63
v_add_f32 v4, v4, v76
v_add_f32 v5, v5, v77
v_add_f32 v6, v6, v78
v_add_f32 v7, v7, v79
v_add_f32 v4, v4, v92
v_add_f32 v5, v5, v93
v_add_f32 v6, v6, v94
v_add_f32 v7, v7, v95
v_add_f32 v8, v8, v64
v_add_f32 v9, v9, v65
v_add_f32 v10, v10, v66
v_add_f32 v11, v11, v67
v_add_f32 v8, v8, v80
v_add_f32 v9, v9, v81
v_add_f32 v10, v10, v82
v_add_f32 v11, v11, v83
v_add_f32 v8, v8, v96
v_add_f32 v9, v9, v97
v_add_f32 v10, v10, v98
v_add_f32 v11, v11, v99
v_add_f32 v32, v32, v68
v_add_f32 v33, v33, v69
v_add_f32 v34, v34, v70
v_add_f32 v35, v35, v71
v_add_f32 v32, v32, v84
v_add_f32 v33, v33, v85
v_add_f32 v34, v34, v86
v_add_f32 v35, v35, v87
v_add_f32 v32, v32, v100
v_add_f32 v33, v33, v101
v_add_f32 v34, v34, v102
v_add_f32 v35, v35, v103
v_add_f32 v36, v36, v72
v_add_f32 v37, v37, v73
v_add_f32 v38, v38, v74
v_add_f32 v39, v39, v75
v_add_f32 v36, v36, v88
v_add_f32 v37, v37, v89
v_add_f32 v38, v38, v90
v_add_f32 v39, v39, v91
v_add_f32 v36, v36, v104
v_add_f32 v37, v37, v105
v_add_f32 v38, v38, v106
v_add_f32 v39, v39, v107
CV 116,4,5
CV 117,6,7
CV 118,8,9
CV 119,10,11
buffer_store_dwordx4 v[116:119], v160, s[16:19], s42 offen
CV 120,32,33
CV 121,34,35
CV 122,36,37
CV 123,38,39
s_add_u32 s42, s42, 14336
buffer_store_dwordx4 v[120:123], v160, s[16:19], s42 offen
s_branch Lk3_end
Lk3_ks1:
s_add_u32 s42, s44, 0
s_lshl_b32 s42, s42, 2
s_add_u32 s42, s42, 1
s_lshl_b32 s42, s42, 2
s_add_u32 s42, s42, s28
s_lshl_b32 s42, s42, 12
s_add_u32 s42, s42, 84934656
buffer_store_dwordx4 v[4:7], v154, s[12:15], s42 offen sc1
buffer_store_dwordx4 v[8:11], v154, s[12:15], s42 offen offset:1024 sc1
buffer_store_dwordx4 v[32:35], v154, s[12:15], s42 offen offset:2048 sc1
buffer_store_dwordx4 v[36:39], v154, s[12:15], s42 offen offset:3072 sc1
s_add_u32 s42, s44, 2
s_lshl_b32 s42, s42, 2
s_add_u32 s42, s42, 1
s_lshl_b32 s42, s42, 2
s_add_u32 s42, s42, s28
s_lshl_b32 s42, s42, 12
s_add_u32 s42, s42, 84934656
buffer_store_dwordx4 v[20:23], v154, s[12:15], s42 offen sc1
buffer_store_dwordx4 v[24:27], v154, s[12:15], s42 offen offset:1024 sc1
buffer_store_dwordx4 v[48:51], v154, s[12:15], s42 offen offset:2048 sc1
buffer_store_dwordx4 v[52:55], v154, s[12:15], s42 offen offset:3072 sc1
s_add_u32 s42, s44, 3
s_lshl_b32 s42, s42, 2
s_add_u32 s42, s42, 1
s_lshl_b32 s42, s42, 2
s_add_u32 s42, s42, s28
s_lshl_b32 s42, s42, 12
s_add_u32 s42, s42, 84934656
buffer_store_dwordx4 v[28:31], v154, s[12:15], s42 offen sc1
buffer_store_dwordx4 v[56:59], v154, s[12:15], s42 offen offset:1024 sc1
s_waitcnt vmcnt(0)
v_mov_b32 v159, 1
v_lshrrev_b32 v158, 4, v154
v_lshlrev_b32 v158, 8, v158
s_lshl_b32 s42, s44, 8
s_add_u32 s42, s42, 125829184
s_mov_b64 exec, 13
buffer_atomic_add v159, v158, s[12:15], s42 offen
s_mov_b64 exec, -1
s_add_u32 s42, s42, 256
v_mov_b32 v158, 0
s_mov_b32 s36, 0
Lk3_poll1:
buffer_load_dword v159, v158, s[12:15], s42 offen sc1
s_waitcnt vmcnt(0)
v_readfirstlane_b32 s43, v159
s_cmp_ge_u32 s43, 12
s_cbranch_scc1 Lk3_got1
s_add_u32 s36, s36, 1
s_cmp_lt_u32 s36, 1048576
s_sleep 1
s_cbranch_scc1 Lk3_poll1
Lk3_got1:
s_barrier
s_add_u32 s42, s44, 1
s_lshl_b32 s42, s42, 2
s_add_u32 s42, s42, 0
s_lshl_b32 s42, s42, 2
s_add_u32 s42, s42, s28
s_lshl_b32 s42, s42, 12
s_add_u32 s42, s42, 84934656
buffer_load_dwordx4 v[60:63], v154, s[12:15], s42 offen sc1
buffer_load_dwordx4 v[64:67], v154, s[12:15], s42 offen offset:1024 sc1
buffer_load_dwordx4 v[68:71], v154, s[12:15], s42 offen offset:2048 sc1
buffer_load_dwordx4 v[72:75], v154, s[12:15], s42 offen offset:3072 sc1
s_add_u32 s42, s44, 1
s_lshl_b32 s42, s42, 2
s_add_u32 s42, s42, 2
s_lshl_b32 s42, s42, 2
s_add_u32 s42, s42, s28
s_lshl_b32 s42, s42, 12
s_add_u32 s42, s42, 84934656
buffer_load_dwordx4 v[76:79], v154, s[12:15], s42 offen sc1
buffer_load_dwordx4 v[80:83], v154, s[12:15], s42 offen offset:1024 sc1
buffer_load_dwordx4 v[84:87], v154, s[12:15], s42 offen offset:2048 sc1
buffer_load_dwordx4 v[88:91], v154, s[12:15], s42 offen offset:3072 sc1
s_add_u32 s42, s44, 1
s_lshl_b32 s42, s42, 2
s_add_u32 s42, s42, 3
s_lshl_b32 s42, s42, 2
s_add_u32 s42, s42, s28
s_lshl_b32 s42, s42, 12
s_add_u32 s42, s42, 84934656
buffer_load_dwordx4 v[92:95], v154, s[12:15], s42 offen sc1
buffer_load_dwordx4 v[96:99], v154, s[12:15], s42 offen offset:1024 sc1
buffer_load_dwordx4 v[100:103], v154, s[12:15], s42 offen offset:2048 sc1
buffer_load_dwordx4 v[104:107], v154, s[12:15], s42 offen offset:3072 sc1
v_lshrrev_b32 v158, 4, v154
v_and_b32 v159, 15, v158
v_lshrrev_b32 v158, 4, v158
v_mul_u32_u24 v159, 896, v159
v_lshlrev_b32 v160, 4, v158
v_add_u32 v160, v159, v160
s_lshl_b32 s42, s30, 2
s_add_u32 s42, s42, s28
s_mul_i32 s42, s42, 28672
s_mul_i32 s43, s31, 224
s_add_u32 s42, s42, s43
s_add_u32 s42, s42, 64
s_waitcnt vmcnt(0)
v_add_f32 v12, v12, v60
v_add_f32 v13, v13, v61
v_add_f32 v14, v14, v62
v_add_f32 v15, v15, v63
v_add_f32 v12, v12, v76
v_add_f32 v13, v13, v77
v_add_f32 v14, v14, v78
v_add_f32 v15, v15, v79
v_add_f32 v12, v12, v92
v_add_f32 v13, v13, v93
v_add_f32 v14, v14, v94
v_add_f32 v15, v15, v95
v_add_f32 v16, v16, v64
v_add_f32 v17, v17, v65
v_add_f32 v18, v18, v66
v_add_f32 v19, v19, v67
v_add_f32 v16, v16, v80
v_add_f32 v17, v17, v81
v_add_f32 v18, v18, v82
v_add_f32 v19, v19, v83
v_add_f32 v16, v16, v96
v_add_f32 v17, v17, v97
v_add_f32 v18, v18, v98
v_add_f32 v19, v19, v99
v_add_f32 v40, v40, v68
v_add_f32 v41, v41, v69
v_add_f32 v42, v42, v70
v_add_f32 v43, v43, v71
v_add_f32 v40, v40, v84
v_add_f32 v41, v41, v85
v_add_f32 v42, v42, v86
v_add_f32 v43, v43, v87
v_add_f32 v40, v40, v100
v_add_f32 v41, v41, v101
v_add_f32 v42, v42, v102
v_add_f32 v43, v43, v103
v_add_f32 v44, v44, v72
v_add_f32 v45, v45, v73
v_add_f32 v46, v46, v74
v_add_f32 v47, v47, v75
v_add_f32 v44, v44, v88
v_add_f32 v45, v45, v89
v_add_f32 v46, v46, v90
v_add_f32 v47, v47, v91
v_add_f32 v44, v44, v104
v_add_f32 v45, v45, v105
v_add_f32 v46, v46, v106
v_add_f32 v47, v47, v107
CV 116,12,13
CV 117,14,15
CV 118,16,17
CV 119,18,19
buffer_store_dwordx4 v[116:119], v160, s[16:19], s42 offen
CV 120,40,41
CV 121,42,43
CV 122,44,45
CV 123,46,47
s_add_u32 s42, s42, 14336
buffer_store_dwordx4 v[120:123], v160, s[16:19], s42 offen
s_branch Lk3_end
Lk3_ks2:
s_add_u32 s42, s44, 0
s_lshl_b32 s42, s42, 2
s_add_u32 s42, s42, 2
s_lshl_b32 s42, s42, 2
s_add_u32 s42, s42, s28
s_lshl_b32 s42, s42, 12
s_add_u32 s42, s42, 84934656
buffer_store_dwordx4 v[4:7], v154, s[12:15], s42 offen sc1
buffer_store_dwordx4 v[8:11], v154, s[12:15], s42 offen offset:1024 sc1
buffer_store_dwordx4 v[32:35], v154, s[12:15], s42 offen offset:2048 sc1
buffer_store_dwordx4 v[36:39], v154, s[12:15], s42 offen offset:3072 sc1
s_add_u32 s42, s44, 1
s_lshl_b32 s42, s42, 2
s_add_u32 s42, s42, 2
s_lshl_b32 s42, s42, 2
s_add_u32 s42, s42, s28
s_lshl_b32 s42, s42, 12
s_add_u32 s42, s42, 84934656
buffer_store_dwordx4 v[12:15], v154, s[12:15], s42 offen sc1
buffer_store_dwordx4 v[16:19], v154, s[12:15], s42 offen offset:1024 sc1
buffer_store_dwordx4 v[40:43], v154, s[12:15], s42 offen offset:2048 sc1
buffer_store_dwordx4 v[44:47], v154, s[12:15], s42 offen offset:3072 sc1
s_add_u32 s42, s44, 3
s_lshl_b32 s42, s42, 2
s_add_u32 s42, s42, 2
s_lshl_b32 s42, s42, 2
s_add_u32 s42, s42, s28
s_lshl_b32 s42, s42, 12
s_add_u32 s42, s42, 84934656
buffer_store_dwordx4 v[28:31], v154, s[12:15], s42 offen sc1
buffer_store_dwordx4 v[56:59], v154, s[12:15], s42 offen offset:1024 sc1
s_waitcnt vmcnt(0)
v_mov_b32 v159, 1
v_lshrrev_b32 v158, 4, v154
v_lshlrev_b32 v158, 8, v158
s_lshl_b32 s42, s44, 8
s_add_u32 s42, s42, 125829184
s_mov_b64 exec, 11
buffer_atomic_add v159, v158, s[12:15], s42 offen
s_mov_b64 exec, -1
s_add_u32 s42, s42, 512
v_mov_b32 v158, 0
s_mov_b32 s36, 0
Lk3_poll2:
buffer_load_dword v159, v158, s[12:15], s42 offen sc1
s_waitcnt vmcnt(0)
v_readfirstlane_b32 s43, v159
s_cmp_ge_u32 s43, 12
s_cbranch_scc1 Lk3_got2
s_add_u32 s36, s36, 1
s_cmp_lt_u32 s36, 1048576
s_sleep 1
s_cbranch_scc1 Lk3_poll2
Lk3_got2:
s_barrier
s_add_u32 s42, s44, 2
s_lshl_b32 s42, s42, 2
s_add_u32 s42, s42, 0
s_lshl_b32 s42, s42, 2
s_add_u32 s42, s42, s28
s_lshl_b32 s42, s42, 12
s_add_u32 s42, s42, 84934656
buffer_load_dwordx4 v[60:63], v154, s[12:15], s42 offen sc1
buffer_load_dwordx4 v[64:67], v154, s[12:15], s42 offen offset:1024 sc1
buffer_load_dwordx4 v[68:71], v154, s[12:15], s42 offen offset:2048 sc1
buffer_load_dwordx4 v[72:75], v154, s[12:15], s42 offen offset:3072 sc1
s_add_u32 s42, s44, 2
s_lshl_b32 s42, s42, 2
s_add_u32 s42, s42, 1
s_lshl_b32 s42, s42, 2
s_add_u32 s42, s42, s28
s_lshl_b32 s42, s42, 12
s_add_u32 s42, s42, 84934656
buffer_load_dwordx4 v[76:79], v154, s[12:15], s42 offen sc1
buffer_load_dwordx4 v[80:83], v154, s[12:15], s42 offen offset:1024 sc1
buffer_load_dwordx4 v[84:87], v154, s[12:15], s42 offen offset:2048 sc1
buffer_load_dwordx4 v[88:91], v154, s[12:15], s42 offen offset:3072 sc1
s_add_u32 s42, s44, 2
s_lshl_b32 s42, s42, 2
s_add_u32 s42, s42, 3
s_lshl_b32 s42, s42, 2
s_add_u32 s42, s42, s28
s_lshl_b32 s42, s42, 12
s_add_u32 s42, s42, 84934656
buffer_load_dwordx4 v[92:95], v154, s[12:15], s42 offen sc1
buffer_load_dwordx4 v[96:99], v154, s[12:15], s42 offen offset:1024 sc1
buffer_load_dwordx4 v[100:103], v154, s[12:15], s42 offen offset:2048 sc1
buffer_load_dwordx4 v[104:107], v154, s[12:15], s42 offen offset:3072 sc1
v_lshrrev_b32 v158, 4, v154
v_and_b32 v159, 15, v158
v_lshrrev_b32 v158, 4, v158
v_mul_u32_u24 v159, 896, v159
v_lshlrev_b32 v160, 4, v158
v_add_u32 v160, v159, v160
s_lshl_b32 s42, s30, 2
s_add_u32 s42, s42, s28
s_mul_i32 s42, s42, 28672
s_mul_i32 s43, s31, 224
s_add_u32 s42, s42, s43
s_add_u32 s42, s42, 128
s_waitcnt vmcnt(0)
v_add_f32 v20, v20, v60
v_add_f32 v21, v21, v61
v_add_f32 v22, v22, v62
v_add_f32 v23, v23, v63
v_add_f32 v20, v20, v76
v_add_f32 v21, v21, v77
v_add_f32 v22, v22, v78
v_add_f32 v23, v23, v79
v_add_f32 v20, v20, v92
v_add_f32 v21, v21, v93
v_add_f32 v22, v22, v94
v_add_f32 v23, v23, v95
v_add_f32 v24, v24, v64
v_add_f32 v25, v25, v65
v_add_f32 v26, v26, v66
v_add_f32 v27, v27, v67
v_add_f32 v24, v24, v80
v_add_f32 v25, v25, v81
v_add_f32 v26, v26, v82
v_add_f32 v27, v27, v83
v_add_f32 v24, v24, v96
v_add_f32 v25, v25, v97
v_add_f32 v26, v26, v98
v_add_f32 v27, v27, v99
v_add_f32 v48, v48, v68
v_add_f32 v49, v49, v69
v_add_f32 v50, v50, v70
v_add_f32 v51, v51, v71
v_add_f32 v48, v48, v84
v_add_f32 v49, v49, v85
v_add_f32 v50, v50, v86
v_add_f32 v51, v51, v87
v_add_f32 v48, v48, v100
v_add_f32 v49, v49, v101
v_add_f32 v50, v50, v102
v_add_f32 v51, v51, v103
v_add_f32 v52, v52, v72
v_add_f32 v53, v53, v73
v_add_f32 v54, v54, v74
v_add_f32 v55, v55, v75
v_add_f32 v52, v52, v88
v_add_f32 v53, v53, v89
v_add_f32 v54, v54, v90
v_add_f32 v55, v55, v91
v_add_f32 v52, v52, v104
v_add_f32 v53, v53, v105
v_add_f32 v54, v54, v106
v_add_f32 v55, v55, v107
CV 116,20,21
CV 117,22,23
CV 118,24,25
CV 119,26,27
buffer_store_dwordx4 v[116:119], v160, s[16:19], s42 offen
CV 120,48,49
CV 121,50,51
CV 122,52,53
CV 123,54,55
s_add_u32 s42, s42, 14336
buffer_store_dwordx4 v[120:123], v160, s[16:19], s42 offen
s_branch Lk3_end
Lk3_ks3:
s_add_u32 s42, s44, 0
s_lshl_b32 s42, s42, 2
s_add_u32 s42, s42, 3
s_lshl_b32 s42, s42, 2
s_add_u32 s42, s42, s28
s_lshl_b32 s42, s42, 12
s_add_u32 s42, s42, 84934656
buffer_store_dwordx4 v[4:7], v154, s[12:15], s42 offen sc1
buffer_store_dwordx4 v[8:11], v154, s[12:15], s42 offen offset:1024 sc1
buffer_store_dwordx4 v[32:35], v154, s[12:15], s42 offen offset:2048 sc1
buffer_store_dwordx4 v[36:39], v154, s[12:15], s42 offen offset:3072 sc1
s_add_u32 s42, s44, 1
s_lshl_b32 s42, s42, 2
s_add_u32 s42, s42, 3
s_lshl_b32 s42, s42, 2
s_add_u32 s42, s42, s28
s_lshl_b32 s42, s42, 12
s_add_u32 s42, s42, 84934656
buffer_store_dwordx4 v[12:15], v154, s[12:15], s42 offen sc1
buffer_store_dwordx4 v[16:19], v154, s[12:15], s42 offen offset:1024 sc1
buffer_store_dwordx4 v[40:43], v154, s[12:15], s42 offen offset:2048 sc1
buffer_store_dwordx4 v[44:47], v154, s[12:15], s42 offen offset:3072 sc1
s_add_u32 s42, s44, 2
s_lshl_b32 s42, s42, 2
s_add_u32 s42, s42, 3
s_lshl_b32 s42, s42, 2
s_add_u32 s42, s42, s28
s_lshl_b32 s42, s42, 12
s_add_u32 s42, s42, 84934656
buffer_store_dwordx4 v[20:23], v154, s[12:15], s42 offen sc1
buffer_store_dwordx4 v[24:27], v154, s[12:15], s42 offen offset:1024 sc1
buffer_store_dwordx4 v[48:51], v154, s[12:15], s42 offen offset:2048 sc1
buffer_store_dwordx4 v[52:55], v154, s[12:15], s42 offen offset:3072 sc1
s_waitcnt vmcnt(0)
v_mov_b32 v159, 1
v_lshrrev_b32 v158, 4, v154
v_lshlrev_b32 v158, 8, v158
s_lshl_b32 s42, s44, 8
s_add_u32 s42, s42, 125829184
s_mov_b64 exec, 7
buffer_atomic_add v159, v158, s[12:15], s42 offen
s_mov_b64 exec, -1
s_add_u32 s42, s42, 768
v_mov_b32 v158, 0
s_mov_b32 s36, 0
Lk3_poll3:
buffer_load_dword v159, v158, s[12:15], s42 offen sc1
s_waitcnt vmcnt(0)
v_readfirstlane_b32 s43, v159
s_cmp_ge_u32 s43, 12
s_cbranch_scc1 Lk3_got3
s_add_u32 s36, s36, 1
s_cmp_lt_u32 s36, 1048576
s_sleep 1
s_cbranch_scc1 Lk3_poll3
Lk3_got3:
s_barrier
s_add_u32 s42, s44, 3
s_lshl_b32 s42, s42, 2
s_add_u32 s42, s42, 0
s_lshl_b32 s42, s42, 2
s_add_u32 s42, s42, s28
s_lshl_b32 s42, s42, 12
s_add_u32 s42, s42, 84934656
buffer_load_dwordx4 v[60:63], v154, s[12:15], s42 offen sc1
buffer_load_dwordx4 v[64:67], v154, s[12:15], s42 offen offset:1024 sc1
s_add_u32 s42, s44, 3
s_lshl_b32 s42, s42, 2
s_add_u32 s42, s42, 1
s_lshl_b32 s42, s42, 2
s_add_u32 s42, s42, s28
s_lshl_b32 s42, s42, 12
s_add_u32 s42, s42, 84934656
buffer_load_dwordx4 v[76:79], v154, s[12:15], s42 offen sc1
buffer_load_dwordx4 v[80:83], v154, s[12:15], s42 offen offset:1024 sc1
s_add_u32 s42, s44, 3
s_lshl_b32 s42, s42, 2
s_add_u32 s42, s42, 2
s_lshl_b32 s42, s42, 2
s_add_u32 s42, s42, s28
s_lshl_b32 s42, s42, 12
s_add_u32 s42, s42, 84934656
buffer_load_dwordx4 v[92:95], v154, s[12:15], s42 offen sc1
buffer_load_dwordx4 v[96:99], v154, s[12:15], s42 offen offset:1024 sc1
v_lshrrev_b32 v158, 4, v154
v_and_b32 v159, 15, v158
v_lshrrev_b32 v158, 4, v158
v_mul_u32_u24 v159, 896, v159
v_lshlrev_b32 v160, 3, v158
v_add_u32 v160, v159, v160
s_lshl_b32 s42, s30, 2
s_add_u32 s42, s42, s28
s_mul_i32 s42, s42, 28672
s_mul_i32 s43, s31, 224
s_add_u32 s42, s42, s43
s_add_u32 s42, s42, 192
s_waitcnt vmcnt(0)
v_add_f32 v28, v28, v60
v_add_f32 v29, v29, v61
v_add_f32 v30, v30, v62
v_add_f32 v31, v31, v63
v_add_f32 v28, v28, v76
v_add_f32 v29, v29, v77
v_add_f32 v30, v30, v78
v_add_f32 v31, v31, v79
v_add_f32 v28, v28, v92
v_add_f32 v29, v29, v93
v_add_f32 v30, v30, v94
v_add_f32 v31, v31, v95
v_add_f32 v56, v56, v64
v_add_f32 v57, v57, v65
v_add_f32 v58, v58, v66
v_add_f32 v59, v59, v67
v_add_f32 v56, v56, v80
v_add_f32 v57, v57, v81
v_add_f32 v58, v58, v82
v_add_f32 v59, v59, v83
v_add_f32 v56, v56, v96
v_add_f32 v57, v57, v97
v_add_f32 v58, v58, v98
v_add_f32 v59, v59, v99
CV 116,28,29
CV 117,30,31
buffer_store_dwordx2 v[116:117], v160, s[16:19], s42 offen
CV 120,56,57
CV 121,58,59
s_add_u32 s42, s42, 14336
buffer_store_dwordx2 v[120:121], v160, s[16:19], s42 offen
s_branch Lk3_end
Lk3_end:
s_waitcnt vmcnt(0)
v_mov_b32 %0, v1
.purgem MZaaa
.purgem MAaaa
.purgem MZaav
.purgem MAaav
.purgem MZava
.purgem MAava
.purgem MZavv
.purgem MAavv
.purgem MZvaa
.purgem MAvaa
.purgem MZvav
.purgem MAvav
.purgem MZvva
.purgem MAvva
.purgem MZvvv
.purgem MAvvv
.purgem Ra
.purgem Ta
.purgem Rv
.purgem Tv
.purgem AR
.purgem CV
.purgem EX
)ASM"
  : "+v"(tid) : "s"(ka), "s"(bid), "s"((unsigned)(size_t)lds) :
"v1", "v2", "v3", "v4", "v5", "v6", "v7", "v8", "v9", "v10", "v11", "v12", "v13", "v14", "v15", "v16", "v17", "v18", "v19", "v20", "v21", "v22", "v23", "v24",
"v25", "v26", "v27", "v28", "v29", "v30", "v31", "v32", "v33", "v34", "v35", "v36", "v37", "v38", "v39", "v40", "v41", "v42", "v43", "v44", "v45", "v46", "v47", "v48",
"v49", "v50", "v51", "v52", "v53", "v54", "v55", "v56", "v57", "v58", "v59", "v60", "v61", "v62", "v63", "v64", "v65", "v66", "v67", "v68", "v69", "v70", "v71", "v72",
"v73", "v74", "v75", "v76", "v77", "v78", "v79", "v80", "v81", "v82", "v83", "v84", "v85", "v86", "v87", "v88", "v89", "v90", "v91", "v92", "v93", "v94", "v95", "v96",
"v97", "v98", "v99", "v100", "v101", "v102", "v103", "v104", "v105", "v106", "v107", "v108", "v109", "v110", "v111", "v112", "v113", "v114", "v115", "v116", "v117", "v118", "v119", "v120",
"v121", "v122", "v123", "v124", "v125", "v126", "v127", "v128", "v129", "v130", "v131", "v132", "v133", "v134", "v135", "v136", "v137", "v138", "v139", "v140", "v141", "v142", "v143", "v144",
"v145", "v146", "v147", "v148", "v149", "v150", "v151", "v152", "v153", "v154", "v155", "v156", "v157", "v158", "v159", "v160", "v161", "v162", "v163", "v164", "v165", "v166", "v167", "v168",
"v169", "v170", "v171", "v172", "v173", "v174", "v175", "v176", "v177", "v178", "v179", "v180", "v181", "v182", "v183", "v184", "v185", "v186", "v187", "v188", "v189", "v190", "v191", "v192",
"v193", "v194", "v195", "v196", "v197", "v198", "v199", "v200", "v201", "v202", "v203", "v204", "v205", "v206", "v207", "v208", "v209", "v210", "v211", "v212", "v213", "v214", "v215", "v216",
"v217", "v218", "v219", "v220", "v221", "v222", "v223", "v224", "v225", "v226", "v227", "v228", "v229", "v230", "v231", "v232", "v233", "v234", "v235", "v236", "v237", "v238", "v239", "v240",
"v241", "v242", "v243", "v244", "v245", "v246", "v247", "v248", "v249", "v250", "v251", "v252", "v253", "v254", "v255", "a0", "a1", "a2", "a3", "a4", "a5", "a6", "a7", "a8",
"a9", "a10", "a11", "a12", "a13", "a14", "a15", "a16", "a17", "a18", "a19", "a20", "a21", "a22", "a23", "a24", "a25", "a26", "a27", "a28", "a29", "a30", "a31", "a32",
"a33", "a34", "a35", "a36", "a37", "a38", "a39", "a40", "a41", "a42", "a43", "a44", "a45", "a46", "a47", "a48", "a49", "a50", "a51", "a52", "a53", "a54", "a55", "a56",
"a57", "a58", "a59", "a60", "a61", "a62", "a63", "a64", "a65", "a66", "a67", "a68", "a69", "a70", "a71", "a72", "a73", "a74", "a75", "a76", "a77", "a78", "a79", "a80",
"a81", "a82", "a83", "a84", "a85", "a86", "a87", "a88", "a89", "a90", "a91", "a92", "a93", "a94", "a95", "a96", "a97", "a98", "a99", "a100", "a101", "a102", "a103", "a104",
"a105", "a106", "a107", "a108", "a109", "a110", "a111", "a112", "a113", "a114", "a115", "a116", "a117", "a118", "a119", "a120", "a121", "a122", "a123", "a124", "a125", "a126", "a127", "a128",
"a129", "a130", "a131", "a132", "a133", "a134", "a135", "a136", "a137", "a138", "a139", "a140", "a141", "a142", "a143", "a144", "a145", "a146", "a147", "a148", "a149", "a150", "a151", "a152",
"a153", "a154", "a155", "a156", "a157", "a158", "a159", "a160", "a161", "a162", "a163", "a164", "a165", "a166", "a167", "a168", "a169", "a170", "a171", "a172", "a173", "a174", "a175", "a176",
"a177", "a178", "a179", "a180", "a181", "a182", "a183", "a184", "a185", "a186", "a187", "a188", "a189", "a190", "a191", "a192", "a193", "a194", "a195", "a196", "a197", "a198", "a199", "a200",
"a201", "a202", "a203", "a204", "a205", "a206", "a207", "a208", "a209", "a210", "a211", "a212", "a213", "a214", "a215", "a216", "a217", "a218", "a219", "a220", "a221", "a222", "a223", "a224",
"a225", "a226", "a227", "a228", "a229", "a230", "a231", "a232", "a233", "a234", "a235", "a236", "a237", "a238", "a239", "a240", "a241", "a242", "a243", "a244", "a245", "a246", "a247", "a248",
"a249", "a250", "a251", "a252", "a253", "a254", "a255", "s8", "s9", "s10", "s11", "s12", "s13", "s14", "s15", "s16", "s17", "s18", "s19", "s20", "s21", "s22", "s23", "s24",
"s25", "s26", "s27", "s28", "s29", "s30", "s31", "s34", "s35", "s36", "s37", "s38", "s39", "s40", "s41", "s42", "s43", "s44", "s45", "s46", "s47", "s48", "s49", "s50",
"s51", "s52", "s53", "s54", "s55", "s56", "s57", "s58", "s59", "s60", "s61", "s62", "s63", "s64", "s65", "s66", "s67", "s68", "s69", "s70", "s71", "s72", "s73", "s74",
"s75", "s76", "s77", "s78", "s79", "s80", "s81", "s82", "s83", "s84", "s85", "s86", "s87", "s88", "s89", "s90", "s91", "s92", "s93", "s94", "s95", "s96", "s97", "s98",
"s99", "vcc", "scc", "memory"
  );

}

extern "C" void kernel_launch(void* const* d_in, const int* in_sizes, int n_in, void* d_out, int out_size, void* d_ws, size_t ws_size, hipStream_t stream) {
  (void)in_sizes; (void)n_in; (void)out_size;
  const size_t WSOFF = 0 * ws_size;
  d_ws = (char*)d_ws + WSOFF;
  K1Args a;
  a.x = d_in[0]; a.ckv = d_in[1]; a.wq = d_in[2]; a.wuk = d_in[3]; a.wuv = d_in[4]; a.win = d_in[7]; a.gw = d_in[11]; a.ws = d_ws; a.wo = d_in[5]; a.wout = d_in[10]; a.wsp = d_in[8];
  K2Args b; b.ws = d_ws; b.wsp = d_in[8]; b.bsp = d_in[9];
  K3Args c; c.ws = d_ws; c.out = d_out;
  k1_proj<<<256, 256, 0, stream>>>(a);
  k2_mix<<<256, 256, 0, stream>>>(b);
  k3_out<<<256, 256, 0, stream>>>(c);
}
```
